```python
import torch

def _softmax(x, axis=-1):
    return torch.softmax(x, dim=axis)
def _gelu(x):
    return torch.nn.functional.gelu(x, approximate="tanh")
def _silu(x):
    return torch.nn.functional.silu(x)
def _sigmoid(x):
    return torch.sigmoid(x)
def _layernorm(x, axis=-1, eps=1e-5):
    m = x.mean(dim=axis, keepdim=True)
    v = x.var(dim=axis, keepdim=True, unbiased=False)
    return (x - m) / torch.sqrt(v + eps)
def _scatter_add(tgt, idx, src, axis=0):
    return tgt.clone().index_add_(axis, idx, src)
def _segment_sum(src, seg, n):
    out = torch.zeros((n,) + src.shape[1:], dtype=src.dtype)
    return out.index_add_(0, seg, src)
def _pad(x, pw):
    flat = tuple(v for pair in reversed(pw) for v in pair)
    return torch.nn.functional.pad(x, flat)

kernel_name = 'parallel_sum__ssm_conv_s5__glu_gated_glu__prompt'

def setup_inputs():

    _w = (lambda a: (lambda t: t.to(torch.bfloat16) if t.is_floating_point() else t)(torch.as_tensor(a)))
    def _A_setup():
        B, S, D = 1, 2048, 1024
        P = D
        Lambda_re = -0.5 * (lambda _x: torch.exp(torch.as_tensor(_x)))((lambda *s: torch.randn(*s, dtype=torch.bfloat16) * 0.8)(P))
        Lambda_im = (lambda *s: torch.randn(*s, dtype=torch.bfloat16) * 0.8)(P) * 1.0
        B_re = (lambda *s: torch.randn(*s, dtype=torch.bfloat16) * 0.8)(P, D) * (D ** -0.5)
        B_im = (lambda *s: torch.randn(*s, dtype=torch.bfloat16) * 0.8)(P, D) * (D ** -0.5)
        C_re = (lambda *s: torch.randn(*s, dtype=torch.bfloat16) * 0.8)(D, P) * (P ** -0.5)
        C_im = (lambda *s: torch.randn(*s, dtype=torch.bfloat16) * 0.8)(D, P) * (P ** -0.5)
        log_dt = (lambda *s: torch.randn(*s, dtype=torch.bfloat16) * 0.8)(P) * 0.3 - 2.0
        Dskip = (lambda *s: torch.randn(*s, dtype=torch.bfloat16) * 0.8)(D)
        return {
            "x": _w((lambda *s: torch.randn(*s, dtype=torch.bfloat16) * 0.05)(B, S, D)),
            "Lambda_re": _w(Lambda_re),
            "Lambda_im": _w(Lambda_im),
            "B_re": _w(B_re),
            "B_im": _w(B_im),
            "C_re": _w(C_re),
            "C_im": _w(C_im),
            "log_dt": _w(log_dt),
            "Dskip": _w(Dskip),
        }
    _A_in = _A_setup()

    def _B_setup():
        B, S, D, F = 1, 2048, 1024, 3584
        return {
            "x": _w((lambda *s: torch.randn(*s, dtype=torch.bfloat16) * 0.05)(B, S, D)),
            "w_gate": _w((lambda *s: torch.randn(*s, dtype=torch.bfloat16) * 0.05)(D, F)),
            "w_up": _w((lambda *s: torch.randn(*s, dtype=torch.bfloat16) * 0.05)(D, F)),
            "w_down": _w((lambda *s: torch.randn(*s, dtype=torch.bfloat16) * 0.05)(F, D)),
        }
    _B_in = _B_setup()

    return {
        "A_x": _A_in["x"],
        "A_Lambda_re": _A_in["Lambda_re"],
        "A_Lambda_im": _A_in["Lambda_im"],
        "A_B_re": _A_in["B_re"],
        "A_B_im": _A_in["B_im"],
        "A_C_re": _A_in["C_re"],
        "A_C_im": _A_in["C_im"],
        "A_log_dt": _A_in["log_dt"],
        "A_Dskip": _A_in["Dskip"],
        "B_x": _B_in["x"],
        "B_w_gate": _B_in["w_gate"],
        "B_w_up": _B_in["w_up"],
        "B_w_down": _B_in["w_down"],
    }

def reference(A_x, A_Lambda_re, A_Lambda_im, A_B_re, A_B_im, A_C_re, A_C_im, A_log_dt, A_Dskip, B_x, B_w_gate, B_w_up, B_w_down):
    def _A_ref(x, Lambda_re, Lambda_im, B_re, B_im, C_re, C_im, log_dt, Dskip):
        Bsz, L, H = x.shape
        Lam = Lambda_re + 1j * Lambda_im
        Bmat = B_re + 1j * B_im
        Cmat = C_re + 1j * C_im
        dt = torch.exp(log_dt)
        Lam_bar = torch.exp(dt * Lam)
        B_bar = ((Lam_bar - 1.0) / Lam)[:, None] * Bmat
        Bu = torch.einsum("bld,pd->blp", x + 0j, B_bar)
        h = torch.zeros_like(Bu[:, 0, :])
        hs = []
        for t in range(L):
            h = Lam_bar[None, :] * h + Bu[:, t, :]
            hs.append(h)
        hseq = torch.stack(hs, 1)
        y = 2.0 * torch.einsum("blp,dp->bld", hseq, Cmat).real + x * Dskip[None, None, :]
        return y

    def _B_ref(x, w_gate, w_up, w_down):
        g = torch.einsum("bsd,df->bsf", x, w_gate)
        u = torch.einsum("bsd,df->bsf", x, w_up)
        h = _sigmoid(g) * u
        return torch.einsum("bsf,fd->bsd", h, w_down)

    x = A_x
    xn = _layernorm(x, axis=-1)
    return x + _A_ref(xn, A_Lambda_re, A_Lambda_im, A_B_re, A_B_im, A_C_re, A_C_im, A_log_dt, A_Dskip) + _B_ref(xn, B_w_gate, B_w_up, B_w_down)
```

```cpp
#include <hip/hip_runtime.h>
#include <hip/hip_bf16.h>

typedef unsigned short u16;
typedef float  f32x2  __attribute__((ext_vector_type(2)));
typedef float  f32x16 __attribute__((ext_vector_type(16)));
typedef unsigned u32x4 __attribute__((ext_vector_type(4)));
typedef unsigned u32x16 __attribute__((ext_vector_type(16)));
typedef __bf16 bf16x2 __attribute__((ext_vector_type(2)));
#define DI __device__ __forceinline__
#define LAS __attribute__((address_space(3)))

struct Params {
  const u16* x; const u16* lre; const u16* lim; const u16* bre; const u16* bim; const u16* cre; const u16* cim;
  const u16* logdt; const u16* dskip; const u16* wg; const u16* wu; const u16* wd; float* out; char* ws;
};

DI float bf2f(unsigned h) { return __uint_as_float(h << 16); }
DI unsigned pk2(float lo, float hi) { f32x2 v = {lo, hi}; bf16x2 b = __builtin_convertvector(v, bf16x2); return __builtin_bit_cast(unsigned, b); }
DI float rbf(float v) { return bf2f(pk2(v, 0.f) & 0xffffu); }
DI unsigned rfl(unsigned v) { return __builtin_amdgcn_readfirstlane(v); }
DI u32x4 mksrd(const void* p, unsigned n) { const unsigned long long a = (unsigned long long)p; return (u32x4){(unsigned)a, (unsigned)(a >> 32), n, 0x20000u}; }
struct cpx { float r, i; };
DI cpx cmul(cpx a, cpx b) { return {fmaf(a.r, b.r, -a.i * b.i), fmaf(a.r, b.i, a.i * b.r)}; }
DI unsigned tokmap(unsigned mblk, unsigned c) { return 64 * (mblk >> 2) + 16 * (c >> 2) + 8 * (c & 1) + 2 * (mblk & 3) + ((c >> 1) & 1); }
DI f32x2 sw16(float v) { auto r = __builtin_amdgcn_permlane16_swap(__float_as_uint(v), __float_as_uint(v), false, false); return (f32x2){__uint_as_float(r[0]), __uint_as_float(r[1])}; }
DI float red8(float s) {
  s += __uint_as_float(__builtin_amdgcn_update_dpp(0u, __float_as_uint(s), 0x128, 0xf, 0xf, false));
  const f32x2 a = sw16(s); s = a[0] + a[1];
  auto r = __builtin_amdgcn_permlane32_swap(__float_as_uint(s), __float_as_uint(s), false, false);
  return __uint_as_float(r[0]) + __uint_as_float(r[1]);
}
#define ST(i)
#define NT0 4

__global__ __launch_bounds__(256, 1) void k_s5_glu(Params p) {
  extern __shared__ __attribute__((aligned(16))) char smem[];
  const unsigned tid = threadIdx.x, lane = tid & 63, w = rfl(tid >> 6), bid = blockIdx.x;
  unsigned stv = 0;
  ST(0);
  const unsigned xcd = bid & 7, r = bid >> 3, mt = r & 7, nt = 4 * xcd + (r >> 3);
  const unsigned g = lane >> 4, c = lane & 15;
  unsigned sink0, sink1;
  const unsigned rho = r * 4 + w;
  { unsigned long long pc; unsigned csz;
    asm volatile("s_getpc_b64 %0\n.Lk_code_here:\n\ts_mov_b32 %1, .Lk_code_end-.Lk_code_here" : "=s"(pc), "=s"(csz));
    asm volatile("buffer_load_dword %0, %1, %2, 0 offen" : "=v"(sink1) : "v"(lane < 4 ? (rho * 4 + lane) * 128 : 0x80000000u), "s"(mksrd((const void*)pc, csz)) : "memory"); }
  const unsigned lmb = 16 * mt + (nt >> 1), lc = 8 * (nt & 1) + (lane & 7), qq = lane >> 3;
  unsigned ep; u32x4 raw[4]; unsigned pl[3];
  asm volatile("global_load_dword %0, %1, %2 sc1" : "=v"(ep) : "v"(0u), "s"(p.ws + 35651584) : "memory");
  { const unsigned xo = tokmap(lmb, lc) * 2048 + (32 * w + qq) * 16;
#pragma unroll
    for (int i = 0; i < 4; ++i) asm volatile("global_load_dwordx4 %0, %1, %2 offset:%3 nt" : "=v"(raw[i]) : "v"(xo), "s"(p.x), "i"(128 * i) : "memory"); }
  asm volatile("global_load_ushort %0, %3, %4\n\tglobal_load_ushort %1, %3, %5\n\tglobal_load_ushort %2, %3, %6"
               : "=&v"(pl[0]), "=&v"(pl[1]), "=&v"(pl[2]) : "v"((32 * nt + (lane & 31)) * 2), "s"(p.lre), "s"(p.lim), "s"(p.logdt) : "memory");
  ST(23);
  const bool t1w = rho < 112;
  const u16* t1p = rho < 56 ? p.wg : rho < 112 ? p.wu : rho < 120 ? p.bre : p.bim;
  const unsigned t1n = t1w ? 7340032u : 2097152u, t1i = t1w ? 458752u : 128u;
  unsigned t1o, t1t;
  if (t1w) { const unsigned L = 8 * (rho < 56 ? rho : rho - 56) + (lane & 7), row = L / 7; t1o = row * 7168 + xcd * 896 + (L - 7 * row) * 128; t1t = lane >> 3; }
  else { t1o = (128 * xcd + 16 * (rho & 7) + (lane >> 2)) * 2048; t1t = lane & 3; }
  const u32x4 t1srd = mksrd(t1p, t1n);
  u32x16 va, vb;
#pragma unroll
  for (int i = 0; i < 4; ++i) {
    const unsigned ins = i < 3 ? 4 * i + w : 12 + (w & 1), ch = 64 * ins + lane, rw = ch / 14, pos = ch - 14 * rw;
    va[2 + i] = (32 * (rw >> 5) + 8 * ((rw >> 2) & 3) + 4 * ((rw >> 4) & 1) + (rw & 3)) * 7168 + nt * 224 + pos * 16;
  }
  const unsigned mblk0 = 16 * mt + 4 * w, swz = ((lane & 7) ^ (lane >> 3)) * 16;
  va[6] = (32 * nt + 8 * w + (lane >> 3)) * 2048 + swz;
  va[7] = lane * 16 + mblk0 * 1024; va[8] = va[7] + 131072;
  va[9] = 224 * (lane >> 2) + 8 * (lane & 3); va[10] = va[9] + 73728;
  const unsigned sw0 = 128 * c + 16 * (g ^ (c & 7)), sw1 = 128 * c + 16 * ((4 + g) ^ (c & 7));
  va[11] = 28672 + sw0; va[12] = 28672 + sw1; va[13] = va[11] + 73728; va[14] = va[12] + 73728;
  va[15] = t1w ? (t1t == 0 ? t1o : 0x80000000u) : t1o + t1t * 128;
  const unsigned gw = bid * 4 + w;
  const u16* t2p = gw < 601 ? p.wd : gw < 779 ? p.cre : p.cim;
  const unsigned t2n = gw < 601 ? 7340032u : gw < 957 ? 2097152u : 0u, t2i = gw < 601 ? 4776u * 128u : 1360u * 128u;
  va[0] = lane < 8 ? (8 * (gw < 601 ? gw : gw < 779 ? gw - 601 : gw - 779) + lane) * 128 : 0x80000000u;
  const unsigned role = xcd >> 2, mt2 = 4 * (xcd & 3) + (r & 3), nt2 = r >> 2;
  const unsigned mh = w & 1, kp = w >> 1, mb2 = 8 * mt2 + 4 * mh, tok2 = tokmap(mb2, c);
  const unsigned L0 = 2 * (nt2 ^ (((3u - ((unsigned)(size_t)p.wd >> 7)) >> 1) & 1u)), L1 = L0 + 1;
  const unsigned nk = 64 * (kp ? L1 : L0);
  vb[0] = (32 * (g >> 1) + 8 * w + 4 * (g & 1)) * 2048 + ((c & 8) ? L1 : L0) * 128 + (c & 7) * 16;
  vb[1] = (nk + 32 * mh + (lane >> 3)) * 2048 + swz;
  vb[2] = lane * 16 + mb2 * 1024;
  vb[3] = kp * 16896 + 1056 * (lane >> 2) + 8 * (lane & 3);
  vb[4] = kp * 16896 + sw0; vb[5] = kp * 16896 + sw1;
  vb[6] = (mblk0 + (g & 1)) * 1024 + (g >> 1) * 256 + c * 16;
  vb[7] = 159744 + ((4 * w + g) * 16 + c) * 16;
  vb[8] = 135168 + w * 4608 + c * 144 + g * 32;
  vb[9] = 135168 + w * 4608 + (8 * g + ((lane >> 2) & 3)) * 144 + (lane & 3) * 32;
  vb[10] = (nk + 32 * role + 4 * g) * 2;
  vb[11] = 0 + ((nk >> 5) + role) * 131072 + mb2 * 1024 + ((g >> 1) * 16 + c) * 16 + 8 * (g & 1);
  vb[12] = 0;
  vb[13] = tok2 * 4096 + (nk + 32 * role + 4 * g) * 4;
  vb[14] = t1w ? va[15] : 0x80000000u; vb[15] = lane * 16;
  unsigned vpl = 35655680 + (mt * 32 + 8 * w + (lane & 7)) * 64;
  *(LAS u32x4*)(smem + 147456 + tid * 16) = (u32x4){vb[8], vb[9], vb[10], vb[11]};
  *(LAS u32x4*)(smem + 155648 + tid * 16) = (u32x4){0u, 0u, 0u, 0u};
  unsigned so = (8 * w + (qq >> 2)) * 131072 + lmb * 1024 + (qq & 3) * 256 + lc * 16;
  asm volatile("" : "+v"(va), "+v"(vb), "+v"(vpl), "+v"(so));
  asm volatile("buffer_load_dword %0, %1, %2, 0 offen" : "=v"(sink0) : "v"(t1t < NT0 ? t1o + t1t * t1i : 0x80000000u), "s"(t1srd) : "memory");
  asm volatile("s_waitcnt vmcnt(4)" : "+v"(sink1), "+v"(ep), "+v"(raw[0]), "+v"(raw[1]), "+v"(raw[2]), "+v"(raw[3]) :: "memory");
  ST(17);
  const unsigned tag = rfl(ep) + 1u;
  LAS f32x2* red = (LAS f32x2*)(smem + 159744);
  f32x2 v[16];
#pragma unroll
  for (int i = 0; i < 16; ++i) v[i] = (f32x2){__uint_as_float(raw[i >> 2][i & 3] << 16), __uint_as_float(raw[i >> 2][i & 3] & 0xffff0000u)};
  f32x2 s2 = v[0];
#pragma unroll
  for (int i = 1; i < 16; ++i) s2 += v[i];
  const float mw = red8(s2[0] + s2[1]) * (1.f / 256.f);
  f32x2 q2 = {0.f, 0.f};
#pragma unroll
  for (int i = 0; i < 16; ++i) { const f32x2 d = v[i] - mw; q2 = __builtin_elementwise_fma(d, d, q2); }
  float q = q2[0] + q2[1];
  q = red8(q);
  if (lane < 8) red[w * 8 + lane] = (f32x2){mw, q};
  __syncthreads();
  const f32x2 r0 = red[lane & 7], r1 = red[8 + (lane & 7)], r2 = red[16 + (lane & 7)], r3 = red[24 + (lane & 7)];
  const float mean = ((r0[0] + r1[0]) + (r2[0] + r3[0])) * 0.25f;
  const float d0 = r0[0] - mean, d1 = r1[0] - mean, d2 = r2[0] - mean, d3 = r3[0] - mean;
  const float var = (((r0[1] + r1[1]) + (r2[1] + r3[1])) + 256.f * ((d0 * d0 + d1 * d1) + (d2 * d2 + d3 * d3))) * (1.f / 1024.f);
  const float mb = rbf(mean), rinv = __builtin_amdgcn_rcpf(rbf(__builtin_amdgcn_sqrtf(rbf(rbf(var) + 1e-5f))));
  {
#pragma unroll
    for (int i = 0; i < 4; ++i) {
      u32x4 o;
#pragma unroll
      for (int j = 0; j < 4; ++j) { const f32x2 d = (v[4 * i + j] - mb) * rinv; o[j] = pk2(d[0], d[1]); }
      asm volatile("global_store_dwordx4 %0, %1, %2 sc1\n\ts_nop 1" :: "v"(so + i * 262144), "v"(o), "s"(p.ws + 0) : "memory");
    } }
  ST(18);
#pragma unroll
  for (int i = 0; i < 4; ++i) asm volatile("global_store_dwordx4 %0, %1, %2 sc1\n\ts_nop 1" :: "v"(so + i * 262144), "v"(raw[i]), "s"(p.ws + 37748736) : "memory");
  asm volatile("s_waitcnt vmcnt(9)" : "+v"(pl[0]), "+v"(pl[1]), "+v"(pl[2]) :: "memory");
  const float lr = bf2f(pl[0]), li = bf2f(pl[1]);
  const float dt = rbf(expf(bf2f(pl[2]))), ez = expf(dt * lr);
  float sn, cs; sincosf(dt * li, &sn, &cs);
  cpx lam = {ez * cs, ez * sn};
  const float nr = lam.r - 1.f, inv = 2.f / (lr * lr + li * li);
  cpx c2 = {(nr * lr + lam.i * li) * inv, (lam.i * lr - nr * li) * inv};
  asm volatile("s_waitcnt vmcnt(4)" : "+v"(sink0), "+v"(lam.r), "+v"(lam.i), "+v"(c2.r), "+v"(c2.i) :: "memory");
  ST(19);
  __builtin_amdgcn_s_barrier();
  if (tid == 0) asm volatile("global_store_dwordx2 %0, %1, %2 sc1" :: "v"((mt * 32 + nt) * 64), "v"(((unsigned long long)(tag ^ 0x5a5a5a5au) << 32) | tag), "s"(p.ws + 35655680) : "memory");
  asm volatile(
    "s_mov_b64 s[44:45], %[wg]\n\t"
    "s_mov_b32 s46, 7340032\n\t"
    "s_mov_b32 s47, 0x20000\n\t"
    "s_mov_b64 s[48:49], %[wu]\n\t"
    "s_mov_b32 s50, 7340032\n\t"
    "s_mov_b32 s51, 0x20000\n\t"
    "s_mov_b64 s[52:53], %[wm]\n\t"
    "s_mov_b32 s54, 7340032\n\t"
    "s_mov_b32 s55, 0x20000\n\t"
    "s_mov_b64 s[56:57], %[bre]\n\t"
    "s_mov_b32 s58, 2097152\n\t"
    "s_mov_b32 s59, 0x20000\n\t"
    "s_mov_b64 s[60:61], %[bim]\n\t"
    "s_mov_b32 s62, 2097152\n\t"
    "s_mov_b32 s63, 0x20000\n\t"
    "s_mov_b32 s68, %[sW]\n\t"
    "s_mov_b32 s69, %[sE]\n\t"
    "s_mov_b32 s64, 0\n\t"
    "s_mov_b32 s65, 0\n\t"
    "s_add_u32 m0, s68, 0\n\t"
    "s_nop 0\n\t"
    "buffer_load_dwordx4 v2, s[44:47], s64 offen lds\n\t"
    "s_add_u32 m0, s68, 14336\n\t"
    "s_nop 0\n\t"
    "buffer_load_dwordx4 v2, s[48:51], s64 offen lds\n\t"
    "s_add_u32 m0, s68, 4096\n\t"
    "s_nop 0\n\t"
    "buffer_load_dwordx4 v3, s[44:47], s64 offen lds\n\t"
    "s_add_u32 m0, s68, 18432\n\t"
    "s_nop 0\n\t"
    "buffer_load_dwordx4 v3, s[48:51], s64 offen lds\n\t"
    "s_add_u32 m0, s68, 8192\n\t"
    "s_nop 0\n\t"
    "buffer_load_dwordx4 v4, s[44:47], s64 offen lds\n\t"
    "s_add_u32 m0, s68, 22528\n\t"
    "s_nop 0\n\t"
    "buffer_load_dwordx4 v4, s[48:51], s64 offen lds\n\t"
    "s_add_u32 m0, s69, 0\n\t"
    "s_nop 0\n\t"
    "buffer_load_dwordx4 v5, s[52:55], s64 offen lds\n\t"
    "s_add_u32 m0, s68, 28672\n\t"
    "s_nop 0\n\t"
    "buffer_load_dwordx4 v6, s[56:59], s65 offen lds\n\t"
    "s_add_u32 m0, s68, 32768\n\t"
    "s_nop 0\n\t"
    "buffer_load_dwordx4 v6, s[60:63], s65 offen lds\n\t"
    "s_add_u32 s64, s64, 458752\n\t"
    "s_add_u32 s65, s65, 128\n\t"
    "s_add_u32 m0, s68, 36864\n\t"
    "s_nop 0\n\t"
    "buffer_load_dwordx4 v2, s[44:47], s64 offen lds\n\t"
    "s_add_u32 m0, s68, 51200\n\t"
    "s_nop 0\n\t"
    "buffer_load_dwordx4 v2, s[48:51], s64 offen lds\n\t"
    "s_add_u32 m0, s68, 40960\n\t"
    "s_nop 0\n\t"
    "buffer_load_dwordx4 v3, s[44:47], s64 offen lds\n\t"
    "s_add_u32 m0, s68, 55296\n\t"
    "s_nop 0\n\t"
    "buffer_load_dwordx4 v3, s[48:51], s64 offen lds\n\t"
    "s_add_u32 m0, s68, 45056\n\t"
    "s_nop 0\n\t"
    "buffer_load_dwordx4 v4, s[44:47], s64 offen lds\n\t"
    "s_add_u32 m0, s68, 59392\n\t"
    "s_nop 0\n\t"
    "buffer_load_dwordx4 v4, s[48:51], s64 offen lds\n\t"
    "s_add_u32 m0, s69, 36864\n\t"
    "s_nop 0\n\t"
    "buffer_load_dwordx4 v5, s[52:55], s64 offen lds\n\t"
    "s_add_u32 m0, s68, 65536\n\t"
    "s_nop 0\n\t"
    "buffer_load_dwordx4 v6, s[56:59], s65 offen lds\n\t"
    "s_add_u32 m0, s68, 69632\n\t"
    "s_nop 0\n\t"
    "buffer_load_dwordx4 v6, s[60:63], s65 offen lds\n\t"
    "s_add_u32 s64, s64, 458752\n\t"
    "s_add_u32 s65, s65, 128\n\t"
    "s_add_u32 m0, s68, 73728\n\t"
    "s_nop 0\n\t"
    "buffer_load_dwordx4 v2, s[44:47], s64 offen lds\n\t"
    "s_add_u32 m0, s68, 88064\n\t"
    "s_nop 0\n\t"
    "buffer_load_dwordx4 v2, s[48:51], s64 offen lds\n\t"
    "s_add_u32 m0, s68, 77824\n\t"
    "s_nop 0\n\t"
    "buffer_load_dwordx4 v3, s[44:47], s64 offen lds\n\t"
    "s_add_u32 m0, s68, 92160\n\t"
    "s_nop 0\n\t"
    "buffer_load_dwordx4 v3, s[48:51], s64 offen lds\n\t"
    "s_add_u32 m0, s68, 81920\n\t"
    "s_nop 0\n\t"
    "buffer_load_dwordx4 v4, s[44:47], s64 offen lds\n\t"
    "s_add_u32 m0, s68, 96256\n\t"
    "s_nop 0\n\t"
    "buffer_load_dwordx4 v4, s[48:51], s64 offen lds\n\t"
    "s_add_u32 m0, s69, 73728\n\t"
    "s_nop 0\n\t"
    "buffer_load_dwordx4 v5, s[52:55], s64 offen lds\n\t"
    "s_add_u32 m0, s68, 102400\n\t"
    "s_nop 0\n\t"
    "buffer_load_dwordx4 v6, s[56:59], s65 offen lds\n\t"
    "s_add_u32 m0, s68, 106496\n\t"
    "s_nop 0\n\t"
    "buffer_load_dwordx4 v6, s[60:63], s65 offen lds\n\t"
    "s_add_u32 s64, s64, 458752\n\t"
    "s_add_u32 s65, s65, 128\n\t"
    :: "{v[0:15]}"(va), [wg]"s"(p.wg), [wu]"s"(p.wu), [wm]"s"(w < 2 ? p.wg : p.wu), [bre]"s"(p.bre), [bim]"s"(p.bim),
       [sW]"s"(1024 * w), [sE]"s"((w < 2 ? 12288u : 26624u) + 1024 * (w & 1))
    : "s44","s45","s46","s47","s48","s49","s50","s51","s52","s53","s54","s55","s56","s57","s58","s59","s60","s61","s62","s63","s64","s65","s66","s67","s68","s69", "scc", "memory");
  asm volatile("" : "+v"(lam.r), "+v"(lam.i));
  f32x16 par;
  { cpx m8 = cmul(lam, lam); m8 = cmul(m8, m8); m8 = cmul(m8, m8);
    const f32x2 a = sw16(lam.r), b = sw16(lam.i), cr = sw16(c2.r), ci = sw16(c2.i), mr = sw16(m8.r), mi = sw16(m8.i);
#pragma unroll
    for (int h = 0; h < 2; ++h) {
      par[2 * h] = a[h]; par[2 * h + 1] = b[h]; par[4 + 2 * h] = cr[h]; par[5 + 2 * h] = ci[h]; par[8 + h] = mr[h]; par[10 + h] = mi[h];
      cpx m = {mr[h], mi[h]}, pw = {1.f, 0.f};
#pragma unroll
      for (int k = 0; k < 4; ++k) { m = cmul(m, m); const cpx t = cmul(pw, m); if (((4 * w + g) >> k) & 1) pw = t; }
      par[12 + h] = pw.r; par[14 + h] = pw.i;
    } }
  va[1] = stv;
  asm volatile(
    "s_load_dwordx2 s[56:57], %[ka], 0x18\n\t"
    "s_load_dwordx2 s[60:61], %[ka], 0x20\n\t"
    "s_load_dwordx2 s[78:79], %[ka], 0x28\n\t"
    "s_load_dwordx2 s[80:81], %[ka], 0x30\n\t"
    "s_load_dwordx2 s[30:31], %[ka], 0x40\n\t"
    "s_load_dwordx2 s[44:45], %[ka], 0x48\n\t"
    "s_load_dwordx2 s[48:49], %[ka], 0x50\n\t"
    "s_load_dwordx2 s[70:71], %[ka], 0x58\n\t"
    "s_load_dwordx2 s[34:35], %[ka], 0x60\n\t"
    "s_load_dwordx2 s[16:17], %[ka], 0x68\n\t"
    "s_mov_b64 s[20:21], %[t1p]\n\t"
    "s_mov_b64 s[24:25], %[t2p]\n\t"
    "s_mov_b32 s26, %[t2n]\n\t"
    "s_mov_b32 s23, 0x20000\n\t"
    "s_mov_b32 s27, 0x20000\n\t"
    "s_mov_b32 s85, %[t1i]\n\t"
    "s_mov_b32 s90, 2097152\n\t"
    "s_cmp_eq_u32 s85, 128\n\t"
    "s_cselect_b32 s22, s90, 7340032\n\t"
    "s_lshl_b32 s84, s85, 2\n\t"
    "s_mov_b32 s86, 0\n\t"
    "s_mov_b32 s39, %[t2i]\n\t"
    "s_mov_b32 s87, %[w]\n\t"
    "s_bfe_u32 s88, %[bid], 0x30003\n\t"
    "s_and_b32 s89, %[bid], 7\n\t"
    "s_lshr_b32 s73, s89, 2\n\t"
    "s_lshl_b32 s89, s89, 2\n\t"
    "s_lshr_b32 s90, %[bid], 6\n\t"
    "s_add_u32 s89, s89, s90\n\t"
    "s_and_b32 s38, %[bid], 3\n\t"
    "s_lshl_b32 s38, s38, 2\n\t"
    "s_bfe_u32 s90, %[bid], 0x20003\n\t"
    "s_add_u32 s38, s38, s90\n\t"
    "s_lshl_b32 s38, s38, 3\n\t"
    "s_lshr_b32 s90, %[bid], 5\n\t"
    "s_add_u32 s38, s38, s90\n\t"
    "s_mov_b32 s82, %[tag]\n\t"
    "s_xor_b32 s83, s82, 0x5a5a5a5a\n\t"
    "s_lshl_b32 s91, s88, 5\n\t"
    "s_add_u32 s91, s91, s89\n\t"
    "s_lshl_b32 s91, s91, 6\n\t"
    "s_lshr_b32 s99, s38, 4\n\t"
    "s_lshl_b32 s99, s99, 11\n\t"
    "s_mov_b32 s92, 0xbfb8aa3b\n\t"
    "s_mov_b32 s93, 0xbfb8aa3b\n\t"
    "s_mul_i32 s90, s89, 7\n\t"
    "s_lshr_b32 s96, s90, 1\n\t"
    "s_lshl_b32 s96, s96, 17\n\t"
    "s_and_b32 s90, s90, 1\n\t"
    "s_lshl_b32 s94, s90, 9\n\t"
    "s_add_u32 s96, s96, s94\n\t"
    "s_add_u32 s96, s96, 4194304\n\t"
    "s_movk_i32 s94, 512\n\t"
    "s_cmp_eq_u32 s90, 0\n\t"
    "s_cselect_b32 s97, s94, 130560\n\t"
    "s_cselect_b32 s98, 130560, s94\n\t"
    "s_lshl_b32 s68, s87, 10\n\t"
    "s_and_b32 s69, s87, 1\n\t"
    "s_lshl_b32 s69, s69, 10\n\t"
    "s_movk_i32 s94, 12288\n\t"
    "s_cmp_lt_u32 s87, 2\n\t"
    "s_cselect_b32 s90, s94, 26624\n\t"
    "s_add_u32 s69, s69, s90\n\t"
    "s_mov_b32 s64, 1376256\n\t"
    "s_mov_b32 s65, 384\n\t"
    "s_mov_b32 s66, 0\n\t"
    "s_waitcnt lgkmcnt(0)\n\t"
    "s_mov_b32 s58, 2097152\n\t"
    "s_mov_b32 s59, 0x20000\n\t"
    "s_mov_b32 s62, 2097152\n\t"
    "s_mov_b32 s63, 0x20000\n\t"
    "s_mov_b32 s46, 7340032\n\t"
    "s_mov_b32 s47, 0x20000\n\t"
    "s_mov_b32 s50, 7340032\n\t"
    "s_mov_b32 s51, 0x20000\n\t"
    "s_mov_b32 s18, 42991616\n\t"
    "s_mov_b32 s19, 0x20000\n\t"
    "s_mov_b32 s54, 7340032\n\t"
    "s_mov_b32 s55, 0x20000\n\t"
    "s_mov_b32 s42, 4194304\n\t"
    "s_mov_b32 s43, 0x20000\n\t"
    "s_cmp_lt_u32 s87, 2\n\t"
    "s_cselect_b64 s[52:53], s[44:45], s[48:49]\n\t"
    "s_mov_b64 s[40:41], s[16:17]\n\t"
    "s_mov_b32 s77, 0\n\t"
    "L_pln_%=:\n\t"
    "buffer_load_dwordx2 v[252:253], v255, s[16:19], 0 offen sc1\n\t"
    "s_add_u32 s77, s77, 1\n\t"
    "s_waitcnt vmcnt(0)\n\t"
    "v_cmp_eq_u64_e32 vcc, s[82:83], v[252:253]\n\t"
    "s_cmp_eq_u64 vcc, -1\n\t"
    "s_cbranch_scc1 L_plnok_%=\n\t"
    "s_cmp_lt_u32 s77, 0x40000\n\t"
    "s_cbranch_scc1 L_pln_%=\n\t"
    "L_plnok_%=:\n\t"
    "buffer_load_dwordx4 a[64:67], v7, s[40:43], s66 offen offset:0 sc1\n\t"
    "buffer_load_dwordx4 a[68:71], v8, s[40:43], s66 offen offset:0 sc1\n\t"
    "buffer_load_dwordx4 a[72:75], v7, s[40:43], s66 offen offset:1024 sc1\n\t"
    "buffer_load_dwordx4 a[76:79], v8, s[40:43], s66 offen offset:1024 sc1\n\t"
    "buffer_load_dwordx4 a[80:83], v7, s[40:43], s66 offen offset:2048 sc1\n\t"
    "buffer_load_dwordx4 a[84:87], v8, s[40:43], s66 offen offset:2048 sc1\n\t"
    "buffer_load_dwordx4 a[88:91], v7, s[40:43], s66 offen offset:3072 sc1\n\t"
    "buffer_load_dwordx4 a[92:95], v8, s[40:43], s66 offen offset:3072 sc1\n\t"
    "s_add_u32 s66, s66, 262144\n\t"
    "s_barrier\n\t"
    "ds_read_b64_tr_b16 a[192:193], v9 offset:0\n\t"
    "ds_read_b64_tr_b16 a[194:195], v9 offset:3584\n\t"
    "ds_read_b64_tr_b16 a[196:197], v9 offset:7168\n\t"
    "ds_read_b64_tr_b16 a[198:199], v9 offset:10752\n\t"
    "ds_read_b64_tr_b16 a[200:201], v9 offset:14336\n\t"
    "ds_read_b64_tr_b16 a[202:203], v9 offset:17920\n\t"
    "ds_read_b64_tr_b16 a[204:205], v9 offset:21504\n\t"
    "ds_read_b64_tr_b16 a[206:207], v9 offset:25088\n\t"
    "ds_read_b64_tr_b16 a[208:209], v9 offset:32\n\t"
    "ds_read_b64_tr_b16 a[210:211], v9 offset:3616\n\t"
    "ds_read_b64_tr_b16 a[212:213], v9 offset:7200\n\t"
    "ds_read_b64_tr_b16 a[214:215], v9 offset:10784\n\t"
    "s_mov_b32 s67, 3\n\t"
    "s_waitcnt vmcnt(0)\n\t"
    "s_waitcnt lgkmcnt(8)\n\t"
    "v_mfma_f32_16x16x32_bf16 v[192:195], a[192:195], a[64:67], 0\n\t"
    "ds_read_b64_tr_b16 a[216:217], v9 offset:14368\n\t"
    "v_mfma_f32_16x16x32_bf16 v[192:195], a[196:199], a[68:71], v[192:195]\n\t"
    "v_mfma_f32_16x16x32_bf16 v[196:199], a[192:195], a[72:75], 0\n\t"
    "ds_read_b64_tr_b16 a[218:219], v9 offset:17952\n\t"
    "buffer_load_dwordx4 a[96:99], v7, s[40:43], s66 offen offset:0 sc1\n\t"
    "v_mfma_f32_16x16x32_bf16 v[196:199], a[196:199], a[76:79], v[196:199]\n\t"
    "v_mfma_f32_16x16x32_bf16 v[200:203], a[192:195], a[80:83], 0\n\t"
    "ds_read_b64_tr_b16 a[220:221], v9 offset:21536\n\t"
    "buffer_load_dwordx4 a[100:103], v8, s[40:43], s66 offen offset:0 sc1\n\t"
    "v_mfma_f32_16x16x32_bf16 v[200:203], a[196:199], a[84:87], v[200:203]\n\t"
    "v_mfma_f32_16x16x32_bf16 v[204:207], a[192:195], a[88:91], 0\n\t"
    "ds_read_b64_tr_b16 a[222:223], v9 offset:25120\n\t"
    "v_mfma_f32_16x16x32_bf16 v[204:207], a[196:199], a[92:95], v[204:207]\n\t"
    "buffer_load_dwordx4 a[104:107], v7, s[40:43], s66 offen offset:1024 sc1\n\t"
    "s_waitcnt lgkmcnt(8)\n\t"
    "v_mfma_f32_16x16x32_bf16 v[220:223], a[200:203], a[88:91], 0\n\t"
    "ds_read_b64_tr_b16 a[224:225], v9 offset:64\n\t"
    "v_mfma_f32_16x16x32_bf16 v[220:223], a[204:207], a[92:95], v[220:223]\n\t"
    "v_mfma_f32_16x16x32_bf16 v[216:219], a[200:203], a[80:83], 0\n\t"
    "ds_read_b64_tr_b16 a[226:227], v9 offset:3648\n\t"
    "buffer_load_dwordx4 a[108:111], v8, s[40:43], s66 offen offset:1024 sc1\n\t"
    "v_mfma_f32_16x16x32_bf16 v[216:219], a[204:207], a[84:87], v[216:219]\n\t"
    "v_mfma_f32_16x16x32_bf16 v[212:215], a[200:203], a[72:75], 0\n\t"
    "ds_read_b64_tr_b16 a[228:229], v9 offset:7232\n\t"
    "v_mfma_f32_16x16x32_bf16 v[212:215], a[204:207], a[76:79], v[212:215]\n\t"
    "buffer_load_dwordx4 a[112:115], v7, s[40:43], s66 offen offset:2048 sc1\n\t"
    "v_mfma_f32_16x16x32_bf16 v[208:211], a[200:203], a[64:67], 0\n\t"
    "ds_read_b64_tr_b16 a[230:231], v9 offset:10816\n\t"
    "v_mfma_f32_16x16x32_bf16 v[208:211], a[204:207], a[68:71], v[208:211]\n\t"
    "s_waitcnt lgkmcnt(8)\n\t"
    "v_mfma_f32_16x16x32_bf16 v[176:179], a[208:211], a[64:67], 0\n\t"
    "ds_read_b64_tr_b16 a[232:233], v9 offset:14400\n\t"
    "buffer_load_dwordx4 a[116:119], v8, s[40:43], s66 offen offset:2048 sc1\n\t"
    "v_mfma_f32_16x16x32_bf16 v[176:179], a[212:215], a[68:71], v[176:179]\n\t"
    "v_mfma_f32_16x16x32_bf16 v[180:183], a[208:211], a[72:75], 0\n\t"
    "ds_read_b64_tr_b16 a[234:235], v9 offset:17984\n\t"
    "v_mfma_f32_16x16x32_bf16 v[180:183], a[212:215], a[76:79], v[180:183]\n\t"
    "v_mfma_f32_16x16x32_bf16 v[184:187], a[208:211], a[80:83], 0\n\t"
    "ds_read_b64_tr_b16 a[236:237], v9 offset:21568\n\t"
    "buffer_load_dwordx4 a[120:123], v7, s[40:43], s66 offen offset:3072 sc1\n\t"
    "v_mfma_f32_16x16x32_bf16 v[184:187], a[212:215], a[84:87], v[184:187]\n\t"
    "v_mfma_f32_16x16x32_bf16 v[188:191], a[208:211], a[88:91], 0\n\t"
    "ds_read_b64_tr_b16 a[238:239], v9 offset:25152\n\t"
    "v_mfma_f32_16x16x32_bf16 v[188:191], a[212:215], a[92:95], v[188:191]\n\t"
    "buffer_load_dwordx4 a[124:127], v8, s[40:43], s66 offen offset:3072 sc1\n\t"
    "s_waitcnt lgkmcnt(8)\n\t"
    "v_mfma_f32_16x16x32_bf16 v[236:239], a[216:219], a[88:91], 0\n\t"
    "ds_read_b64_tr_b16 a[192:193], v9 offset:96\n\t"
    "v_mfma_f32_16x16x32_bf16 v[236:239], a[220:223], a[92:95], v[236:239]\n\t"
    "v_mfma_f32_16x16x32_bf16 v[232:235], a[216:219], a[80:83], 0\n\t"
    "ds_read_b64_tr_b16 a[194:195], v9 offset:3680\n\t"
    "s_add_u32 s66, s66, 262144\n\t"
    "v_mfma_f32_16x16x32_bf16 v[232:235], a[220:223], a[84:87], v[232:235]\n\t"
    "v_mfma_f32_16x16x32_bf16 v[228:231], a[216:219], a[72:75], 0\n\t"
    "ds_read_b64_tr_b16 a[196:197], v9 offset:7264\n\t"
    "v_mfma_f32_16x16x32_bf16 v[228:231], a[220:223], a[76:79], v[228:231]\n\t"
    "buffer_load_dwordx4 a[128:131], v7, s[40:43], s66 offen offset:0 sc1\n\t"
    "v_mfma_f32_16x16x32_bf16 v[224:227], a[216:219], a[64:67], 0\n\t"
    "ds_read_b64_tr_b16 a[198:199], v9 offset:10848\n\t"
    "v_mfma_f32_16x16x32_bf16 v[224:227], a[220:223], a[68:71], v[224:227]\n\t"
    "s_waitcnt lgkmcnt(8)\n\t"
    "v_mfma_f32_16x16x32_bf16 v[160:163], a[224:227], a[64:67], 0\n\t"
    "ds_read_b64_tr_b16 a[200:201], v9 offset:14432\n\t"
    "buffer_load_dwordx4 a[132:135], v8, s[40:43], s66 offen offset:0 sc1\n\t"
    "v_mfma_f32_16x16x32_bf16 v[160:163], a[228:231], a[68:71], v[160:163]\n\t"
    "v_mfma_f32_16x16x32_bf16 v[164:167], a[224:227], a[72:75], 0\n\t"
    "ds_read_b64_tr_b16 a[202:203], v9 offset:18016\n\t"
    "v_mfma_f32_16x16x32_bf16 v[164:167], a[228:231], a[76:79], v[164:167]\n\t"
    "v_mfma_f32_16x16x32_bf16 v[168:171], a[224:227], a[80:83], 0\n\t"
    "ds_read_b64_tr_b16 a[204:205], v9 offset:21600\n\t"
    "buffer_load_dwordx4 a[136:139], v7, s[40:43], s66 offen offset:1024 sc1\n\t"
    "v_mfma_f32_16x16x32_bf16 v[168:171], a[228:231], a[84:87], v[168:171]\n\t"
    "v_mfma_f32_16x16x32_bf16 v[172:175], a[224:227], a[88:91], 0\n\t"
    "ds_read_b64_tr_b16 a[206:207], v9 offset:25184\n\t"
    "v_mfma_f32_16x16x32_bf16 v[172:175], a[228:231], a[92:95], v[172:175]\n\t"
    "buffer_load_dwordx4 a[140:143], v8, s[40:43], s66 offen offset:1024 sc1\n\t"
    "s_waitcnt lgkmcnt(8)\n\t"
    "v_mfma_f32_16x16x32_bf16 v[252:255], a[232:235], a[88:91], 0\n\t"
    "ds_read_b64_tr_b16 a[208:209], v9 offset:128\n\t"
    "v_mfma_f32_16x16x32_bf16 v[252:255], a[236:239], a[92:95], v[252:255]\n\t"
    "v_mfma_f32_16x16x32_bf16 v[248:251], a[232:235], a[80:83], 0\n\t"
    "ds_read_b64_tr_b16 a[210:211], v9 offset:3712\n\t"
    "buffer_load_dwordx4 a[144:147], v7, s[40:43], s66 offen offset:2048 sc1\n\t"
    "v_mfma_f32_16x16x32_bf16 v[248:251], a[236:239], a[84:87], v[248:251]\n\t"
    "v_mfma_f32_16x16x32_bf16 v[244:247], a[232:235], a[72:75], 0\n\t"
    "ds_read_b64_tr_b16 a[212:213], v9 offset:7296\n\t"
    "v_mfma_f32_16x16x32_bf16 v[244:247], a[236:239], a[76:79], v[244:247]\n\t"
    "buffer_load_dwordx4 a[148:151], v8, s[40:43], s66 offen offset:2048 sc1\n\t"
    "v_mfma_f32_16x16x32_bf16 v[240:243], a[232:235], a[64:67], 0\n\t"
    "ds_read_b64_tr_b16 a[214:215], v9 offset:10880\n\t"
    "v_mfma_f32_16x16x32_bf16 v[240:243], a[236:239], a[68:71], v[240:243]\n\t"
    "s_waitcnt lgkmcnt(8)\n\t"
    "v_mfma_f32_16x16x32_bf16 v[144:147], a[192:195], a[64:67], 0\n\t"
    "ds_read_b64_tr_b16 a[216:217], v9 offset:14464\n\t"
    "buffer_load_dwordx4 a[152:155], v7, s[40:43], s66 offen offset:3072 sc1\n\t"
    "v_mfma_f32_16x16x32_bf16 v[144:147], a[196:199], a[68:71], v[144:147]\n\t"
    "v_mfma_f32_16x16x32_bf16 v[148:151], a[192:195], a[72:75], 0\n\t"
    "ds_read_b64_tr_b16 a[218:219], v9 offset:18048\n\t"
    "v_mfma_f32_16x16x32_bf16 v[148:151], a[196:199], a[76:79], v[148:151]\n\t"
    "v_mfma_f32_16x16x32_bf16 v[152:155], a[192:195], a[80:83], 0\n\t"
    "ds_read_b64_tr_b16 a[220:221], v9 offset:21632\n\t"
    "buffer_load_dwordx4 a[156:159], v8, s[40:43], s66 offen offset:3072 sc1\n\t"
    "v_mfma_f32_16x16x32_bf16 v[152:155], a[196:199], a[84:87], v[152:155]\n\t"
    "v_mfma_f32_16x16x32_bf16 v[156:159], a[192:195], a[88:91], 0\n\t"
    "ds_read_b64_tr_b16 a[222:223], v9 offset:25216\n\t"
    "v_mfma_f32_16x16x32_bf16 v[156:159], a[196:199], a[92:95], v[156:159]\n\t"
    "s_add_u32 s66, s66, 262144\n\t"
    "s_waitcnt lgkmcnt(8)\n\t"
    "v_mfma_f32_16x16x32_bf16 a[12:15], a[200:203], a[88:91], 0\n\t"
    "ds_read_b64_tr_b16 a[224:225], v9 offset:160\n\t"
    "v_mfma_f32_16x16x32_bf16 a[12:15], a[204:207], a[92:95], a[12:15]\n\t"
    "s_add_u32 m0, s68, 110592\n\t"
    "v_mfma_f32_16x16x32_bf16 a[8:11], a[200:203], a[80:83], 0\n\t"
    "ds_read_b64_tr_b16 a[226:227], v9 offset:3744\n\t"
    "buffer_load_dwordx4 v2, s[44:47], s64 offen lds\n\t"
    "v_mfma_f32_16x16x32_bf16 a[8:11], a[204:207], a[84:87], a[8:11]\n\t"
    "v_mfma_f32_16x16x32_bf16 a[4:7], a[200:203], a[72:75], 0\n\t"
    "ds_read_b64_tr_b16 a[228:229], v9 offset:7328\n\t"
    "v_mfma_f32_16x16x32_bf16 a[4:7], a[204:207], a[76:79], a[4:7]\n\t"
    "buffer_load_dwordx4 a[160:163], v7, s[40:43], s66 offen offset:0 sc1\n\t"
    "v_mfma_f32_16x16x32_bf16 a[0:3], a[200:203], a[64:67], 0\n\t"
    "ds_read_b64_tr_b16 a[230:231], v9 offset:10912\n\t"
    "v_mfma_f32_16x16x32_bf16 a[0:3], a[204:207], a[68:71], a[0:3]\n\t"
    "s_add_u32 m0, s68, 124928\n\t"
    "s_waitcnt lgkmcnt(8)\n\t"
    "v_mfma_f32_16x16x32_bf16 v[128:131], a[208:211], a[64:67], 0\n\t"
    "ds_read_b64_tr_b16 a[232:233], v9 offset:14496\n\t"
    "buffer_load_dwordx4 v2, s[48:51], s64 offen lds\n\t"
    "v_mfma_f32_16x16x32_bf16 v[128:131], a[212:215], a[68:71], v[128:131]\n\t"
    "v_mfma_f32_16x16x32_bf16 v[132:135], a[208:211], a[72:75], 0\n\t"
    "ds_read_b64_tr_b16 a[234:235], v9 offset:18080\n\t"
    "v_mfma_f32_16x16x32_bf16 v[132:135], a[212:215], a[76:79], v[132:135]\n\t"
    "buffer_load_dwordx4 a[164:167], v8, s[40:43], s66 offen offset:0 sc1\n\t"
    "v_mfma_f32_16x16x32_bf16 v[136:139], a[208:211], a[80:83], 0\n\t"
    "ds_read_b64_tr_b16 a[236:237], v9 offset:21664\n\t"
    "v_mfma_f32_16x16x32_bf16 v[136:139], a[212:215], a[84:87], v[136:139]\n\t"
    "v_mfma_f32_16x16x32_bf16 v[140:143], a[208:211], a[88:91], 0\n\t"
    "s_add_u32 m0, s68, 114688\n\t"
    "ds_read_b64_tr_b16 a[238:239], v9 offset:25248\n\t"
    "v_mfma_f32_16x16x32_bf16 v[140:143], a[212:215], a[92:95], v[140:143]\n\t"
    "buffer_load_dwordx4 v3, s[44:47], s64 offen lds\n\t"
    "s_waitcnt lgkmcnt(8)\n\t"
    "v_mfma_f32_16x16x32_bf16 a[28:31], a[216:219], a[88:91], 0\n\t"
    "ds_read_b64_tr_b16 a[192:193], v9 offset:192\n\t"
    "v_mfma_f32_16x16x32_bf16 a[28:31], a[220:223], a[92:95], a[28:31]\n\t"
    "v_mfma_f32_16x16x32_bf16 a[24:27], a[216:219], a[80:83], 0\n\t"
    "ds_read_b64_tr_b16 a[194:195], v9 offset:3776\n\t"
    "buffer_load_dwordx4 a[168:171], v7, s[40:43], s66 offen offset:1024 sc1\n\t"
    "v_mfma_f32_16x16x32_bf16 a[24:27], a[220:223], a[84:87], a[24:27]\n\t"
    "v_mfma_f32_16x16x32_bf16 a[20:23], a[216:219], a[72:75], 0\n\t"
    "s_add_u32 m0, s68, 129024\n\t"
    "ds_read_b64_tr_b16 a[196:197], v9 offset:7360\n\t"
    "v_mfma_f32_16x16x32_bf16 a[20:23], a[220:223], a[76:79], a[20:23]\n\t"
    "buffer_load_dwordx4 v3, s[48:51], s64 offen lds\n\t"
    "v_mfma_f32_16x16x32_bf16 a[16:19], a[216:219], a[64:67], 0\n\t"
    "ds_read_b64_tr_b16 a[198:199], v9 offset:10944\n\t"
    "v_mfma_f32_16x16x32_bf16 a[16:19], a[220:223], a[68:71], a[16:19]\n\t"
    "s_waitcnt lgkmcnt(8)\n\t"
    "v_mfma_f32_16x16x32_bf16 v[112:115], a[224:227], a[64:67], 0\n\t"
    "ds_read_b64_tr_b16 a[200:201], v9 offset:14528\n\t"
    "buffer_load_dwordx4 a[172:175], v8, s[40:43], s66 offen offset:1024 sc1\n\t"
    "v_mfma_f32_16x16x32_bf16 v[112:115], a[228:231], a[68:71], v[112:115]\n\t"
    "v_mfma_f32_16x16x32_bf16 v[116:119], a[224:227], a[72:75], 0\n\t"
    "s_add_u32 m0, s68, 118784\n\t"
    "ds_read_b64_tr_b16 a[202:203], v9 offset:18112\n\t"
    "v_mfma_f32_16x16x32_bf16 v[116:119], a[228:231], a[76:79], v[116:119]\n\t"
    "buffer_load_dwordx4 v4, s[44:47], s64 offen lds\n\t"
    "v_mfma_f32_16x16x32_bf16 v[120:123], a[224:227], a[80:83], 0\n\t"
    "ds_read_b64_tr_b16 a[204:205], v9 offset:21696\n\t"
    "v_mfma_f32_16x16x32_bf16 v[120:123], a[228:231], a[84:87], v[120:123]\n\t"
    "v_mfma_f32_16x16x32_bf16 v[124:127], a[224:227], a[88:91], 0\n\t"
    "ds_read_b64_tr_b16 a[206:207], v9 offset:25280\n\t"
    "v_mfma_f32_16x16x32_bf16 v[124:127], a[228:231], a[92:95], v[124:127]\n\t"
    "buffer_load_dwordx4 a[176:179], v7, s[40:43], s66 offen offset:2048 sc1\n\t"
    "s_waitcnt lgkmcnt(8)\n\t"
    "v_mfma_f32_16x16x32_bf16 a[44:47], a[232:235], a[88:91], 0\n\t"
    "ds_read_b128 a[208:211], v11 offset:0\n\t"
    "v_mfma_f32_16x16x32_bf16 a[44:47], a[236:239], a[92:95], a[44:47]\n\t"
    "s_add_u32 m0, s68, 133120\n\t"
    "v_mfma_f32_16x16x32_bf16 a[40:43], a[232:235], a[80:83], 0\n\t"
    "ds_read_b128 a[212:215], v12 offset:0\n\t"
    "buffer_load_dwordx4 v4, s[48:51], s64 offen lds\n\t"
    "v_mfma_f32_16x16x32_bf16 a[40:43], a[236:239], a[84:87], a[40:43]\n\t"
    "v_mfma_f32_16x16x32_bf16 a[36:39], a[232:235], a[72:75], 0\n\t"
    "v_mfma_f32_16x16x32_bf16 a[36:39], a[236:239], a[76:79], a[36:39]\n\t"
    "buffer_load_dwordx4 a[180:183], v8, s[40:43], s66 offen offset:2048 sc1\n\t"
    "v_mfma_f32_16x16x32_bf16 a[32:35], a[232:235], a[64:67], 0\n\t"
    "v_mfma_f32_16x16x32_bf16 a[32:35], a[236:239], a[68:71], a[32:35]\n\t"
    "s_add_u32 m0, s69, 110592\n\t"
    "s_waitcnt lgkmcnt(6)\n\t"
    "v_mfma_f32_16x16x32_bf16 v[96:99], a[192:195], a[64:67], 0\n\t"
    "ds_read_b128 a[216:219], v11 offset:4096\n\t"
    "buffer_load_dwordx4 v5, s[52:55], s64 offen lds\n\t"
    "v_mfma_f32_16x16x32_bf16 v[96:99], a[196:199], a[68:71], v[96:99]\n\t"
    "v_mfma_f32_16x16x32_bf16 v[100:103], a[192:195], a[72:75], 0\n\t"
    "ds_read_b128 a[220:223], v12 offset:4096\n\t"
    "v_mfma_f32_16x16x32_bf16 v[100:103], a[196:199], a[76:79], v[100:103]\n\t"
    "buffer_load_dwordx4 a[184:187], v7, s[40:43], s66 offen offset:3072 sc1\n\t"
    "v_mfma_f32_16x16x32_bf16 v[104:107], a[192:195], a[80:83], 0\n\t"
    "v_mfma_f32_16x16x32_bf16 v[104:107], a[196:199], a[84:87], v[104:107]\n\t"
    "v_mfma_f32_16x16x32_bf16 v[108:111], a[192:195], a[88:91], 0\n\t"
    "s_add_u32 m0, s68, 139264\n\t"
    "v_mfma_f32_16x16x32_bf16 v[108:111], a[196:199], a[92:95], v[108:111]\n\t"
    "buffer_load_dwordx4 v6, s[56:59], s65 offen lds\n\t"
    "s_waitcnt lgkmcnt(4)\n\t"
    "v_mfma_f32_16x16x32_bf16 a[60:63], a[200:203], a[88:91], 0\n\t"
    "ds_read_b128 a[224:227], v11 offset:2048\n\t"
    "v_mfma_f32_16x16x32_bf16 a[60:63], a[204:207], a[92:95], a[60:63]\n\t"
    "v_mfma_f32_16x16x32_bf16 a[56:59], a[200:203], a[80:83], 0\n\t"
    "ds_read_b128 a[228:231], v12 offset:2048\n\t"
    "buffer_load_dwordx4 a[188:191], v8, s[40:43], s66 offen offset:3072 sc1\n\t"
    "v_mfma_f32_16x16x32_bf16 a[56:59], a[204:207], a[84:87], a[56:59]\n\t"
    "v_mfma_f32_16x16x32_bf16 a[52:55], a[200:203], a[72:75], 0\n\t"
    "s_add_u32 m0, s68, 143360\n\t"
    "v_mfma_f32_16x16x32_bf16 a[52:55], a[204:207], a[76:79], a[52:55]\n\t"
    "buffer_load_dwordx4 v6, s[60:63], s65 offen lds\n\t"
    "v_mfma_f32_16x16x32_bf16 a[48:51], a[200:203], a[64:67], 0\n\t"
    "v_mfma_f32_16x16x32_bf16 a[48:51], a[204:207], a[68:71], a[48:51]\n\t"
    "s_waitcnt lgkmcnt(4)\n\t"
    "v_mfma_f32_16x16x32_bf16 v[32:35], a[64:67], a[208:211], 0\n\t"
    "ds_read_b128 a[232:235], v11 offset:6144\n\t"
    "buffer_load_dword v28, v15, s[20:23], s84 offen\n\t"
    "v_mfma_f32_16x16x32_bf16 v[32:35], a[68:71], a[212:215], v[32:35]\n\t"
    "v_mov_b32_e32 v80, s82\n\t"
    "v_mfma_f32_16x16x32_bf16 v[36:39], a[72:75], a[208:211], 0\n\t"
    "ds_read_b128 a[236:239], v12 offset:6144\n\t"
    "v_mfma_f32_16x16x32_bf16 v[36:39], a[76:79], a[212:215], v[36:39]\n\t"
    "buffer_load_dword v28, v0, s[24:27], s86 offen\n\t"
    "v_mov_b32_e32 v81, s83\n\t"
    "v_mfma_f32_16x16x32_bf16 v[40:43], a[80:83], a[208:211], 0\n\t"
    "v_mfma_f32_16x16x32_bf16 v[40:43], a[84:87], a[212:215], v[40:43]\n\t"
    "v_lshl_or_b32 v82, s87, 10, v31\n\t"
    "v_mfma_f32_16x16x32_bf16 v[44:47], a[88:91], a[208:211], 0\n\t"
    "v_mfma_f32_16x16x32_bf16 v[44:47], a[92:95], a[212:215], v[44:47]\n\t"
    "v_cmp_eq_u32_e32 vcc, 0, v82\n\t"
    "s_waitcnt lgkmcnt(4)\n\t"
    "v_mfma_f32_16x16x32_bf16 v[76:79], a[88:91], a[216:219], 0\n\t"
    "v_mfma_f32_16x16x32_bf16 v[76:79], a[92:95], a[220:223], v[76:79]\n\t"
    "v_bfrev_b32_e32 v82, 1\n\t"
    "v_mfma_f32_16x16x32_bf16 v[72:75], a[80:83], a[216:219], 0\n\t"
    "v_mfma_f32_16x16x32_bf16 v[72:75], a[84:87], a[220:223], v[72:75]\n\t"
    "s_add_u32 s64, s64, 458752\n\t"
    "s_add_u32 s90, s91, 35704832\n\t"
    "v_mfma_f32_16x16x32_bf16 v[68:71], a[72:75], a[216:219], 0\n\t"
    "s_add_u32 s65, s65, 128\n\t"
    "v_mfma_f32_16x16x32_bf16 v[68:71], a[76:79], a[220:223], v[68:71]\n\t"
    "s_add_u32 s66, s66, 262144\n\t"
    "v_cndmask_b32_e64 v82, v82, 0, vcc\n\t"
    "v_mfma_f32_16x16x32_bf16 v[64:67], a[64:67], a[216:219], 0\n\t"
    "s_add_u32 s84, s84, s85\n\t"
    "v_mfma_f32_16x16x32_bf16 v[64:67], a[68:71], a[220:223], v[64:67]\n\t"
    "s_add_u32 s86, s86, s39\n\t"
    "s_waitcnt vmcnt(27) lgkmcnt(0)\n\t"
    "s_barrier\n\t"
    "buffer_store_dwordx2 v[80:81], v82, s[16:19], s90 offen sc1\n\t"
    "v_mfma_f32_16x16x32_bf16 v[48:51], a[64:67], a[224:227], 0\n\t"
    "ds_read_b64_tr_b16 a[192:193], v9 offset:36864\n\t"
    "v_mfma_f32_16x16x32_bf16 v[48:51], a[68:71], a[228:231], v[48:51]\n\t"
    "ds_read_b64_tr_b16 a[194:195], v9 offset:40448\n\t"
    "v_mfma_f32_16x16x32_bf16 v[52:55], a[72:75], a[224:227], 0\n\t"
    "ds_read_b64_tr_b16 a[196:197], v9 offset:44032\n\t"
    "v_mfma_f32_16x16x32_bf16 v[52:55], a[76:79], a[228:231], v[52:55]\n\t"
    "ds_read_b64_tr_b16 a[198:199], v9 offset:47616\n\t"
    "v_mfma_f32_16x16x32_bf16 v[56:59], a[80:83], a[224:227], 0\n\t"
    "ds_read_b64_tr_b16 a[200:201], v9 offset:51200\n\t"
    "v_mfma_f32_16x16x32_bf16 v[56:59], a[84:87], a[228:231], v[56:59]\n\t"
    "ds_read_b64_tr_b16 a[202:203], v9 offset:54784\n\t"
    "v_mfma_f32_16x16x32_bf16 v[60:63], a[88:91], a[224:227], 0\n\t"
    "ds_read_b64_tr_b16 a[204:205], v9 offset:58368\n\t"
    "v_mfma_f32_16x16x32_bf16 v[60:63], a[92:95], a[228:231], v[60:63]\n\t"
    "ds_read_b64_tr_b16 a[206:207], v9 offset:61952\n\t"
    "v_mfma_f32_16x16x32_bf16 v[92:95], a[88:91], a[232:235], 0\n\t"
    "ds_read_b64_tr_b16 a[208:209], v9 offset:36896\n\t"
    "v_mfma_f32_16x16x32_bf16 v[92:95], a[92:95], a[236:239], v[92:95]\n\t"
    "ds_read_b64_tr_b16 a[210:211], v9 offset:40480\n\t"
    "v_mfma_f32_16x16x32_bf16 v[88:91], a[80:83], a[232:235], 0\n\t"
    "ds_read_b64_tr_b16 a[212:213], v9 offset:44064\n\t"
    "v_mfma_f32_16x16x32_bf16 v[88:91], a[84:87], a[236:239], v[88:91]\n\t"
    "ds_read_b64_tr_b16 a[214:215], v9 offset:47648\n\t"
    "v_mfma_f32_16x16x32_bf16 v[84:87], a[72:75], a[232:235], 0\n\t"
    "v_mfma_f32_16x16x32_bf16 v[84:87], a[76:79], a[236:239], v[84:87]\n\t"
    "v_mfma_f32_16x16x32_bf16 v[80:83], a[64:67], a[232:235], 0\n\t"
    "v_mfma_f32_16x16x32_bf16 v[80:83], a[68:71], a[236:239], v[80:83]\n\t"
    "s_branch L_g1p1_%=\n\t"
    "L_g1_%=:\n\t"
    "s_waitcnt lgkmcnt(8)\n\t"
    "v_mfma_f32_16x16x32_bf16 v[192:195], a[192:195], a[64:67], v[192:195]\n\t"
    "ds_read_b64_tr_b16 a[216:217], v9 offset:14368\n\t"
    "v_mfma_f32_16x16x32_bf16 v[192:195], a[196:199], a[68:71], v[192:195]\n\t"
    "s_add_u32 m0, s68, 110592\n\t"
    "v_mfma_f32_16x16x32_bf16 v[196:199], a[192:195], a[72:75], v[196:199]\n\t"
    "ds_read_b64_tr_b16 a[218:219], v9 offset:17952\n\t"
    "v_mfma_f32_16x16x32_bf16 v[196:199], a[196:199], a[76:79], v[196:199]\n\t"
    "buffer_load_dwordx4 v2, s[44:47], s64 offen lds\n\t"
    "v_mfma_f32_16x16x32_bf16 v[200:203], a[192:195], a[80:83], v[200:203]\n\t"
    "ds_read_b64_tr_b16 a[220:221], v9 offset:21536\n\t"
    "v_mfma_f32_16x16x32_bf16 v[200:203], a[196:199], a[84:87], v[200:203]\n\t"
    "v_mfma_f32_16x16x32_bf16 v[204:207], a[192:195], a[88:91], v[204:207]\n\t"
    "ds_read_b64_tr_b16 a[222:223], v9 offset:25120\n\t"
    "v_mfma_f32_16x16x32_bf16 v[204:207], a[196:199], a[92:95], v[204:207]\n\t"
    "buffer_load_dwordx4 a[160:163], v7, s[40:43], s66 offen offset:0 sc1\n\t"
    "s_waitcnt lgkmcnt(8)\n\t"
    "v_mfma_f32_16x16x32_bf16 v[220:223], a[200:203], a[88:91], v[220:223]\n\t"
    "ds_read_b64_tr_b16 a[224:225], v9 offset:64\n\t"
    "v_mfma_f32_16x16x32_bf16 v[220:223], a[204:207], a[92:95], v[220:223]\n\t"
    "v_mfma_f32_16x16x32_bf16 v[216:219], a[200:203], a[80:83], v[216:219]\n\t"
    "ds_read_b64_tr_b16 a[226:227], v9 offset:3648\n\t"
    "v_mfma_f32_16x16x32_bf16 v[216:219], a[204:207], a[84:87], v[216:219]\n\t"
    "s_add_u32 m0, s68, 124928\n\t"
    "v_mfma_f32_16x16x32_bf16 v[212:215], a[200:203], a[72:75], v[212:215]\n\t"
    "ds_read_b64_tr_b16 a[228:229], v9 offset:7232\n\t"
    "v_mfma_f32_16x16x32_bf16 v[212:215], a[204:207], a[76:79], v[212:215]\n\t"
    "buffer_load_dwordx4 v2, s[48:51], s64 offen lds\n\t"
    "v_mfma_f32_16x16x32_bf16 v[208:211], a[200:203], a[64:67], v[208:211]\n\t"
    "ds_read_b64_tr_b16 a[230:231], v9 offset:10816\n\t"
    "v_mfma_f32_16x16x32_bf16 v[208:211], a[204:207], a[68:71], v[208:211]\n\t"
    "s_waitcnt lgkmcnt(8)\n\t"
    "v_mfma_f32_16x16x32_bf16 v[176:179], a[208:211], a[64:67], v[176:179]\n\t"
    "ds_read_b64_tr_b16 a[232:233], v9 offset:14400\n\t"
    "v_mfma_f32_16x16x32_bf16 v[176:179], a[212:215], a[68:71], v[176:179]\n\t"
    "buffer_load_dwordx4 a[164:167], v8, s[40:43], s66 offen offset:0 sc1\n\t"
    "v_mfma_f32_16x16x32_bf16 v[180:183], a[208:211], a[72:75], v[180:183]\n\t"
    "ds_read_b64_tr_b16 a[234:235], v9 offset:17984\n\t"
    "v_mfma_f32_16x16x32_bf16 v[180:183], a[212:215], a[76:79], v[180:183]\n\t"
    "s_add_u32 m0, s68, 114688\n\t"
    "v_mfma_f32_16x16x32_bf16 v[184:187], a[208:211], a[80:83], v[184:187]\n\t"
    "ds_read_b64_tr_b16 a[236:237], v9 offset:21568\n\t"
    "v_mfma_f32_16x16x32_bf16 v[184:187], a[212:215], a[84:87], v[184:187]\n\t"
    "buffer_load_dwordx4 v3, s[44:47], s64 offen lds\n\t"
    "v_mfma_f32_16x16x32_bf16 v[188:191], a[208:211], a[88:91], v[188:191]\n\t"
    "ds_read_b64_tr_b16 a[238:239], v9 offset:25152\n\t"
    "v_mfma_f32_16x16x32_bf16 v[188:191], a[212:215], a[92:95], v[188:191]\n\t"
    "s_waitcnt lgkmcnt(8)\n\t"
    "v_mfma_f32_16x16x32_bf16 v[236:239], a[216:219], a[88:91], v[236:239]\n\t"
    "ds_read_b64_tr_b16 a[192:193], v9 offset:96\n\t"
    "v_mfma_f32_16x16x32_bf16 v[236:239], a[220:223], a[92:95], v[236:239]\n\t"
    "v_mfma_f32_16x16x32_bf16 v[232:235], a[216:219], a[80:83], v[232:235]\n\t"
    "ds_read_b64_tr_b16 a[194:195], v9 offset:3680\n\t"
    "v_mfma_f32_16x16x32_bf16 v[232:235], a[220:223], a[84:87], v[232:235]\n\t"
    "buffer_load_dwordx4 a[168:171], v7, s[40:43], s66 offen offset:1024 sc1\n\t"
    "v_mfma_f32_16x16x32_bf16 v[228:231], a[216:219], a[72:75], v[228:231]\n\t"
    "ds_read_b64_tr_b16 a[196:197], v9 offset:7264\n\t"
    "v_mfma_f32_16x16x32_bf16 v[228:231], a[220:223], a[76:79], v[228:231]\n\t"
    "s_add_u32 m0, s68, 129024\n\t"
    "v_mfma_f32_16x16x32_bf16 v[224:227], a[216:219], a[64:67], v[224:227]\n\t"
    "ds_read_b64_tr_b16 a[198:199], v9 offset:10848\n\t"
    "v_mfma_f32_16x16x32_bf16 v[224:227], a[220:223], a[68:71], v[224:227]\n\t"
    "buffer_load_dwordx4 v3, s[48:51], s64 offen lds\n\t"
    "s_waitcnt lgkmcnt(8)\n\t"
    "v_mfma_f32_16x16x32_bf16 v[160:163], a[224:227], a[64:67], v[160:163]\n\t"
    "ds_read_b64_tr_b16 a[200:201], v9 offset:14432\n\t"
    "v_mfma_f32_16x16x32_bf16 v[160:163], a[228:231], a[68:71], v[160:163]\n\t"
    "v_mfma_f32_16x16x32_bf16 v[164:167], a[224:227], a[72:75], v[164:167]\n\t"
    "ds_read_b64_tr_b16 a[202:203], v9 offset:18016\n\t"
    "v_mfma_f32_16x16x32_bf16 v[164:167], a[228:231], a[76:79], v[164:167]\n\t"
    "v_mfma_f32_16x16x32_bf16 v[168:171], a[224:227], a[80:83], v[168:171]\n\t"
    "ds_read_b64_tr_b16 a[204:205], v9 offset:21600\n\t"
    "v_mfma_f32_16x16x32_bf16 v[168:171], a[228:231], a[84:87], v[168:171]\n\t"
    "buffer_load_dwordx4 a[172:175], v8, s[40:43], s66 offen offset:1024 sc1\n\t"
    "v_mfma_f32_16x16x32_bf16 v[172:175], a[224:227], a[88:91], v[172:175]\n\t"
    "ds_read_b64_tr_b16 a[206:207], v9 offset:25184\n\t"
    "v_mfma_f32_16x16x32_bf16 v[172:175], a[228:231], a[92:95], v[172:175]\n\t"
    "s_add_u32 m0, s68, 118784\n\t"
    "s_waitcnt lgkmcnt(8)\n\t"
    "v_mfma_f32_16x16x32_bf16 v[252:255], a[232:235], a[88:91], v[252:255]\n\t"
    "ds_read_b64_tr_b16 a[208:209], v9 offset:128\n\t"
    "v_mfma_f32_16x16x32_bf16 v[252:255], a[236:239], a[92:95], v[252:255]\n\t"
    "buffer_load_dwordx4 v4, s[44:47], s64 offen lds\n\t"
    "v_mfma_f32_16x16x32_bf16 v[248:251], a[232:235], a[80:83], v[248:251]\n\t"
    "ds_read_b64_tr_b16 a[210:211], v9 offset:3712\n\t"
    "v_mfma_f32_16x16x32_bf16 v[248:251], a[236:239], a[84:87], v[248:251]\n\t"
    "v_mfma_f32_16x16x32_bf16 v[244:247], a[232:235], a[72:75], v[244:247]\n\t"
    "ds_read_b64_tr_b16 a[212:213], v9 offset:7296\n\t"
    "v_mfma_f32_16x16x32_bf16 v[244:247], a[236:239], a[76:79], v[244:247]\n\t"
    "buffer_load_dwordx4 a[176:179], v7, s[40:43], s66 offen offset:2048 sc1\n\t"
    "v_mfma_f32_16x16x32_bf16 v[240:243], a[232:235], a[64:67], v[240:243]\n\t"
    "ds_read_b64_tr_b16 a[214:215], v9 offset:10880\n\t"
    "v_mfma_f32_16x16x32_bf16 v[240:243], a[236:239], a[68:71], v[240:243]\n\t"
    "s_waitcnt lgkmcnt(8)\n\t"
    "v_mfma_f32_16x16x32_bf16 v[144:147], a[192:195], a[64:67], v[144:147]\n\t"
    "ds_read_b64_tr_b16 a[216:217], v9 offset:14464\n\t"
    "v_mfma_f32_16x16x32_bf16 v[144:147], a[196:199], a[68:71], v[144:147]\n\t"
    "s_add_u32 m0, s68, 133120\n\t"
    "v_mfma_f32_16x16x32_bf16 v[148:151], a[192:195], a[72:75], v[148:151]\n\t"
    "ds_read_b64_tr_b16 a[218:219], v9 offset:18048\n\t"
    "v_mfma_f32_16x16x32_bf16 v[148:151], a[196:199], a[76:79], v[148:151]\n\t"
    "buffer_load_dwordx4 v4, s[48:51], s64 offen lds\n\t"
    "v_mfma_f32_16x16x32_bf16 v[152:155], a[192:195], a[80:83], v[152:155]\n\t"
    "ds_read_b64_tr_b16 a[220:221], v9 offset:21632\n\t"
    "v_mfma_f32_16x16x32_bf16 v[152:155], a[196:199], a[84:87], v[152:155]\n\t"
    "v_mfma_f32_16x16x32_bf16 v[156:159], a[192:195], a[88:91], v[156:159]\n\t"
    "ds_read_b64_tr_b16 a[222:223], v9 offset:25216\n\t"
    "v_mfma_f32_16x16x32_bf16 v[156:159], a[196:199], a[92:95], v[156:159]\n\t"
    "buffer_load_dwordx4 a[180:183], v8, s[40:43], s66 offen offset:2048 sc1\n\t"
    "s_waitcnt lgkmcnt(8)\n\t"
    "v_mfma_f32_16x16x32_bf16 a[12:15], a[200:203], a[88:91], a[12:15]\n\t"
    "ds_read_b64_tr_b16 a[224:225], v9 offset:160\n\t"
    "v_mfma_f32_16x16x32_bf16 a[12:15], a[204:207], a[92:95], a[12:15]\n\t"
    "s_add_u32 m0, s69, 110592\n\t"
    "v_mfma_f32_16x16x32_bf16 a[8:11], a[200:203], a[80:83], a[8:11]\n\t"
    "ds_read_b64_tr_b16 a[226:227], v9 offset:3744\n\t"
    "v_mfma_f32_16x16x32_bf16 a[8:11], a[204:207], a[84:87], a[8:11]\n\t"
    "buffer_load_dwordx4 v5, s[52:55], s64 offen lds\n\t"
    "v_mfma_f32_16x16x32_bf16 a[4:7], a[200:203], a[72:75], a[4:7]\n\t"
    "ds_read_b64_tr_b16 a[228:229], v9 offset:7328\n\t"
    "v_mfma_f32_16x16x32_bf16 a[4:7], a[204:207], a[76:79], a[4:7]\n\t"
    "v_mfma_f32_16x16x32_bf16 a[0:3], a[200:203], a[64:67], a[0:3]\n\t"
    "ds_read_b64_tr_b16 a[230:231], v9 offset:10912\n\t"
    "v_mfma_f32_16x16x32_bf16 a[0:3], a[204:207], a[68:71], a[0:3]\n\t"
    "s_waitcnt lgkmcnt(8)\n\t"
    "v_mfma_f32_16x16x32_bf16 v[128:131], a[208:211], a[64:67], v[128:131]\n\t"
    "ds_read_b64_tr_b16 a[232:233], v9 offset:14496\n\t"
    "v_mfma_f32_16x16x32_bf16 v[128:131], a[212:215], a[68:71], v[128:131]\n\t"
    "buffer_load_dwordx4 a[184:187], v7, s[40:43], s66 offen offset:3072 sc1\n\t"
    "v_mfma_f32_16x16x32_bf16 v[132:135], a[208:211], a[72:75], v[132:135]\n\t"
    "ds_read_b64_tr_b16 a[234:235], v9 offset:18080\n\t"
    "v_mfma_f32_16x16x32_bf16 v[132:135], a[212:215], a[76:79], v[132:135]\n\t"
    "s_add_u32 m0, s68, 139264\n\t"
    "v_mfma_f32_16x16x32_bf16 v[136:139], a[208:211], a[80:83], v[136:139]\n\t"
    "ds_read_b64_tr_b16 a[236:237], v9 offset:21664\n\t"
    "v_mfma_f32_16x16x32_bf16 v[136:139], a[212:215], a[84:87], v[136:139]\n\t"
    "buffer_load_dwordx4 v6, s[56:59], s65 offen lds\n\t"
    "v_mfma_f32_16x16x32_bf16 v[140:143], a[208:211], a[88:91], v[140:143]\n\t"
    "ds_read_b64_tr_b16 a[238:239], v9 offset:25248\n\t"
    "v_mfma_f32_16x16x32_bf16 v[140:143], a[212:215], a[92:95], v[140:143]\n\t"
    "s_waitcnt lgkmcnt(8)\n\t"
    "v_mfma_f32_16x16x32_bf16 a[28:31], a[216:219], a[88:91], a[28:31]\n\t"
    "ds_read_b64_tr_b16 a[192:193], v9 offset:192\n\t"
    "v_mfma_f32_16x16x32_bf16 a[28:31], a[220:223], a[92:95], a[28:31]\n\t"
    "v_mfma_f32_16x16x32_bf16 a[24:27], a[216:219], a[80:83], a[24:27]\n\t"
    "ds_read_b64_tr_b16 a[194:195], v9 offset:3776\n\t"
    "v_mfma_f32_16x16x32_bf16 a[24:27], a[220:223], a[84:87], a[24:27]\n\t"
    "buffer_load_dwordx4 a[188:191], v8, s[40:43], s66 offen offset:3072 sc1\n\t"
    "v_mfma_f32_16x16x32_bf16 a[20:23], a[216:219], a[72:75], a[20:23]\n\t"
    "ds_read_b64_tr_b16 a[196:197], v9 offset:7360\n\t"
    "v_mfma_f32_16x16x32_bf16 a[20:23], a[220:223], a[76:79], a[20:23]\n\t"
    "s_add_u32 m0, s68, 143360\n\t"
    "v_mfma_f32_16x16x32_bf16 a[16:19], a[216:219], a[64:67], a[16:19]\n\t"
    "ds_read_b64_tr_b16 a[198:199], v9 offset:10944\n\t"
    "v_mfma_f32_16x16x32_bf16 a[16:19], a[220:223], a[68:71], a[16:19]\n\t"
    "buffer_load_dwordx4 v6, s[60:63], s65 offen lds\n\t"
    "s_waitcnt lgkmcnt(8)\n\t"
    "v_mfma_f32_16x16x32_bf16 v[112:115], a[224:227], a[64:67], v[112:115]\n\t"
    "ds_read_b64_tr_b16 a[200:201], v9 offset:14528\n\t"
    "v_mfma_f32_16x16x32_bf16 v[112:115], a[228:231], a[68:71], v[112:115]\n\t"
    "v_mfma_f32_16x16x32_bf16 v[116:119], a[224:227], a[72:75], v[116:119]\n\t"
    "ds_read_b64_tr_b16 a[202:203], v9 offset:18112\n\t"
    "v_mfma_f32_16x16x32_bf16 v[116:119], a[228:231], a[76:79], v[116:119]\n\t"
    "buffer_load_dword v28, v15, s[20:23], s84 offen\n\t"
    "v_mfma_f32_16x16x32_bf16 v[120:123], a[224:227], a[80:83], v[120:123]\n\t"
    "ds_read_b64_tr_b16 a[204:205], v9 offset:21696\n\t"
    "v_mfma_f32_16x16x32_bf16 v[120:123], a[228:231], a[84:87], v[120:123]\n\t"
    "v_mfma_f32_16x16x32_bf16 v[124:127], a[224:227], a[88:91], v[124:127]\n\t"
    "ds_read_b64_tr_b16 a[206:207], v9 offset:25280\n\t"
    "v_mfma_f32_16x16x32_bf16 v[124:127], a[228:231], a[92:95], v[124:127]\n\t"
    "s_waitcnt lgkmcnt(8)\n\t"
    "v_mfma_f32_16x16x32_bf16 a[44:47], a[232:235], a[88:91], a[44:47]\n\t"
    "ds_read_b128 a[208:211], v11 offset:0\n\t"
    "v_mfma_f32_16x16x32_bf16 a[44:47], a[236:239], a[92:95], a[44:47]\n\t"
    "buffer_load_dword v28, v0, s[24:27], s86 offen\n\t"
    "v_mfma_f32_16x16x32_bf16 a[40:43], a[232:235], a[80:83], a[40:43]\n\t"
    "ds_read_b128 a[212:215], v12 offset:0\n\t"
    "v_mfma_f32_16x16x32_bf16 a[40:43], a[236:239], a[84:87], a[40:43]\n\t"
    "v_mfma_f32_16x16x32_bf16 a[36:39], a[232:235], a[72:75], a[36:39]\n\t"
    "v_mfma_f32_16x16x32_bf16 a[36:39], a[236:239], a[76:79], a[36:39]\n\t"
    "v_mfma_f32_16x16x32_bf16 a[32:35], a[232:235], a[64:67], a[32:35]\n\t"
    "v_mfma_f32_16x16x32_bf16 a[32:35], a[236:239], a[68:71], a[32:35]\n\t"
    "s_waitcnt lgkmcnt(6)\n\t"
    "v_mfma_f32_16x16x32_bf16 v[96:99], a[192:195], a[64:67], v[96:99]\n\t"
    "ds_read_b128 a[216:219], v11 offset:4096\n\t"
    "v_mfma_f32_16x16x32_bf16 v[96:99], a[196:199], a[68:71], v[96:99]\n\t"
    "v_mfma_f32_16x16x32_bf16 v[100:103], a[192:195], a[72:75], v[100:103]\n\t"
    "ds_read_b128 a[220:223], v12 offset:4096\n\t"
    "v_mfma_f32_16x16x32_bf16 v[100:103], a[196:199], a[76:79], v[100:103]\n\t"
    "v_mfma_f32_16x16x32_bf16 v[104:107], a[192:195], a[80:83], v[104:107]\n\t"
    "v_mfma_f32_16x16x32_bf16 v[104:107], a[196:199], a[84:87], v[104:107]\n\t"
    "v_mfma_f32_16x16x32_bf16 v[108:111], a[192:195], a[88:91], v[108:111]\n\t"
    "v_mfma_f32_16x16x32_bf16 v[108:111], a[196:199], a[92:95], v[108:111]\n\t"
    "s_waitcnt lgkmcnt(4)\n\t"
    "v_mfma_f32_16x16x32_bf16 a[60:63], a[200:203], a[88:91], a[60:63]\n\t"
    "ds_read_b128 a[224:227], v11 offset:2048\n\t"
    "v_mfma_f32_16x16x32_bf16 a[60:63], a[204:207], a[92:95], a[60:63]\n\t"
    "v_mfma_f32_16x16x32_bf16 a[56:59], a[200:203], a[80:83], a[56:59]\n\t"
    "ds_read_b128 a[228:231], v12 offset:2048\n\t"
    "v_mfma_f32_16x16x32_bf16 a[56:59], a[204:207], a[84:87], a[56:59]\n\t"
    "v_mfma_f32_16x16x32_bf16 a[52:55], a[200:203], a[72:75], a[52:55]\n\t"
    "v_mfma_f32_16x16x32_bf16 a[52:55], a[204:207], a[76:79], a[52:55]\n\t"
    "v_mfma_f32_16x16x32_bf16 a[48:51], a[200:203], a[64:67], a[48:51]\n\t"
    "v_mfma_f32_16x16x32_bf16 a[48:51], a[204:207], a[68:71], a[48:51]\n\t"
    "s_waitcnt lgkmcnt(4)\n\t"
    "v_mfma_f32_16x16x32_bf16 v[32:35], a[64:67], a[208:211], v[32:35]\n\t"
    "ds_read_b128 a[232:235], v11 offset:6144\n\t"
    "v_mfma_f32_16x16x32_bf16 v[32:35], a[68:71], a[212:215], v[32:35]\n\t"
    "v_mfma_f32_16x16x32_bf16 v[36:39], a[72:75], a[208:211], v[36:39]\n\t"
    "ds_read_b128 a[236:239], v12 offset:6144\n\t"
    "v_mfma_f32_16x16x32_bf16 v[36:39], a[76:79], a[212:215], v[36:39]\n\t"
    "v_mfma_f32_16x16x32_bf16 v[40:43], a[80:83], a[208:211], v[40:43]\n\t"
    "v_mfma_f32_16x16x32_bf16 v[40:43], a[84:87], a[212:215], v[40:43]\n\t"
    "v_mfma_f32_16x16x32_bf16 v[44:47], a[88:91], a[208:211], v[44:47]\n\t"
    "v_mfma_f32_16x16x32_bf16 v[44:47], a[92:95], a[212:215], v[44:47]\n\t"
    "s_waitcnt lgkmcnt(4)\n\t"
    "v_mfma_f32_16x16x32_bf16 v[76:79], a[88:91], a[216:219], v[76:79]\n\t"
    "v_mfma_f32_16x16x32_bf16 v[76:79], a[92:95], a[220:223], v[76:79]\n\t"
    "v_mfma_f32_16x16x32_bf16 v[72:75], a[80:83], a[216:219], v[72:75]\n\t"
    "v_mfma_f32_16x16x32_bf16 v[72:75], a[84:87], a[220:223], v[72:75]\n\t"
    "s_add_u32 s64, s64, 458752\n\t"
    "v_mfma_f32_16x16x32_bf16 v[68:71], a[72:75], a[216:219], v[68:71]\n\t"
    "s_add_u32 s65, s65, 128\n\t"
    "v_mfma_f32_16x16x32_bf16 v[68:71], a[76:79], a[220:223], v[68:71]\n\t"
    "s_add_u32 s66, s66, 262144\n\t"
    "v_mfma_f32_16x16x32_bf16 v[64:67], a[64:67], a[216:219], v[64:67]\n\t"
    "s_add_u32 s84, s84, s85\n\t"
    "v_mfma_f32_16x16x32_bf16 v[64:67], a[68:71], a[220:223], v[64:67]\n\t"
    "s_add_u32 s86, s86, s39\n\t"
    "s_waitcnt vmcnt(38) lgkmcnt(0)\n\t"
    "s_barrier\n\t"
    "v_mfma_f32_16x16x32_bf16 v[48:51], a[64:67], a[224:227], v[48:51]\n\t"
    "ds_read_b64_tr_b16 a[192:193], v9 offset:36864\n\t"
    "v_mfma_f32_16x16x32_bf16 v[48:51], a[68:71], a[228:231], v[48:51]\n\t"
    "ds_read_b64_tr_b16 a[194:195], v9 offset:40448\n\t"
    "v_mfma_f32_16x16x32_bf16 v[52:55], a[72:75], a[224:227], v[52:55]\n\t"
    "ds_read_b64_tr_b16 a[196:197], v9 offset:44032\n\t"
    "v_mfma_f32_16x16x32_bf16 v[52:55], a[76:79], a[228:231], v[52:55]\n\t"
    "ds_read_b64_tr_b16 a[198:199], v9 offset:47616\n\t"
    "v_mfma_f32_16x16x32_bf16 v[56:59], a[80:83], a[224:227], v[56:59]\n\t"
    "ds_read_b64_tr_b16 a[200:201], v9 offset:51200\n\t"
    "v_mfma_f32_16x16x32_bf16 v[56:59], a[84:87], a[228:231], v[56:59]\n\t"
    "ds_read_b64_tr_b16 a[202:203], v9 offset:54784\n\t"
    "v_mfma_f32_16x16x32_bf16 v[60:63], a[88:91], a[224:227], v[60:63]\n\t"
    "ds_read_b64_tr_b16 a[204:205], v9 offset:58368\n\t"
    "v_mfma_f32_16x16x32_bf16 v[60:63], a[92:95], a[228:231], v[60:63]\n\t"
    "ds_read_b64_tr_b16 a[206:207], v9 offset:61952\n\t"
    "v_mfma_f32_16x16x32_bf16 v[92:95], a[88:91], a[232:235], v[92:95]\n\t"
    "ds_read_b64_tr_b16 a[208:209], v9 offset:36896\n\t"
    "v_mfma_f32_16x16x32_bf16 v[92:95], a[92:95], a[236:239], v[92:95]\n\t"
    "ds_read_b64_tr_b16 a[210:211], v9 offset:40480\n\t"
    "v_mfma_f32_16x16x32_bf16 v[88:91], a[80:83], a[232:235], v[88:91]\n\t"
    "ds_read_b64_tr_b16 a[212:213], v9 offset:44064\n\t"
    "v_mfma_f32_16x16x32_bf16 v[88:91], a[84:87], a[236:239], v[88:91]\n\t"
    "ds_read_b64_tr_b16 a[214:215], v9 offset:47648\n\t"
    "v_mfma_f32_16x16x32_bf16 v[84:87], a[72:75], a[232:235], v[84:87]\n\t"
    "v_mfma_f32_16x16x32_bf16 v[84:87], a[76:79], a[236:239], v[84:87]\n\t"
    "v_mfma_f32_16x16x32_bf16 v[80:83], a[64:67], a[232:235], v[80:83]\n\t"
    "v_mfma_f32_16x16x32_bf16 v[80:83], a[68:71], a[236:239], v[80:83]\n\t"
    "L_g1p1_%=:\n\t"
    "s_waitcnt lgkmcnt(8)\n\t"
    "v_mfma_f32_16x16x32_bf16 v[192:195], a[192:195], a[96:99], v[192:195]\n\t"
    "ds_read_b64_tr_b16 a[216:217], v9 offset:51232\n\t"
    "v_mfma_f32_16x16x32_bf16 v[192:195], a[196:199], a[100:103], v[192:195]\n\t"
    "s_add_u32 m0, s68, 0\n\t"
    "v_mfma_f32_16x16x32_bf16 v[196:199], a[192:195], a[104:107], v[196:199]\n\t"
    "ds_read_b64_tr_b16 a[218:219], v9 offset:54816\n\t"
    "v_mfma_f32_16x16x32_bf16 v[196:199], a[196:199], a[108:111], v[196:199]\n\t"
    "buffer_load_dwordx4 v2, s[44:47], s64 offen lds\n\t"
    "v_mfma_f32_16x16x32_bf16 v[200:203], a[192:195], a[112:115], v[200:203]\n\t"
    "ds_read_b64_tr_b16 a[220:221], v9 offset:58400\n\t"
    "v_mfma_f32_16x16x32_bf16 v[200:203], a[196:199], a[116:119], v[200:203]\n\t"
    "v_mfma_f32_16x16x32_bf16 v[204:207], a[192:195], a[120:123], v[204:207]\n\t"
    "ds_read_b64_tr_b16 a[222:223], v9 offset:61984\n\t"
    "v_mfma_f32_16x16x32_bf16 v[204:207], a[196:199], a[124:127], v[204:207]\n\t"
    "buffer_load_dwordx4 a[64:67], v7, s[40:43], s66 offen offset:0 sc1\n\t"
    "s_waitcnt lgkmcnt(8)\n\t"
    "v_mfma_f32_16x16x32_bf16 v[220:223], a[200:203], a[120:123], v[220:223]\n\t"
    "ds_read_b64_tr_b16 a[224:225], v9 offset:36928\n\t"
    "v_mfma_f32_16x16x32_bf16 v[220:223], a[204:207], a[124:127], v[220:223]\n\t"
    "v_mfma_f32_16x16x32_bf16 v[216:219], a[200:203], a[112:115], v[216:219]\n\t"
    "ds_read_b64_tr_b16 a[226:227], v9 offset:40512\n\t"
    "v_mfma_f32_16x16x32_bf16 v[216:219], a[204:207], a[116:119], v[216:219]\n\t"
    "s_add_u32 m0, s68, 14336\n\t"
    "v_mfma_f32_16x16x32_bf16 v[212:215], a[200:203], a[104:107], v[212:215]\n\t"
    "ds_read_b64_tr_b16 a[228:229], v9 offset:44096\n\t"
    "v_mfma_f32_16x16x32_bf16 v[212:215], a[204:207], a[108:111], v[212:215]\n\t"
    "buffer_load_dwordx4 v2, s[48:51], s64 offen lds\n\t"
    "v_mfma_f32_16x16x32_bf16 v[208:211], a[200:203], a[96:99], v[208:211]\n\t"
    "ds_read_b64_tr_b16 a[230:231], v9 offset:47680\n\t"
    "v_mfma_f32_16x16x32_bf16 v[208:211], a[204:207], a[100:103], v[208:211]\n\t"
    "s_waitcnt lgkmcnt(8)\n\t"
    "v_mfma_f32_16x16x32_bf16 v[176:179], a[208:211], a[96:99], v[176:179]\n\t"
    "ds_read_b64_tr_b16 a[232:233], v9 offset:51264\n\t"
    "v_mfma_f32_16x16x32_bf16 v[176:179], a[212:215], a[100:103], v[176:179]\n\t"
    "buffer_load_dwordx4 a[68:71], v8, s[40:43], s66 offen offset:0 sc1\n\t"
    "v_mfma_f32_16x16x32_bf16 v[180:183], a[208:211], a[104:107], v[180:183]\n\t"
    "ds_read_b64_tr_b16 a[234:235], v9 offset:54848\n\t"
    "v_mfma_f32_16x16x32_bf16 v[180:183], a[212:215], a[108:111], v[180:183]\n\t"
    "s_add_u32 m0, s68, 4096\n\t"
    "v_mfma_f32_16x16x32_bf16 v[184:187], a[208:211], a[112:115], v[184:187]\n\t"
    "ds_read_b64_tr_b16 a[236:237], v9 offset:58432\n\t"
    "v_mfma_f32_16x16x32_bf16 v[184:187], a[212:215], a[116:119], v[184:187]\n\t"
    "buffer_load_dwordx4 v3, s[44:47], s64 offen lds\n\t"
    "v_mfma_f32_16x16x32_bf16 v[188:191], a[208:211], a[120:123], v[188:191]\n\t"
    "ds_read_b64_tr_b16 a[238:239], v9 offset:62016\n\t"
    "v_mfma_f32_16x16x32_bf16 v[188:191], a[212:215], a[124:127], v[188:191]\n\t"
    "s_waitcnt lgkmcnt(8)\n\t"
    "v_mfma_f32_16x16x32_bf16 v[236:239], a[216:219], a[120:123], v[236:239]\n\t"
    "ds_read_b64_tr_b16 a[192:193], v9 offset:36960\n\t"
    "v_mfma_f32_16x16x32_bf16 v[236:239], a[220:223], a[124:127], v[236:239]\n\t"
    "v_mfma_f32_16x16x32_bf16 v[232:235], a[216:219], a[112:115], v[232:235]\n\t"
    "ds_read_b64_tr_b16 a[194:195], v9 offset:40544\n\t"
    "v_mfma_f32_16x16x32_bf16 v[232:235], a[220:223], a[116:119], v[232:235]\n\t"
    "buffer_load_dwordx4 a[72:75], v7, s[40:43], s66 offen offset:1024 sc1\n\t"
    "v_mfma_f32_16x16x32_bf16 v[228:231], a[216:219], a[104:107], v[228:231]\n\t"
    "ds_read_b64_tr_b16 a[196:197], v9 offset:44128\n\t"
    "v_mfma_f32_16x16x32_bf16 v[228:231], a[220:223], a[108:111], v[228:231]\n\t"
    "s_add_u32 m0, s68, 18432\n\t"
    "v_mfma_f32_16x16x32_bf16 v[224:227], a[216:219], a[96:99], v[224:227]\n\t"
    "ds_read_b64_tr_b16 a[198:199], v9 offset:47712\n\t"
    "v_mfma_f32_16x16x32_bf16 v[224:227], a[220:223], a[100:103], v[224:227]\n\t"
    "buffer_load_dwordx4 v3, s[48:51], s64 offen lds\n\t"
    "s_waitcnt lgkmcnt(8)\n\t"
    "v_mfma_f32_16x16x32_bf16 v[160:163], a[224:227], a[96:99], v[160:163]\n\t"
    "ds_read_b64_tr_b16 a[200:201], v9 offset:51296\n\t"
    "v_mfma_f32_16x16x32_bf16 v[160:163], a[228:231], a[100:103], v[160:163]\n\t"
    "v_mfma_f32_16x16x32_bf16 v[164:167], a[224:227], a[104:107], v[164:167]\n\t"
    "ds_read_b64_tr_b16 a[202:203], v9 offset:54880\n\t"
    "v_mfma_f32_16x16x32_bf16 v[164:167], a[228:231], a[108:111], v[164:167]\n\t"
    "v_mfma_f32_16x16x32_bf16 v[168:171], a[224:227], a[112:115], v[168:171]\n\t"
    "ds_read_b64_tr_b16 a[204:205], v9 offset:58464\n\t"
    "v_mfma_f32_16x16x32_bf16 v[168:171], a[228:231], a[116:119], v[168:171]\n\t"
    "buffer_load_dwordx4 a[76:79], v8, s[40:43], s66 offen offset:1024 sc1\n\t"
    "v_mfma_f32_16x16x32_bf16 v[172:175], a[224:227], a[120:123], v[172:175]\n\t"
    "ds_read_b64_tr_b16 a[206:207], v9 offset:62048\n\t"
    "v_mfma_f32_16x16x32_bf16 v[172:175], a[228:231], a[124:127], v[172:175]\n\t"
    "s_add_u32 m0, s68, 8192\n\t"
    "s_waitcnt lgkmcnt(8)\n\t"
    "v_mfma_f32_16x16x32_bf16 v[252:255], a[232:235], a[120:123], v[252:255]\n\t"
    "ds_read_b64_tr_b16 a[208:209], v9 offset:36992\n\t"
    "v_mfma_f32_16x16x32_bf16 v[252:255], a[236:239], a[124:127], v[252:255]\n\t"
    "buffer_load_dwordx4 v4, s[44:47], s64 offen lds\n\t"
    "v_mfma_f32_16x16x32_bf16 v[248:251], a[232:235], a[112:115], v[248:251]\n\t"
    "ds_read_b64_tr_b16 a[210:211], v9 offset:40576\n\t"
    "v_mfma_f32_16x16x32_bf16 v[248:251], a[236:239], a[116:119], v[248:251]\n\t"
    "v_mfma_f32_16x16x32_bf16 v[244:247], a[232:235], a[104:107], v[244:247]\n\t"
    "ds_read_b64_tr_b16 a[212:213], v9 offset:44160\n\t"
    "v_mfma_f32_16x16x32_bf16 v[244:247], a[236:239], a[108:111], v[244:247]\n\t"
    "buffer_load_dwordx4 a[80:83], v7, s[40:43], s66 offen offset:2048 sc1\n\t"
    "v_mfma_f32_16x16x32_bf16 v[240:243], a[232:235], a[96:99], v[240:243]\n\t"
    "ds_read_b64_tr_b16 a[214:215], v9 offset:47744\n\t"
    "v_mfma_f32_16x16x32_bf16 v[240:243], a[236:239], a[100:103], v[240:243]\n\t"
    "s_waitcnt lgkmcnt(8)\n\t"
    "v_mfma_f32_16x16x32_bf16 v[144:147], a[192:195], a[96:99], v[144:147]\n\t"
    "ds_read_b64_tr_b16 a[216:217], v9 offset:51328\n\t"
    "v_mfma_f32_16x16x32_bf16 v[144:147], a[196:199], a[100:103], v[144:147]\n\t"
    "s_add_u32 m0, s68, 22528\n\t"
    "v_mfma_f32_16x16x32_bf16 v[148:151], a[192:195], a[104:107], v[148:151]\n\t"
    "ds_read_b64_tr_b16 a[218:219], v9 offset:54912\n\t"
    "v_mfma_f32_16x16x32_bf16 v[148:151], a[196:199], a[108:111], v[148:151]\n\t"
    "buffer_load_dwordx4 v4, s[48:51], s64 offen lds\n\t"
    "v_mfma_f32_16x16x32_bf16 v[152:155], a[192:195], a[112:115], v[152:155]\n\t"
    "ds_read_b64_tr_b16 a[220:221], v9 offset:58496\n\t"
    "v_mfma_f32_16x16x32_bf16 v[152:155], a[196:199], a[116:119], v[152:155]\n\t"
    "v_mfma_f32_16x16x32_bf16 v[156:159], a[192:195], a[120:123], v[156:159]\n\t"
    "ds_read_b64_tr_b16 a[222:223], v9 offset:62080\n\t"
    "v_mfma_f32_16x16x32_bf16 v[156:159], a[196:199], a[124:127], v[156:159]\n\t"
    "buffer_load_dwordx4 a[84:87], v8, s[40:43], s66 offen offset:2048 sc1\n\t"
    "s_waitcnt lgkmcnt(8)\n\t"
    "v_mfma_f32_16x16x32_bf16 a[12:15], a[200:203], a[120:123], a[12:15]\n\t"
    "ds_read_b64_tr_b16 a[224:225], v9 offset:37024\n\t"
    "v_mfma_f32_16x16x32_bf16 a[12:15], a[204:207], a[124:127], a[12:15]\n\t"
    "s_add_u32 m0, s69, 0\n\t"
    "v_mfma_f32_16x16x32_bf16 a[8:11], a[200:203], a[112:115], a[8:11]\n\t"
    "ds_read_b64_tr_b16 a[226:227], v9 offset:40608\n\t"
    "v_mfma_f32_16x16x32_bf16 a[8:11], a[204:207], a[116:119], a[8:11]\n\t"
    "buffer_load_dwordx4 v5, s[52:55], s64 offen lds\n\t"
    "v_mfma_f32_16x16x32_bf16 a[4:7], a[200:203], a[104:107], a[4:7]\n\t"
    "ds_read_b64_tr_b16 a[228:229], v9 offset:44192\n\t"
    "v_mfma_f32_16x16x32_bf16 a[4:7], a[204:207], a[108:111], a[4:7]\n\t"
    "v_mfma_f32_16x16x32_bf16 a[0:3], a[200:203], a[96:99], a[0:3]\n\t"
    "ds_read_b64_tr_b16 a[230:231], v9 offset:47776\n\t"
    "v_mfma_f32_16x16x32_bf16 a[0:3], a[204:207], a[100:103], a[0:3]\n\t"
    "s_waitcnt lgkmcnt(8)\n\t"
    "v_mfma_f32_16x16x32_bf16 v[128:131], a[208:211], a[96:99], v[128:131]\n\t"
    "ds_read_b64_tr_b16 a[232:233], v9 offset:51360\n\t"
    "v_mfma_f32_16x16x32_bf16 v[128:131], a[212:215], a[100:103], v[128:131]\n\t"
    "buffer_load_dwordx4 a[88:91], v7, s[40:43], s66 offen offset:3072 sc1\n\t"
    "v_mfma_f32_16x16x32_bf16 v[132:135], a[208:211], a[104:107], v[132:135]\n\t"
    "ds_read_b64_tr_b16 a[234:235], v9 offset:54944\n\t"
    "v_mfma_f32_16x16x32_bf16 v[132:135], a[212:215], a[108:111], v[132:135]\n\t"
    "s_add_u32 m0, s68, 28672\n\t"
    "v_mfma_f32_16x16x32_bf16 v[136:139], a[208:211], a[112:115], v[136:139]\n\t"
    "ds_read_b64_tr_b16 a[236:237], v9 offset:58528\n\t"
    "v_mfma_f32_16x16x32_bf16 v[136:139], a[212:215], a[116:119], v[136:139]\n\t"
    "buffer_load_dwordx4 v6, s[56:59], s65 offen lds\n\t"
    "v_mfma_f32_16x16x32_bf16 v[140:143], a[208:211], a[120:123], v[140:143]\n\t"
    "ds_read_b64_tr_b16 a[238:239], v9 offset:62112\n\t"
    "v_mfma_f32_16x16x32_bf16 v[140:143], a[212:215], a[124:127], v[140:143]\n\t"
    "s_waitcnt lgkmcnt(8)\n\t"
    "v_mfma_f32_16x16x32_bf16 a[28:31], a[216:219], a[120:123], a[28:31]\n\t"
    "ds_read_b64_tr_b16 a[192:193], v9 offset:37056\n\t"
    "v_mfma_f32_16x16x32_bf16 a[28:31], a[220:223], a[124:127], a[28:31]\n\t"
    "v_mfma_f32_16x16x32_bf16 a[24:27], a[216:219], a[112:115], a[24:27]\n\t"
    "ds_read_b64_tr_b16 a[194:195], v9 offset:40640\n\t"
    "v_mfma_f32_16x16x32_bf16 a[24:27], a[220:223], a[116:119], a[24:27]\n\t"
    "buffer_load_dwordx4 a[92:95], v8, s[40:43], s66 offen offset:3072 sc1\n\t"
    "v_mfma_f32_16x16x32_bf16 a[20:23], a[216:219], a[104:107], a[20:23]\n\t"
    "ds_read_b64_tr_b16 a[196:197], v9 offset:44224\n\t"
    "v_mfma_f32_16x16x32_bf16 a[20:23], a[220:223], a[108:111], a[20:23]\n\t"
    "s_add_u32 m0, s68, 32768\n\t"
    "v_mfma_f32_16x16x32_bf16 a[16:19], a[216:219], a[96:99], a[16:19]\n\t"
    "ds_read_b64_tr_b16 a[198:199], v9 offset:47808\n\t"
    "v_mfma_f32_16x16x32_bf16 a[16:19], a[220:223], a[100:103], a[16:19]\n\t"
    "buffer_load_dwordx4 v6, s[60:63], s65 offen lds\n\t"
    "s_waitcnt lgkmcnt(8)\n\t"
    "v_mfma_f32_16x16x32_bf16 v[112:115], a[224:227], a[96:99], v[112:115]\n\t"
    "ds_read_b64_tr_b16 a[200:201], v9 offset:51392\n\t"
    "v_mfma_f32_16x16x32_bf16 v[112:115], a[228:231], a[100:103], v[112:115]\n\t"
    "v_mfma_f32_16x16x32_bf16 v[116:119], a[224:227], a[104:107], v[116:119]\n\t"
    "ds_read_b64_tr_b16 a[202:203], v9 offset:54976\n\t"
    "v_mfma_f32_16x16x32_bf16 v[116:119], a[228:231], a[108:111], v[116:119]\n\t"
    "buffer_load_dword v28, v30, s[20:23], s84 offen\n\t"
    "v_mfma_f32_16x16x32_bf16 v[120:123], a[224:227], a[112:115], v[120:123]\n\t"
    "ds_read_b64_tr_b16 a[204:205], v9 offset:58560\n\t"
    "v_mfma_f32_16x16x32_bf16 v[120:123], a[228:231], a[116:119], v[120:123]\n\t"
    "v_mfma_f32_16x16x32_bf16 v[124:127], a[224:227], a[120:123], v[124:127]\n\t"
    "ds_read_b64_tr_b16 a[206:207], v9 offset:62144\n\t"
    "v_mfma_f32_16x16x32_bf16 v[124:127], a[228:231], a[124:127], v[124:127]\n\t"
    "s_waitcnt lgkmcnt(8)\n\t"
    "v_mfma_f32_16x16x32_bf16 a[44:47], a[232:235], a[120:123], a[44:47]\n\t"
    "ds_read_b128 a[208:211], v11 offset:36864\n\t"
    "v_mfma_f32_16x16x32_bf16 a[44:47], a[236:239], a[124:127], a[44:47]\n\t"
    "buffer_load_dword v28, v0, s[24:27], s86 offen\n\t"
    "v_mfma_f32_16x16x32_bf16 a[40:43], a[232:235], a[112:115], a[40:43]\n\t"
    "ds_read_b128 a[212:215], v12 offset:36864\n\t"
    "v_mfma_f32_16x16x32_bf16 a[40:43], a[236:239], a[116:119], a[40:43]\n\t"
    "v_mfma_f32_16x16x32_bf16 a[36:39], a[232:235], a[104:107], a[36:39]\n\t"
    "v_mfma_f32_16x16x32_bf16 a[36:39], a[236:239], a[108:111], a[36:39]\n\t"
    "v_mfma_f32_16x16x32_bf16 a[32:35], a[232:235], a[96:99], a[32:35]\n\t"
    "v_mfma_f32_16x16x32_bf16 a[32:35], a[236:239], a[100:103], a[32:35]\n\t"
    "s_waitcnt lgkmcnt(6)\n\t"
    "v_mfma_f32_16x16x32_bf16 v[96:99], a[192:195], a[96:99], v[96:99]\n\t"
    "ds_read_b128 a[216:219], v11 offset:40960\n\t"
    "v_mfma_f32_16x16x32_bf16 v[96:99], a[196:199], a[100:103], v[96:99]\n\t"
    "v_mfma_f32_16x16x32_bf16 v[100:103], a[192:195], a[104:107], v[100:103]\n\t"
    "ds_read_b128 a[220:223], v12 offset:40960\n\t"
    "v_mfma_f32_16x16x32_bf16 v[100:103], a[196:199], a[108:111], v[100:103]\n\t"
    "v_mfma_f32_16x16x32_bf16 v[104:107], a[192:195], a[112:115], v[104:107]\n\t"
    "v_mfma_f32_16x16x32_bf16 v[104:107], a[196:199], a[116:119], v[104:107]\n\t"
    "v_mfma_f32_16x16x32_bf16 v[108:111], a[192:195], a[120:123], v[108:111]\n\t"
    "v_mfma_f32_16x16x32_bf16 v[108:111], a[196:199], a[124:127], v[108:111]\n\t"
    "s_waitcnt lgkmcnt(4)\n\t"
    "v_mfma_f32_16x16x32_bf16 a[60:63], a[200:203], a[120:123], a[60:63]\n\t"
    "ds_read_b128 a[224:227], v11 offset:38912\n\t"
    "v_mfma_f32_16x16x32_bf16 a[60:63], a[204:207], a[124:127], a[60:63]\n\t"
    "v_mfma_f32_16x16x32_bf16 a[56:59], a[200:203], a[112:115], a[56:59]\n\t"
    "ds_read_b128 a[228:231], v12 offset:38912\n\t"
    "v_mfma_f32_16x16x32_bf16 a[56:59], a[204:207], a[116:119], a[56:59]\n\t"
    "v_mfma_f32_16x16x32_bf16 a[52:55], a[200:203], a[104:107], a[52:55]\n\t"
    "v_mfma_f32_16x16x32_bf16 a[52:55], a[204:207], a[108:111], a[52:55]\n\t"
    "v_mfma_f32_16x16x32_bf16 a[48:51], a[200:203], a[96:99], a[48:51]\n\t"
    "v_mfma_f32_16x16x32_bf16 a[48:51], a[204:207], a[100:103], a[48:51]\n\t"
    "s_waitcnt lgkmcnt(4)\n\t"
    "v_mfma_f32_16x16x32_bf16 v[32:35], a[96:99], a[208:211], v[32:35]\n\t"
    "ds_read_b128 a[232:235], v11 offset:43008\n\t"
    "v_mfma_f32_16x16x32_bf16 v[32:35], a[100:103], a[212:215], v[32:35]\n\t"
    "v_mfma_f32_16x16x32_bf16 v[36:39], a[104:107], a[208:211], v[36:39]\n\t"
    "ds_read_b128 a[236:239], v12 offset:43008\n\t"
    "v_mfma_f32_16x16x32_bf16 v[36:39], a[108:111], a[212:215], v[36:39]\n\t"
    "v_mfma_f32_16x16x32_bf16 v[40:43], a[112:115], a[208:211], v[40:43]\n\t"
    "v_mfma_f32_16x16x32_bf16 v[40:43], a[116:119], a[212:215], v[40:43]\n\t"
    "v_mfma_f32_16x16x32_bf16 v[44:47], a[120:123], a[208:211], v[44:47]\n\t"
    "v_mfma_f32_16x16x32_bf16 v[44:47], a[124:127], a[212:215], v[44:47]\n\t"
    "s_waitcnt lgkmcnt(4)\n\t"
    "v_mfma_f32_16x16x32_bf16 v[76:79], a[120:123], a[216:219], v[76:79]\n\t"
    "v_mfma_f32_16x16x32_bf16 v[76:79], a[124:127], a[220:223], v[76:79]\n\t"
    "v_mfma_f32_16x16x32_bf16 v[72:75], a[112:115], a[216:219], v[72:75]\n\t"
    "v_mfma_f32_16x16x32_bf16 v[72:75], a[116:119], a[220:223], v[72:75]\n\t"
    "s_add_u32 s64, s64, 458752\n\t"
    "v_mfma_f32_16x16x32_bf16 v[68:71], a[104:107], a[216:219], v[68:71]\n\t"
    "s_add_u32 s65, s65, 128\n\t"
    "v_mfma_f32_16x16x32_bf16 v[68:71], a[108:111], a[220:223], v[68:71]\n\t"
    "s_add_u32 s66, s66, 262144\n\t"
    "v_mfma_f32_16x16x32_bf16 v[64:67], a[96:99], a[216:219], v[64:67]\n\t"
    "s_add_u32 s84, s84, s85\n\t"
    "v_mfma_f32_16x16x32_bf16 v[64:67], a[100:103], a[220:223], v[64:67]\n\t"
    "s_add_u32 s86, s86, s39\n\t"
    "s_waitcnt vmcnt(38) lgkmcnt(0)\n\t"
    "s_barrier\n\t"
    "v_mfma_f32_16x16x32_bf16 v[48:51], a[96:99], a[224:227], v[48:51]\n\t"
    "ds_read_b64_tr_b16 a[192:193], v10 offset:0\n\t"
    "v_mfma_f32_16x16x32_bf16 v[48:51], a[100:103], a[228:231], v[48:51]\n\t"
    "ds_read_b64_tr_b16 a[194:195], v10 offset:3584\n\t"
    "v_mfma_f32_16x16x32_bf16 v[52:55], a[104:107], a[224:227], v[52:55]\n\t"
    "ds_read_b64_tr_b16 a[196:197], v10 offset:7168\n\t"
    "v_mfma_f32_16x16x32_bf16 v[52:55], a[108:111], a[228:231], v[52:55]\n\t"
    "ds_read_b64_tr_b16 a[198:199], v10 offset:10752\n\t"
    "v_mfma_f32_16x16x32_bf16 v[56:59], a[112:115], a[224:227], v[56:59]\n\t"
    "ds_read_b64_tr_b16 a[200:201], v10 offset:14336\n\t"
    "v_mfma_f32_16x16x32_bf16 v[56:59], a[116:119], a[228:231], v[56:59]\n\t"
    "ds_read_b64_tr_b16 a[202:203], v10 offset:17920\n\t"
    "v_mfma_f32_16x16x32_bf16 v[60:63], a[120:123], a[224:227], v[60:63]\n\t"
    "ds_read_b64_tr_b16 a[204:205], v10 offset:21504\n\t"
    "v_mfma_f32_16x16x32_bf16 v[60:63], a[124:127], a[228:231], v[60:63]\n\t"
    "ds_read_b64_tr_b16 a[206:207], v10 offset:25088\n\t"
    "v_mfma_f32_16x16x32_bf16 v[92:95], a[120:123], a[232:235], v[92:95]\n\t"
    "ds_read_b64_tr_b16 a[208:209], v10 offset:32\n\t"
    "v_mfma_f32_16x16x32_bf16 v[92:95], a[124:127], a[236:239], v[92:95]\n\t"
    "ds_read_b64_tr_b16 a[210:211], v10 offset:3616\n\t"
    "v_mfma_f32_16x16x32_bf16 v[88:91], a[112:115], a[232:235], v[88:91]\n\t"
    "ds_read_b64_tr_b16 a[212:213], v10 offset:7200\n\t"
    "v_mfma_f32_16x16x32_bf16 v[88:91], a[116:119], a[236:239], v[88:91]\n\t"
    "ds_read_b64_tr_b16 a[214:215], v10 offset:10784\n\t"
    "v_mfma_f32_16x16x32_bf16 v[84:87], a[104:107], a[232:235], v[84:87]\n\t"
    "v_mfma_f32_16x16x32_bf16 v[84:87], a[108:111], a[236:239], v[84:87]\n\t"
    "v_mfma_f32_16x16x32_bf16 v[80:83], a[96:99], a[232:235], v[80:83]\n\t"
    "v_mfma_f32_16x16x32_bf16 v[80:83], a[100:103], a[236:239], v[80:83]\n\t"
    "s_waitcnt lgkmcnt(8)\n\t"
    "v_mfma_f32_16x16x32_bf16 v[192:195], a[192:195], a[128:131], v[192:195]\n\t"
    "ds_read_b64_tr_b16 a[216:217], v10 offset:14368\n\t"
    "v_mfma_f32_16x16x32_bf16 v[192:195], a[196:199], a[132:135], v[192:195]\n\t"
    "s_add_u32 m0, s68, 36864\n\t"
    "v_mfma_f32_16x16x32_bf16 v[196:199], a[192:195], a[136:139], v[196:199]\n\t"
    "ds_read_b64_tr_b16 a[218:219], v10 offset:17952\n\t"
    "v_mfma_f32_16x16x32_bf16 v[196:199], a[196:199], a[140:143], v[196:199]\n\t"
    "buffer_load_dwordx4 v2, s[44:47], s64 offen lds\n\t"
    "v_mfma_f32_16x16x32_bf16 v[200:203], a[192:195], a[144:147], v[200:203]\n\t"
    "ds_read_b64_tr_b16 a[220:221], v10 offset:21536\n\t"
    "v_mfma_f32_16x16x32_bf16 v[200:203], a[196:199], a[148:151], v[200:203]\n\t"
    "v_mfma_f32_16x16x32_bf16 v[204:207], a[192:195], a[152:155], v[204:207]\n\t"
    "ds_read_b64_tr_b16 a[222:223], v10 offset:25120\n\t"
    "v_mfma_f32_16x16x32_bf16 v[204:207], a[196:199], a[156:159], v[204:207]\n\t"
    "buffer_load_dwordx4 a[96:99], v7, s[40:43], s66 offen offset:0 sc1\n\t"
    "s_waitcnt lgkmcnt(8)\n\t"
    "v_mfma_f32_16x16x32_bf16 v[220:223], a[200:203], a[152:155], v[220:223]\n\t"
    "ds_read_b64_tr_b16 a[224:225], v10 offset:64\n\t"
    "v_mfma_f32_16x16x32_bf16 v[220:223], a[204:207], a[156:159], v[220:223]\n\t"
    "v_mfma_f32_16x16x32_bf16 v[216:219], a[200:203], a[144:147], v[216:219]\n\t"
    "ds_read_b64_tr_b16 a[226:227], v10 offset:3648\n\t"
    "v_mfma_f32_16x16x32_bf16 v[216:219], a[204:207], a[148:151], v[216:219]\n\t"
    "s_add_u32 m0, s68, 51200\n\t"
    "v_mfma_f32_16x16x32_bf16 v[212:215], a[200:203], a[136:139], v[212:215]\n\t"
    "ds_read_b64_tr_b16 a[228:229], v10 offset:7232\n\t"
    "v_mfma_f32_16x16x32_bf16 v[212:215], a[204:207], a[140:143], v[212:215]\n\t"
    "buffer_load_dwordx4 v2, s[48:51], s64 offen lds\n\t"
    "v_mfma_f32_16x16x32_bf16 v[208:211], a[200:203], a[128:131], v[208:211]\n\t"
    "ds_read_b64_tr_b16 a[230:231], v10 offset:10816\n\t"
    "v_mfma_f32_16x16x32_bf16 v[208:211], a[204:207], a[132:135], v[208:211]\n\t"
    "s_waitcnt lgkmcnt(8)\n\t"
    "v_mfma_f32_16x16x32_bf16 v[176:179], a[208:211], a[128:131], v[176:179]\n\t"
    "ds_read_b64_tr_b16 a[232:233], v10 offset:14400\n\t"
    "v_mfma_f32_16x16x32_bf16 v[176:179], a[212:215], a[132:135], v[176:179]\n\t"
    "buffer_load_dwordx4 a[100:103], v8, s[40:43], s66 offen offset:0 sc1\n\t"
    "v_mfma_f32_16x16x32_bf16 v[180:183], a[208:211], a[136:139], v[180:183]\n\t"
    "ds_read_b64_tr_b16 a[234:235], v10 offset:17984\n\t"
    "v_mfma_f32_16x16x32_bf16 v[180:183], a[212:215], a[140:143], v[180:183]\n\t"
    "s_add_u32 m0, s68, 40960\n\t"
    "v_mfma_f32_16x16x32_bf16 v[184:187], a[208:211], a[144:147], v[184:187]\n\t"
    "ds_read_b64_tr_b16 a[236:237], v10 offset:21568\n\t"
    "v_mfma_f32_16x16x32_bf16 v[184:187], a[212:215], a[148:151], v[184:187]\n\t"
    "buffer_load_dwordx4 v3, s[44:47], s64 offen lds\n\t"
    "v_mfma_f32_16x16x32_bf16 v[188:191], a[208:211], a[152:155], v[188:191]\n\t"
    "ds_read_b64_tr_b16 a[238:239], v10 offset:25152\n\t"
    "v_mfma_f32_16x16x32_bf16 v[188:191], a[212:215], a[156:159], v[188:191]\n\t"
    "s_waitcnt lgkmcnt(8)\n\t"
    "v_mfma_f32_16x16x32_bf16 v[236:239], a[216:219], a[152:155], v[236:239]\n\t"
    "ds_read_b64_tr_b16 a[192:193], v10 offset:96\n\t"
    "v_mfma_f32_16x16x32_bf16 v[236:239], a[220:223], a[156:159], v[236:239]\n\t"
    "v_mfma_f32_16x16x32_bf16 v[232:235], a[216:219], a[144:147], v[232:235]\n\t"
    "ds_read_b64_tr_b16 a[194:195], v10 offset:3680\n\t"
    "v_mfma_f32_16x16x32_bf16 v[232:235], a[220:223], a[148:151], v[232:235]\n\t"
    "buffer_load_dwordx4 a[104:107], v7, s[40:43], s66 offen offset:1024 sc1\n\t"
    "v_mfma_f32_16x16x32_bf16 v[228:231], a[216:219], a[136:139], v[228:231]\n\t"
    "ds_read_b64_tr_b16 a[196:197], v10 offset:7264\n\t"
    "v_mfma_f32_16x16x32_bf16 v[228:231], a[220:223], a[140:143], v[228:231]\n\t"
    "s_add_u32 m0, s68, 55296\n\t"
    "v_mfma_f32_16x16x32_bf16 v[224:227], a[216:219], a[128:131], v[224:227]\n\t"
    "ds_read_b64_tr_b16 a[198:199], v10 offset:10848\n\t"
    "v_mfma_f32_16x16x32_bf16 v[224:227], a[220:223], a[132:135], v[224:227]\n\t"
    "buffer_load_dwordx4 v3, s[48:51], s64 offen lds\n\t"
    "s_waitcnt lgkmcnt(8)\n\t"
    "v_mfma_f32_16x16x32_bf16 v[160:163], a[224:227], a[128:131], v[160:163]\n\t"
    "ds_read_b64_tr_b16 a[200:201], v10 offset:14432\n\t"
    "v_mfma_f32_16x16x32_bf16 v[160:163], a[228:231], a[132:135], v[160:163]\n\t"
    "v_mfma_f32_16x16x32_bf16 v[164:167], a[224:227], a[136:139], v[164:167]\n\t"
    "ds_read_b64_tr_b16 a[202:203], v10 offset:18016\n\t"
    "v_mfma_f32_16x16x32_bf16 v[164:167], a[228:231], a[140:143], v[164:167]\n\t"
    "v_mfma_f32_16x16x32_bf16 v[168:171], a[224:227], a[144:147], v[168:171]\n\t"
    "ds_read_b64_tr_b16 a[204:205], v10 offset:21600\n\t"
    "v_mfma_f32_16x16x32_bf16 v[168:171], a[228:231], a[148:151], v[168:171]\n\t"
    "buffer_load_dwordx4 a[108:111], v8, s[40:43], s66 offen offset:1024 sc1\n\t"
    "v_mfma_f32_16x16x32_bf16 v[172:175], a[224:227], a[152:155], v[172:175]\n\t"
    "ds_read_b64_tr_b16 a[206:207], v10 offset:25184\n\t"
    "v_mfma_f32_16x16x32_bf16 v[172:175], a[228:231], a[156:159], v[172:175]\n\t"
    "s_add_u32 m0, s68, 45056\n\t"
    "s_waitcnt lgkmcnt(8)\n\t"
    "v_mfma_f32_16x16x32_bf16 v[252:255], a[232:235], a[152:155], v[252:255]\n\t"
    "ds_read_b64_tr_b16 a[208:209], v10 offset:128\n\t"
    "v_mfma_f32_16x16x32_bf16 v[252:255], a[236:239], a[156:159], v[252:255]\n\t"
    "buffer_load_dwordx4 v4, s[44:47], s64 offen lds\n\t"
    "v_mfma_f32_16x16x32_bf16 v[248:251], a[232:235], a[144:147], v[248:251]\n\t"
    "ds_read_b64_tr_b16 a[210:211], v10 offset:3712\n\t"
    "v_mfma_f32_16x16x32_bf16 v[248:251], a[236:239], a[148:151], v[248:251]\n\t"
    "v_mfma_f32_16x16x32_bf16 v[244:247], a[232:235], a[136:139], v[244:247]\n\t"
    "ds_read_b64_tr_b16 a[212:213], v10 offset:7296\n\t"
    "v_mfma_f32_16x16x32_bf16 v[244:247], a[236:239], a[140:143], v[244:247]\n\t"
    "buffer_load_dwordx4 a[112:115], v7, s[40:43], s66 offen offset:2048 sc1\n\t"
    "v_mfma_f32_16x16x32_bf16 v[240:243], a[232:235], a[128:131], v[240:243]\n\t"
    "ds_read_b64_tr_b16 a[214:215], v10 offset:10880\n\t"
    "v_mfma_f32_16x16x32_bf16 v[240:243], a[236:239], a[132:135], v[240:243]\n\t"
    "s_waitcnt lgkmcnt(8)\n\t"
    "v_mfma_f32_16x16x32_bf16 v[144:147], a[192:195], a[128:131], v[144:147]\n\t"
    "ds_read_b64_tr_b16 a[216:217], v10 offset:14464\n\t"
    "v_mfma_f32_16x16x32_bf16 v[144:147], a[196:199], a[132:135], v[144:147]\n\t"
    "s_add_u32 m0, s68, 59392\n\t"
    "v_mfma_f32_16x16x32_bf16 v[148:151], a[192:195], a[136:139], v[148:151]\n\t"
    "ds_read_b64_tr_b16 a[218:219], v10 offset:18048\n\t"
    "v_mfma_f32_16x16x32_bf16 v[148:151], a[196:199], a[140:143], v[148:151]\n\t"
    "buffer_load_dwordx4 v4, s[48:51], s64 offen lds\n\t"
    "v_mfma_f32_16x16x32_bf16 v[152:155], a[192:195], a[144:147], v[152:155]\n\t"
    "ds_read_b64_tr_b16 a[220:221], v10 offset:21632\n\t"
    "v_mfma_f32_16x16x32_bf16 v[152:155], a[196:199], a[148:151], v[152:155]\n\t"
    "v_mfma_f32_16x16x32_bf16 v[156:159], a[192:195], a[152:155], v[156:159]\n\t"
    "ds_read_b64_tr_b16 a[222:223], v10 offset:25216\n\t"
    "v_mfma_f32_16x16x32_bf16 v[156:159], a[196:199], a[156:159], v[156:159]\n\t"
    "buffer_load_dwordx4 a[116:119], v8, s[40:43], s66 offen offset:2048 sc1\n\t"
    "s_waitcnt lgkmcnt(8)\n\t"
    "v_mfma_f32_16x16x32_bf16 a[12:15], a[200:203], a[152:155], a[12:15]\n\t"
    "ds_read_b64_tr_b16 a[224:225], v10 offset:160\n\t"
    "v_mfma_f32_16x16x32_bf16 a[12:15], a[204:207], a[156:159], a[12:15]\n\t"
    "s_add_u32 m0, s69, 36864\n\t"
    "v_mfma_f32_16x16x32_bf16 a[8:11], a[200:203], a[144:147], a[8:11]\n\t"
    "ds_read_b64_tr_b16 a[226:227], v10 offset:3744\n\t"
    "v_mfma_f32_16x16x32_bf16 a[8:11], a[204:207], a[148:151], a[8:11]\n\t"
    "buffer_load_dwordx4 v5, s[52:55], s64 offen lds\n\t"
    "v_mfma_f32_16x16x32_bf16 a[4:7], a[200:203], a[136:139], a[4:7]\n\t"
    "ds_read_b64_tr_b16 a[228:229], v10 offset:7328\n\t"
    "v_mfma_f32_16x16x32_bf16 a[4:7], a[204:207], a[140:143], a[4:7]\n\t"
    "v_mfma_f32_16x16x32_bf16 a[0:3], a[200:203], a[128:131], a[0:3]\n\t"
    "ds_read_b64_tr_b16 a[230:231], v10 offset:10912\n\t"
    "v_mfma_f32_16x16x32_bf16 a[0:3], a[204:207], a[132:135], a[0:3]\n\t"
    "s_waitcnt lgkmcnt(8)\n\t"
    "v_mfma_f32_16x16x32_bf16 v[128:131], a[208:211], a[128:131], v[128:131]\n\t"
    "ds_read_b64_tr_b16 a[232:233], v10 offset:14496\n\t"
    "v_mfma_f32_16x16x32_bf16 v[128:131], a[212:215], a[132:135], v[128:131]\n\t"
    "buffer_load_dwordx4 a[120:123], v7, s[40:43], s66 offen offset:3072 sc1\n\t"
    "v_mfma_f32_16x16x32_bf16 v[132:135], a[208:211], a[136:139], v[132:135]\n\t"
    "ds_read_b64_tr_b16 a[234:235], v10 offset:18080\n\t"
    "v_mfma_f32_16x16x32_bf16 v[132:135], a[212:215], a[140:143], v[132:135]\n\t"
    "s_add_u32 m0, s68, 65536\n\t"
    "v_mfma_f32_16x16x32_bf16 v[136:139], a[208:211], a[144:147], v[136:139]\n\t"
    "ds_read_b64_tr_b16 a[236:237], v10 offset:21664\n\t"
    "v_mfma_f32_16x16x32_bf16 v[136:139], a[212:215], a[148:151], v[136:139]\n\t"
    "buffer_load_dwordx4 v6, s[56:59], s65 offen lds\n\t"
    "v_mfma_f32_16x16x32_bf16 v[140:143], a[208:211], a[152:155], v[140:143]\n\t"
    "ds_read_b64_tr_b16 a[238:239], v10 offset:25248\n\t"
    "v_mfma_f32_16x16x32_bf16 v[140:143], a[212:215], a[156:159], v[140:143]\n\t"
    "s_waitcnt lgkmcnt(8)\n\t"
    "v_mfma_f32_16x16x32_bf16 a[28:31], a[216:219], a[152:155], a[28:31]\n\t"
    "ds_read_b64_tr_b16 a[192:193], v10 offset:192\n\t"
    "v_mfma_f32_16x16x32_bf16 a[28:31], a[220:223], a[156:159], a[28:31]\n\t"
    "v_mfma_f32_16x16x32_bf16 a[24:27], a[216:219], a[144:147], a[24:27]\n\t"
    "ds_read_b64_tr_b16 a[194:195], v10 offset:3776\n\t"
    "v_mfma_f32_16x16x32_bf16 a[24:27], a[220:223], a[148:151], a[24:27]\n\t"
    "buffer_load_dwordx4 a[124:127], v8, s[40:43], s66 offen offset:3072 sc1\n\t"
    "v_mfma_f32_16x16x32_bf16 a[20:23], a[216:219], a[136:139], a[20:23]\n\t"
    "ds_read_b64_tr_b16 a[196:197], v10 offset:7360\n\t"
    "v_mfma_f32_16x16x32_bf16 a[20:23], a[220:223], a[140:143], a[20:23]\n\t"
    "s_add_u32 m0, s68, 69632\n\t"
    "v_mfma_f32_16x16x32_bf16 a[16:19], a[216:219], a[128:131], a[16:19]\n\t"
    "ds_read_b64_tr_b16 a[198:199], v10 offset:10944\n\t"
    "v_mfma_f32_16x16x32_bf16 a[16:19], a[220:223], a[132:135], a[16:19]\n\t"
    "buffer_load_dwordx4 v6, s[60:63], s65 offen lds\n\t"
    "s_waitcnt lgkmcnt(8)\n\t"
    "v_mfma_f32_16x16x32_bf16 v[112:115], a[224:227], a[128:131], v[112:115]\n\t"
    "ds_read_b64_tr_b16 a[200:201], v10 offset:14528\n\t"
    "v_mfma_f32_16x16x32_bf16 v[112:115], a[228:231], a[132:135], v[112:115]\n\t"
    "v_mfma_f32_16x16x32_bf16 v[116:119], a[224:227], a[136:139], v[116:119]\n\t"
    "ds_read_b64_tr_b16 a[202:203], v10 offset:18112\n\t"
    "v_mfma_f32_16x16x32_bf16 v[116:119], a[228:231], a[140:143], v[116:119]\n\t"
    "buffer_load_dword v28, v30, s[20:23], s84 offen\n\t"
    "v_mfma_f32_16x16x32_bf16 v[120:123], a[224:227], a[144:147], v[120:123]\n\t"
    "ds_read_b64_tr_b16 a[204:205], v10 offset:21696\n\t"
    "v_mfma_f32_16x16x32_bf16 v[120:123], a[228:231], a[148:151], v[120:123]\n\t"
    "v_mfma_f32_16x16x32_bf16 v[124:127], a[224:227], a[152:155], v[124:127]\n\t"
    "ds_read_b64_tr_b16 a[206:207], v10 offset:25280\n\t"
    "v_mfma_f32_16x16x32_bf16 v[124:127], a[228:231], a[156:159], v[124:127]\n\t"
    "s_waitcnt lgkmcnt(8)\n\t"
    "v_mfma_f32_16x16x32_bf16 a[44:47], a[232:235], a[152:155], a[44:47]\n\t"
    "ds_read_b128 a[208:211], v13 offset:0\n\t"
    "v_mfma_f32_16x16x32_bf16 a[44:47], a[236:239], a[156:159], a[44:47]\n\t"
    "buffer_load_dword v28, v0, s[24:27], s86 offen\n\t"
    "v_mfma_f32_16x16x32_bf16 a[40:43], a[232:235], a[144:147], a[40:43]\n\t"
    "ds_read_b128 a[212:215], v14 offset:0\n\t"
    "v_mfma_f32_16x16x32_bf16 a[40:43], a[236:239], a[148:151], a[40:43]\n\t"
    "v_mfma_f32_16x16x32_bf16 a[36:39], a[232:235], a[136:139], a[36:39]\n\t"
    "v_mfma_f32_16x16x32_bf16 a[36:39], a[236:239], a[140:143], a[36:39]\n\t"
    "v_mfma_f32_16x16x32_bf16 a[32:35], a[232:235], a[128:131], a[32:35]\n\t"
    "v_mfma_f32_16x16x32_bf16 a[32:35], a[236:239], a[132:135], a[32:35]\n\t"
    "s_waitcnt lgkmcnt(6)\n\t"
    "v_mfma_f32_16x16x32_bf16 v[96:99], a[192:195], a[128:131], v[96:99]\n\t"
    "ds_read_b128 a[216:219], v13 offset:4096\n\t"
    "v_mfma_f32_16x16x32_bf16 v[96:99], a[196:199], a[132:135], v[96:99]\n\t"
    "v_mfma_f32_16x16x32_bf16 v[100:103], a[192:195], a[136:139], v[100:103]\n\t"
    "ds_read_b128 a[220:223], v14 offset:4096\n\t"
    "v_mfma_f32_16x16x32_bf16 v[100:103], a[196:199], a[140:143], v[100:103]\n\t"
    "v_mfma_f32_16x16x32_bf16 v[104:107], a[192:195], a[144:147], v[104:107]\n\t"
    "v_mfma_f32_16x16x32_bf16 v[104:107], a[196:199], a[148:151], v[104:107]\n\t"
    "v_mfma_f32_16x16x32_bf16 v[108:111], a[192:195], a[152:155], v[108:111]\n\t"
    "v_mfma_f32_16x16x32_bf16 v[108:111], a[196:199], a[156:159], v[108:111]\n\t"
    "s_waitcnt lgkmcnt(4)\n\t"
    "v_mfma_f32_16x16x32_bf16 a[60:63], a[200:203], a[152:155], a[60:63]\n\t"
    "ds_read_b128 a[224:227], v13 offset:2048\n\t"
    "v_mfma_f32_16x16x32_bf16 a[60:63], a[204:207], a[156:159], a[60:63]\n\t"
    "v_mfma_f32_16x16x32_bf16 a[56:59], a[200:203], a[144:147], a[56:59]\n\t"
    "ds_read_b128 a[228:231], v14 offset:2048\n\t"
    "v_mfma_f32_16x16x32_bf16 a[56:59], a[204:207], a[148:151], a[56:59]\n\t"
    "v_mfma_f32_16x16x32_bf16 a[52:55], a[200:203], a[136:139], a[52:55]\n\t"
    "v_mfma_f32_16x16x32_bf16 a[52:55], a[204:207], a[140:143], a[52:55]\n\t"
    "v_mfma_f32_16x16x32_bf16 a[48:51], a[200:203], a[128:131], a[48:51]\n\t"
    "v_mfma_f32_16x16x32_bf16 a[48:51], a[204:207], a[132:135], a[48:51]\n\t"
    "s_waitcnt lgkmcnt(4)\n\t"
    "v_mfma_f32_16x16x32_bf16 v[32:35], a[128:131], a[208:211], v[32:35]\n\t"
    "ds_read_b128 a[232:235], v13 offset:6144\n\t"
    "v_mfma_f32_16x16x32_bf16 v[32:35], a[132:135], a[212:215], v[32:35]\n\t"
    "v_mfma_f32_16x16x32_bf16 v[36:39], a[136:139], a[208:211], v[36:39]\n\t"
    "ds_read_b128 a[236:239], v14 offset:6144\n\t"
    "v_mfma_f32_16x16x32_bf16 v[36:39], a[140:143], a[212:215], v[36:39]\n\t"
    "v_mfma_f32_16x16x32_bf16 v[40:43], a[144:147], a[208:211], v[40:43]\n\t"
    "v_mfma_f32_16x16x32_bf16 v[40:43], a[148:151], a[212:215], v[40:43]\n\t"
    "v_mfma_f32_16x16x32_bf16 v[44:47], a[152:155], a[208:211], v[44:47]\n\t"
    "v_mfma_f32_16x16x32_bf16 v[44:47], a[156:159], a[212:215], v[44:47]\n\t"
    "s_waitcnt lgkmcnt(4)\n\t"
    "v_mfma_f32_16x16x32_bf16 v[76:79], a[152:155], a[216:219], v[76:79]\n\t"
    "v_mfma_f32_16x16x32_bf16 v[76:79], a[156:159], a[220:223], v[76:79]\n\t"
    "v_mfma_f32_16x16x32_bf16 v[72:75], a[144:147], a[216:219], v[72:75]\n\t"
    "v_mfma_f32_16x16x32_bf16 v[72:75], a[148:151], a[220:223], v[72:75]\n\t"
    "s_add_u32 s64, s64, 458752\n\t"
    "v_mfma_f32_16x16x32_bf16 v[68:71], a[136:139], a[216:219], v[68:71]\n\t"
    "s_add_u32 s65, s65, 128\n\t"
    "v_mfma_f32_16x16x32_bf16 v[68:71], a[140:143], a[220:223], v[68:71]\n\t"
    "s_add_u32 s66, s66, 262144\n\t"
    "v_mfma_f32_16x16x32_bf16 v[64:67], a[128:131], a[216:219], v[64:67]\n\t"
    "s_add_u32 s84, s84, s85\n\t"
    "v_mfma_f32_16x16x32_bf16 v[64:67], a[132:135], a[220:223], v[64:67]\n\t"
    "s_add_u32 s86, s86, s39\n\t"
    "s_waitcnt vmcnt(38) lgkmcnt(0)\n\t"
    "s_barrier\n\t"
    "v_mfma_f32_16x16x32_bf16 v[48:51], a[128:131], a[224:227], v[48:51]\n\t"
    "ds_read_b64_tr_b16 a[192:193], v10 offset:36864\n\t"
    "v_mfma_f32_16x16x32_bf16 v[48:51], a[132:135], a[228:231], v[48:51]\n\t"
    "ds_read_b64_tr_b16 a[194:195], v10 offset:40448\n\t"
    "v_mfma_f32_16x16x32_bf16 v[52:55], a[136:139], a[224:227], v[52:55]\n\t"
    "ds_read_b64_tr_b16 a[196:197], v10 offset:44032\n\t"
    "v_mfma_f32_16x16x32_bf16 v[52:55], a[140:143], a[228:231], v[52:55]\n\t"
    "ds_read_b64_tr_b16 a[198:199], v10 offset:47616\n\t"
    "v_mfma_f32_16x16x32_bf16 v[56:59], a[144:147], a[224:227], v[56:59]\n\t"
    "ds_read_b64_tr_b16 a[200:201], v10 offset:51200\n\t"
    "v_mfma_f32_16x16x32_bf16 v[56:59], a[148:151], a[228:231], v[56:59]\n\t"
    "ds_read_b64_tr_b16 a[202:203], v10 offset:54784\n\t"
    "v_mfma_f32_16x16x32_bf16 v[60:63], a[152:155], a[224:227], v[60:63]\n\t"
    "ds_read_b64_tr_b16 a[204:205], v10 offset:58368\n\t"
    "v_mfma_f32_16x16x32_bf16 v[60:63], a[156:159], a[228:231], v[60:63]\n\t"
    "ds_read_b64_tr_b16 a[206:207], v10 offset:61952\n\t"
    "v_mfma_f32_16x16x32_bf16 v[92:95], a[152:155], a[232:235], v[92:95]\n\t"
    "ds_read_b64_tr_b16 a[208:209], v10 offset:36896\n\t"
    "v_mfma_f32_16x16x32_bf16 v[92:95], a[156:159], a[236:239], v[92:95]\n\t"
    "ds_read_b64_tr_b16 a[210:211], v10 offset:40480\n\t"
    "v_mfma_f32_16x16x32_bf16 v[88:91], a[144:147], a[232:235], v[88:91]\n\t"
    "ds_read_b64_tr_b16 a[212:213], v10 offset:44064\n\t"
    "v_mfma_f32_16x16x32_bf16 v[88:91], a[148:151], a[236:239], v[88:91]\n\t"
    "ds_read_b64_tr_b16 a[214:215], v10 offset:47648\n\t"
    "v_mfma_f32_16x16x32_bf16 v[84:87], a[136:139], a[232:235], v[84:87]\n\t"
    "v_mfma_f32_16x16x32_bf16 v[84:87], a[140:143], a[236:239], v[84:87]\n\t"
    "v_mfma_f32_16x16x32_bf16 v[80:83], a[128:131], a[232:235], v[80:83]\n\t"
    "v_mfma_f32_16x16x32_bf16 v[80:83], a[132:135], a[236:239], v[80:83]\n\t"
    "s_waitcnt lgkmcnt(8)\n\t"
    "v_mfma_f32_16x16x32_bf16 v[192:195], a[192:195], a[160:163], v[192:195]\n\t"
    "ds_read_b64_tr_b16 a[216:217], v10 offset:51232\n\t"
    "v_mfma_f32_16x16x32_bf16 v[192:195], a[196:199], a[164:167], v[192:195]\n\t"
    "s_add_u32 m0, s68, 73728\n\t"
    "v_mfma_f32_16x16x32_bf16 v[196:199], a[192:195], a[168:171], v[196:199]\n\t"
    "ds_read_b64_tr_b16 a[218:219], v10 offset:54816\n\t"
    "v_mfma_f32_16x16x32_bf16 v[196:199], a[196:199], a[172:175], v[196:199]\n\t"
    "buffer_load_dwordx4 v2, s[44:47], s64 offen lds\n\t"
    "v_mfma_f32_16x16x32_bf16 v[200:203], a[192:195], a[176:179], v[200:203]\n\t"
    "ds_read_b64_tr_b16 a[220:221], v10 offset:58400\n\t"
    "v_mfma_f32_16x16x32_bf16 v[200:203], a[196:199], a[180:183], v[200:203]\n\t"
    "v_mfma_f32_16x16x32_bf16 v[204:207], a[192:195], a[184:187], v[204:207]\n\t"
    "ds_read_b64_tr_b16 a[222:223], v10 offset:61984\n\t"
    "v_mfma_f32_16x16x32_bf16 v[204:207], a[196:199], a[188:191], v[204:207]\n\t"
    "buffer_load_dwordx4 a[128:131], v7, s[40:43], s66 offen offset:0 sc1\n\t"
    "s_waitcnt lgkmcnt(8)\n\t"
    "v_mfma_f32_16x16x32_bf16 v[220:223], a[200:203], a[184:187], v[220:223]\n\t"
    "ds_read_b64_tr_b16 a[224:225], v10 offset:36928\n\t"
    "v_mfma_f32_16x16x32_bf16 v[220:223], a[204:207], a[188:191], v[220:223]\n\t"
    "v_mfma_f32_16x16x32_bf16 v[216:219], a[200:203], a[176:179], v[216:219]\n\t"
    "ds_read_b64_tr_b16 a[226:227], v10 offset:40512\n\t"
    "v_mfma_f32_16x16x32_bf16 v[216:219], a[204:207], a[180:183], v[216:219]\n\t"
    "s_add_u32 m0, s68, 88064\n\t"
    "v_mfma_f32_16x16x32_bf16 v[212:215], a[200:203], a[168:171], v[212:215]\n\t"
    "ds_read_b64_tr_b16 a[228:229], v10 offset:44096\n\t"
    "v_mfma_f32_16x16x32_bf16 v[212:215], a[204:207], a[172:175], v[212:215]\n\t"
    "buffer_load_dwordx4 v2, s[48:51], s64 offen lds\n\t"
    "v_mfma_f32_16x16x32_bf16 v[208:211], a[200:203], a[160:163], v[208:211]\n\t"
    "ds_read_b64_tr_b16 a[230:231], v10 offset:47680\n\t"
    "v_mfma_f32_16x16x32_bf16 v[208:211], a[204:207], a[164:167], v[208:211]\n\t"
    "s_waitcnt lgkmcnt(8)\n\t"
    "v_mfma_f32_16x16x32_bf16 v[176:179], a[208:211], a[160:163], v[176:179]\n\t"
    "ds_read_b64_tr_b16 a[232:233], v10 offset:51264\n\t"
    "v_mfma_f32_16x16x32_bf16 v[176:179], a[212:215], a[164:167], v[176:179]\n\t"
    "buffer_load_dwordx4 a[132:135], v8, s[40:43], s66 offen offset:0 sc1\n\t"
    "v_mfma_f32_16x16x32_bf16 v[180:183], a[208:211], a[168:171], v[180:183]\n\t"
    "ds_read_b64_tr_b16 a[234:235], v10 offset:54848\n\t"
    "v_mfma_f32_16x16x32_bf16 v[180:183], a[212:215], a[172:175], v[180:183]\n\t"
    "s_add_u32 m0, s68, 77824\n\t"
    "v_mfma_f32_16x16x32_bf16 v[184:187], a[208:211], a[176:179], v[184:187]\n\t"
    "ds_read_b64_tr_b16 a[236:237], v10 offset:58432\n\t"
    "v_mfma_f32_16x16x32_bf16 v[184:187], a[212:215], a[180:183], v[184:187]\n\t"
    "buffer_load_dwordx4 v3, s[44:47], s64 offen lds\n\t"
    "v_mfma_f32_16x16x32_bf16 v[188:191], a[208:211], a[184:187], v[188:191]\n\t"
    "ds_read_b64_tr_b16 a[238:239], v10 offset:62016\n\t"
    "v_mfma_f32_16x16x32_bf16 v[188:191], a[212:215], a[188:191], v[188:191]\n\t"
    "s_waitcnt lgkmcnt(8)\n\t"
    "v_mfma_f32_16x16x32_bf16 v[236:239], a[216:219], a[184:187], v[236:239]\n\t"
    "ds_read_b64_tr_b16 a[192:193], v10 offset:36960\n\t"
    "v_mfma_f32_16x16x32_bf16 v[236:239], a[220:223], a[188:191], v[236:239]\n\t"
    "v_mfma_f32_16x16x32_bf16 v[232:235], a[216:219], a[176:179], v[232:235]\n\t"
    "ds_read_b64_tr_b16 a[194:195], v10 offset:40544\n\t"
    "v_mfma_f32_16x16x32_bf16 v[232:235], a[220:223], a[180:183], v[232:235]\n\t"
    "buffer_load_dwordx4 a[136:139], v7, s[40:43], s66 offen offset:1024 sc1\n\t"
    "v_mfma_f32_16x16x32_bf16 v[228:231], a[216:219], a[168:171], v[228:231]\n\t"
    "ds_read_b64_tr_b16 a[196:197], v10 offset:44128\n\t"
    "v_mfma_f32_16x16x32_bf16 v[228:231], a[220:223], a[172:175], v[228:231]\n\t"
    "s_add_u32 m0, s68, 92160\n\t"
    "v_mfma_f32_16x16x32_bf16 v[224:227], a[216:219], a[160:163], v[224:227]\n\t"
    "ds_read_b64_tr_b16 a[198:199], v10 offset:47712\n\t"
    "v_mfma_f32_16x16x32_bf16 v[224:227], a[220:223], a[164:167], v[224:227]\n\t"
    "buffer_load_dwordx4 v3, s[48:51], s64 offen lds\n\t"
    "s_waitcnt lgkmcnt(8)\n\t"
    "v_mfma_f32_16x16x32_bf16 v[160:163], a[224:227], a[160:163], v[160:163]\n\t"
    "ds_read_b64_tr_b16 a[200:201], v10 offset:51296\n\t"
    "v_mfma_f32_16x16x32_bf16 v[160:163], a[228:231], a[164:167], v[160:163]\n\t"
    "v_mfma_f32_16x16x32_bf16 v[164:167], a[224:227], a[168:171], v[164:167]\n\t"
    "ds_read_b64_tr_b16 a[202:203], v10 offset:54880\n\t"
    "v_mfma_f32_16x16x32_bf16 v[164:167], a[228:231], a[172:175], v[164:167]\n\t"
    "v_mfma_f32_16x16x32_bf16 v[168:171], a[224:227], a[176:179], v[168:171]\n\t"
    "ds_read_b64_tr_b16 a[204:205], v10 offset:58464\n\t"
    "v_mfma_f32_16x16x32_bf16 v[168:171], a[228:231], a[180:183], v[168:171]\n\t"
    "buffer_load_dwordx4 a[140:143], v8, s[40:43], s66 offen offset:1024 sc1\n\t"
    "v_mfma_f32_16x16x32_bf16 v[172:175], a[224:227], a[184:187], v[172:175]\n\t"
    "ds_read_b64_tr_b16 a[206:207], v10 offset:62048\n\t"
    "v_mfma_f32_16x16x32_bf16 v[172:175], a[228:231], a[188:191], v[172:175]\n\t"
    "s_add_u32 m0, s68, 81920\n\t"
    "s_waitcnt lgkmcnt(8)\n\t"
    "v_mfma_f32_16x16x32_bf16 v[252:255], a[232:235], a[184:187], v[252:255]\n\t"
    "ds_read_b64_tr_b16 a[208:209], v10 offset:36992\n\t"
    "v_mfma_f32_16x16x32_bf16 v[252:255], a[236:239], a[188:191], v[252:255]\n\t"
    "buffer_load_dwordx4 v4, s[44:47], s64 offen lds\n\t"
    "v_mfma_f32_16x16x32_bf16 v[248:251], a[232:235], a[176:179], v[248:251]\n\t"
    "ds_read_b64_tr_b16 a[210:211], v10 offset:40576\n\t"
    "v_mfma_f32_16x16x32_bf16 v[248:251], a[236:239], a[180:183], v[248:251]\n\t"
    "v_mfma_f32_16x16x32_bf16 v[244:247], a[232:235], a[168:171], v[244:247]\n\t"
    "ds_read_b64_tr_b16 a[212:213], v10 offset:44160\n\t"
    "v_mfma_f32_16x16x32_bf16 v[244:247], a[236:239], a[172:175], v[244:247]\n\t"
    "buffer_load_dwordx4 a[144:147], v7, s[40:43], s66 offen offset:2048 sc1\n\t"
    "v_mfma_f32_16x16x32_bf16 v[240:243], a[232:235], a[160:163], v[240:243]\n\t"
    "ds_read_b64_tr_b16 a[214:215], v10 offset:47744\n\t"
    "v_mfma_f32_16x16x32_bf16 v[240:243], a[236:239], a[164:167], v[240:243]\n\t"
    "s_waitcnt lgkmcnt(8)\n\t"
    "v_mfma_f32_16x16x32_bf16 v[144:147], a[192:195], a[160:163], v[144:147]\n\t"
    "ds_read_b64_tr_b16 a[216:217], v10 offset:51328\n\t"
    "v_mfma_f32_16x16x32_bf16 v[144:147], a[196:199], a[164:167], v[144:147]\n\t"
    "s_add_u32 m0, s68, 96256\n\t"
    "v_mfma_f32_16x16x32_bf16 v[148:151], a[192:195], a[168:171], v[148:151]\n\t"
    "ds_read_b64_tr_b16 a[218:219], v10 offset:54912\n\t"
    "v_mfma_f32_16x16x32_bf16 v[148:151], a[196:199], a[172:175], v[148:151]\n\t"
    "buffer_load_dwordx4 v4, s[48:51], s64 offen lds\n\t"
    "v_mfma_f32_16x16x32_bf16 v[152:155], a[192:195], a[176:179], v[152:155]\n\t"
    "ds_read_b64_tr_b16 a[220:221], v10 offset:58496\n\t"
    "v_mfma_f32_16x16x32_bf16 v[152:155], a[196:199], a[180:183], v[152:155]\n\t"
    "v_mfma_f32_16x16x32_bf16 v[156:159], a[192:195], a[184:187], v[156:159]\n\t"
    "ds_read_b64_tr_b16 a[222:223], v10 offset:62080\n\t"
    "v_mfma_f32_16x16x32_bf16 v[156:159], a[196:199], a[188:191], v[156:159]\n\t"
    "buffer_load_dwordx4 a[148:151], v8, s[40:43], s66 offen offset:2048 sc1\n\t"
    "s_waitcnt lgkmcnt(8)\n\t"
    "v_mfma_f32_16x16x32_bf16 a[12:15], a[200:203], a[184:187], a[12:15]\n\t"
    "ds_read_b64_tr_b16 a[224:225], v10 offset:37024\n\t"
    "v_mfma_f32_16x16x32_bf16 a[12:15], a[204:207], a[188:191], a[12:15]\n\t"
    "s_add_u32 m0, s69, 73728\n\t"
    "v_mfma_f32_16x16x32_bf16 a[8:11], a[200:203], a[176:179], a[8:11]\n\t"
    "ds_read_b64_tr_b16 a[226:227], v10 offset:40608\n\t"
    "v_mfma_f32_16x16x32_bf16 a[8:11], a[204:207], a[180:183], a[8:11]\n\t"
    "buffer_load_dwordx4 v5, s[52:55], s64 offen lds\n\t"
    "v_mfma_f32_16x16x32_bf16 a[4:7], a[200:203], a[168:171], a[4:7]\n\t"
    "ds_read_b64_tr_b16 a[228:229], v10 offset:44192\n\t"
    "v_mfma_f32_16x16x32_bf16 a[4:7], a[204:207], a[172:175], a[4:7]\n\t"
    "v_mfma_f32_16x16x32_bf16 a[0:3], a[200:203], a[160:163], a[0:3]\n\t"
    "ds_read_b64_tr_b16 a[230:231], v10 offset:47776\n\t"
    "v_mfma_f32_16x16x32_bf16 a[0:3], a[204:207], a[164:167], a[0:3]\n\t"
    "s_waitcnt lgkmcnt(8)\n\t"
    "v_mfma_f32_16x16x32_bf16 v[128:131], a[208:211], a[160:163], v[128:131]\n\t"
    "ds_read_b64_tr_b16 a[232:233], v10 offset:51360\n\t"
    "v_mfma_f32_16x16x32_bf16 v[128:131], a[212:215], a[164:167], v[128:131]\n\t"
    "buffer_load_dwordx4 a[152:155], v7, s[40:43], s66 offen offset:3072 sc1\n\t"
    "v_mfma_f32_16x16x32_bf16 v[132:135], a[208:211], a[168:171], v[132:135]\n\t"
    "ds_read_b64_tr_b16 a[234:235], v10 offset:54944\n\t"
    "v_mfma_f32_16x16x32_bf16 v[132:135], a[212:215], a[172:175], v[132:135]\n\t"
    "s_add_u32 m0, s68, 102400\n\t"
    "v_mfma_f32_16x16x32_bf16 v[136:139], a[208:211], a[176:179], v[136:139]\n\t"
    "ds_read_b64_tr_b16 a[236:237], v10 offset:58528\n\t"
    "v_mfma_f32_16x16x32_bf16 v[136:139], a[212:215], a[180:183], v[136:139]\n\t"
    "buffer_load_dwordx4 v6, s[56:59], s65 offen lds\n\t"
    "v_mfma_f32_16x16x32_bf16 v[140:143], a[208:211], a[184:187], v[140:143]\n\t"
    "ds_read_b64_tr_b16 a[238:239], v10 offset:62112\n\t"
    "v_mfma_f32_16x16x32_bf16 v[140:143], a[212:215], a[188:191], v[140:143]\n\t"
    "s_waitcnt lgkmcnt(8)\n\t"
    "v_mfma_f32_16x16x32_bf16 a[28:31], a[216:219], a[184:187], a[28:31]\n\t"
    "ds_read_b64_tr_b16 a[192:193], v10 offset:37056\n\t"
    "v_mfma_f32_16x16x32_bf16 a[28:31], a[220:223], a[188:191], a[28:31]\n\t"
    "v_mfma_f32_16x16x32_bf16 a[24:27], a[216:219], a[176:179], a[24:27]\n\t"
    "ds_read_b64_tr_b16 a[194:195], v10 offset:40640\n\t"
    "v_mfma_f32_16x16x32_bf16 a[24:27], a[220:223], a[180:183], a[24:27]\n\t"
    "buffer_load_dwordx4 a[156:159], v8, s[40:43], s66 offen offset:3072 sc1\n\t"
    "v_mfma_f32_16x16x32_bf16 a[20:23], a[216:219], a[168:171], a[20:23]\n\t"
    "ds_read_b64_tr_b16 a[196:197], v10 offset:44224\n\t"
    "v_mfma_f32_16x16x32_bf16 a[20:23], a[220:223], a[172:175], a[20:23]\n\t"
    "s_add_u32 m0, s68, 106496\n\t"
    "v_mfma_f32_16x16x32_bf16 a[16:19], a[216:219], a[160:163], a[16:19]\n\t"
    "ds_read_b64_tr_b16 a[198:199], v10 offset:47808\n\t"
    "v_mfma_f32_16x16x32_bf16 a[16:19], a[220:223], a[164:167], a[16:19]\n\t"
    "buffer_load_dwordx4 v6, s[60:63], s65 offen lds\n\t"
    "s_waitcnt lgkmcnt(8)\n\t"
    "v_mfma_f32_16x16x32_bf16 v[112:115], a[224:227], a[160:163], v[112:115]\n\t"
    "ds_read_b64_tr_b16 a[200:201], v10 offset:51392\n\t"
    "v_mfma_f32_16x16x32_bf16 v[112:115], a[228:231], a[164:167], v[112:115]\n\t"
    "v_mfma_f32_16x16x32_bf16 v[116:119], a[224:227], a[168:171], v[116:119]\n\t"
    "ds_read_b64_tr_b16 a[202:203], v10 offset:54976\n\t"
    "v_mfma_f32_16x16x32_bf16 v[116:119], a[228:231], a[172:175], v[116:119]\n\t"
    "buffer_load_dword v28, v30, s[20:23], s84 offen\n\t"
    "v_mfma_f32_16x16x32_bf16 v[120:123], a[224:227], a[176:179], v[120:123]\n\t"
    "ds_read_b64_tr_b16 a[204:205], v10 offset:58560\n\t"
    "v_mfma_f32_16x16x32_bf16 v[120:123], a[228:231], a[180:183], v[120:123]\n\t"
    "v_mfma_f32_16x16x32_bf16 v[124:127], a[224:227], a[184:187], v[124:127]\n\t"
    "ds_read_b64_tr_b16 a[206:207], v10 offset:62144\n\t"
    "v_mfma_f32_16x16x32_bf16 v[124:127], a[228:231], a[188:191], v[124:127]\n\t"
    "s_waitcnt lgkmcnt(8)\n\t"
    "v_mfma_f32_16x16x32_bf16 a[44:47], a[232:235], a[184:187], a[44:47]\n\t"
    "ds_read_b128 a[208:211], v13 offset:36864\n\t"
    "v_mfma_f32_16x16x32_bf16 a[44:47], a[236:239], a[188:191], a[44:47]\n\t"
    "buffer_load_dword v28, v0, s[24:27], s86 offen\n\t"
    "v_mfma_f32_16x16x32_bf16 a[40:43], a[232:235], a[176:179], a[40:43]\n\t"
    "ds_read_b128 a[212:215], v14 offset:36864\n\t"
    "v_mfma_f32_16x16x32_bf16 a[40:43], a[236:239], a[180:183], a[40:43]\n\t"
    "v_mfma_f32_16x16x32_bf16 a[36:39], a[232:235], a[168:171], a[36:39]\n\t"
    "v_mfma_f32_16x16x32_bf16 a[36:39], a[236:239], a[172:175], a[36:39]\n\t"
    "v_mfma_f32_16x16x32_bf16 a[32:35], a[232:235], a[160:163], a[32:35]\n\t"
    "v_mfma_f32_16x16x32_bf16 a[32:35], a[236:239], a[164:167], a[32:35]\n\t"
    "s_waitcnt lgkmcnt(6)\n\t"
    "v_mfma_f32_16x16x32_bf16 v[96:99], a[192:195], a[160:163], v[96:99]\n\t"
    "ds_read_b128 a[216:219], v13 offset:40960\n\t"
    "v_mfma_f32_16x16x32_bf16 v[96:99], a[196:199], a[164:167], v[96:99]\n\t"
    "v_mfma_f32_16x16x32_bf16 v[100:103], a[192:195], a[168:171], v[100:103]\n\t"
    "ds_read_b128 a[220:223], v14 offset:40960\n\t"
    "v_mfma_f32_16x16x32_bf16 v[100:103], a[196:199], a[172:175], v[100:103]\n\t"
    "v_mfma_f32_16x16x32_bf16 v[104:107], a[192:195], a[176:179], v[104:107]\n\t"
    "v_mfma_f32_16x16x32_bf16 v[104:107], a[196:199], a[180:183], v[104:107]\n\t"
    "v_mfma_f32_16x16x32_bf16 v[108:111], a[192:195], a[184:187], v[108:111]\n\t"
    "v_mfma_f32_16x16x32_bf16 v[108:111], a[196:199], a[188:191], v[108:111]\n\t"
    "s_waitcnt lgkmcnt(4)\n\t"
    "v_mfma_f32_16x16x32_bf16 a[60:63], a[200:203], a[184:187], a[60:63]\n\t"
    "ds_read_b128 a[224:227], v13 offset:38912\n\t"
    "v_mfma_f32_16x16x32_bf16 a[60:63], a[204:207], a[188:191], a[60:63]\n\t"
    "v_mfma_f32_16x16x32_bf16 a[56:59], a[200:203], a[176:179], a[56:59]\n\t"
    "ds_read_b128 a[228:231], v14 offset:38912\n\t"
    "v_mfma_f32_16x16x32_bf16 a[56:59], a[204:207], a[180:183], a[56:59]\n\t"
    "v_mfma_f32_16x16x32_bf16 a[52:55], a[200:203], a[168:171], a[52:55]\n\t"
    "v_mfma_f32_16x16x32_bf16 a[52:55], a[204:207], a[172:175], a[52:55]\n\t"
    "v_mfma_f32_16x16x32_bf16 a[48:51], a[200:203], a[160:163], a[48:51]\n\t"
    "v_mfma_f32_16x16x32_bf16 a[48:51], a[204:207], a[164:167], a[48:51]\n\t"
    "s_waitcnt lgkmcnt(4)\n\t"
    "v_mfma_f32_16x16x32_bf16 v[32:35], a[160:163], a[208:211], v[32:35]\n\t"
    "ds_read_b128 a[232:235], v13 offset:43008\n\t"
    "v_mfma_f32_16x16x32_bf16 v[32:35], a[164:167], a[212:215], v[32:35]\n\t"
    "v_mfma_f32_16x16x32_bf16 v[36:39], a[168:171], a[208:211], v[36:39]\n\t"
    "ds_read_b128 a[236:239], v14 offset:43008\n\t"
    "v_mfma_f32_16x16x32_bf16 v[36:39], a[172:175], a[212:215], v[36:39]\n\t"
    "v_mfma_f32_16x16x32_bf16 v[40:43], a[176:179], a[208:211], v[40:43]\n\t"
    "v_mfma_f32_16x16x32_bf16 v[40:43], a[180:183], a[212:215], v[40:43]\n\t"
    "v_mfma_f32_16x16x32_bf16 v[44:47], a[184:187], a[208:211], v[44:47]\n\t"
    "v_mfma_f32_16x16x32_bf16 v[44:47], a[188:191], a[212:215], v[44:47]\n\t"
    "s_waitcnt lgkmcnt(4)\n\t"
    "v_mfma_f32_16x16x32_bf16 v[76:79], a[184:187], a[216:219], v[76:79]\n\t"
    "v_mfma_f32_16x16x32_bf16 v[76:79], a[188:191], a[220:223], v[76:79]\n\t"
    "v_mfma_f32_16x16x32_bf16 v[72:75], a[176:179], a[216:219], v[72:75]\n\t"
    "v_mfma_f32_16x16x32_bf16 v[72:75], a[180:183], a[220:223], v[72:75]\n\t"
    "s_add_u32 s64, s64, 458752\n\t"
    "v_mfma_f32_16x16x32_bf16 v[68:71], a[168:171], a[216:219], v[68:71]\n\t"
    "s_add_u32 s65, s65, 128\n\t"
    "v_mfma_f32_16x16x32_bf16 v[68:71], a[172:175], a[220:223], v[68:71]\n\t"
    "s_add_u32 s66, s66, 262144\n\t"
    "v_mfma_f32_16x16x32_bf16 v[64:67], a[160:163], a[216:219], v[64:67]\n\t"
    "s_add_u32 s84, s84, s85\n\t"
    "v_mfma_f32_16x16x32_bf16 v[64:67], a[164:167], a[220:223], v[64:67]\n\t"
    "s_add_u32 s86, s86, s39\n\t"
    "s_waitcnt vmcnt(38) lgkmcnt(0)\n\t"
    "s_barrier\n\t"
    "v_mfma_f32_16x16x32_bf16 v[48:51], a[160:163], a[224:227], v[48:51]\n\t"
    "ds_read_b64_tr_b16 a[192:193], v9 offset:0\n\t"
    "v_mfma_f32_16x16x32_bf16 v[48:51], a[164:167], a[228:231], v[48:51]\n\t"
    "ds_read_b64_tr_b16 a[194:195], v9 offset:3584\n\t"
    "v_mfma_f32_16x16x32_bf16 v[52:55], a[168:171], a[224:227], v[52:55]\n\t"
    "ds_read_b64_tr_b16 a[196:197], v9 offset:7168\n\t"
    "v_mfma_f32_16x16x32_bf16 v[52:55], a[172:175], a[228:231], v[52:55]\n\t"
    "ds_read_b64_tr_b16 a[198:199], v9 offset:10752\n\t"
    "v_mfma_f32_16x16x32_bf16 v[56:59], a[176:179], a[224:227], v[56:59]\n\t"
    "ds_read_b64_tr_b16 a[200:201], v9 offset:14336\n\t"
    "v_mfma_f32_16x16x32_bf16 v[56:59], a[180:183], a[228:231], v[56:59]\n\t"
    "ds_read_b64_tr_b16 a[202:203], v9 offset:17920\n\t"
    "v_mfma_f32_16x16x32_bf16 v[60:63], a[184:187], a[224:227], v[60:63]\n\t"
    "ds_read_b64_tr_b16 a[204:205], v9 offset:21504\n\t"
    "v_mfma_f32_16x16x32_bf16 v[60:63], a[188:191], a[228:231], v[60:63]\n\t"
    "ds_read_b64_tr_b16 a[206:207], v9 offset:25088\n\t"
    "v_mfma_f32_16x16x32_bf16 v[92:95], a[184:187], a[232:235], v[92:95]\n\t"
    "ds_read_b64_tr_b16 a[208:209], v9 offset:32\n\t"
    "v_mfma_f32_16x16x32_bf16 v[92:95], a[188:191], a[236:239], v[92:95]\n\t"
    "ds_read_b64_tr_b16 a[210:211], v9 offset:3616\n\t"
    "v_mfma_f32_16x16x32_bf16 v[88:91], a[176:179], a[232:235], v[88:91]\n\t"
    "ds_read_b64_tr_b16 a[212:213], v9 offset:7200\n\t"
    "v_mfma_f32_16x16x32_bf16 v[88:91], a[180:183], a[236:239], v[88:91]\n\t"
    "ds_read_b64_tr_b16 a[214:215], v9 offset:10784\n\t"
    "v_mfma_f32_16x16x32_bf16 v[84:87], a[168:171], a[232:235], v[84:87]\n\t"
    "v_mfma_f32_16x16x32_bf16 v[84:87], a[172:175], a[236:239], v[84:87]\n\t"
    "v_mfma_f32_16x16x32_bf16 v[80:83], a[160:163], a[232:235], v[80:83]\n\t"
    "v_mfma_f32_16x16x32_bf16 v[80:83], a[164:167], a[236:239], v[80:83]\n\t"
    "s_sub_u32 s67, s67, 1\n\t"
    "s_cmp_lg_u32 s67, 0\n\t"
    "s_cbranch_scc1 L_g1_%=\n\t"
    "s_waitcnt lgkmcnt(8)\n\t"
    "v_mfma_f32_16x16x32_bf16 v[192:195], a[192:195], a[64:67], v[192:195]\n\t"
    "ds_read_b64_tr_b16 a[216:217], v9 offset:14368\n\t"
    "s_getpc_b64 s[20:21]\n\t"
    ".Lk_code_mid:\n\t"
    "s_mov_b32 s22, .Lk_code_end-.Lk_code_mid\n\t"
    "v_mfma_f32_16x16x32_bf16 v[192:195], a[196:199], a[68:71], v[192:195]\n\t"
    "s_lshr_b32 s90, %[bid], 3\n\t"
    "s_lshl_b32 s90, s90, 2\n\t"
    "s_add_u32 s90, s90, s87\n\t"
    "v_mfma_f32_16x16x32_bf16 v[196:199], a[192:195], a[72:75], v[196:199]\n\t"
    "ds_read_b64_tr_b16 a[218:219], v9 offset:17952\n\t"
    "s_lshl_b32 s90, s90, 9\n\t"
    "v_lshlrev_b32_e32 v15, 3, v31\n\t"
    "v_add_u32_e32 v0, s90, v15\n\t"
    "v_mfma_f32_16x16x32_bf16 v[196:199], a[196:199], a[76:79], v[196:199]\n\t"
    "v_cmp_gt_u32_e32 vcc, 0x200, v15\n\t"
    "v_bfrev_b32_e32 v15, 1\n\t"
    "s_nop 0\n\t"
    "v_mfma_f32_16x16x32_bf16 v[200:203], a[192:195], a[80:83], v[200:203]\n\t"
    "ds_read_b64_tr_b16 a[220:221], v9 offset:21536\n\t"
    "v_cndmask_b32_e32 v0, v15, v0, vcc\n\t"
    "buffer_load_dword v28, v0, s[20:23], 0 offen\n\t"
    "v_mfma_f32_16x16x32_bf16 v[200:203], a[196:199], a[84:87], v[200:203]\n\t"
    "s_add_u32 m0, s68, 110592\n\t"
    "v_mfma_f32_16x16x32_bf16 v[204:207], a[192:195], a[88:91], v[204:207]\n\t"
    "ds_read_b64_tr_b16 a[222:223], v9 offset:25120\n\t"
    "v_mfma_f32_16x16x32_bf16 v[204:207], a[196:199], a[92:95], v[204:207]\n\t"
    "buffer_load_dwordx4 v2, s[44:47], s64 offen lds\n\t"
    "s_waitcnt lgkmcnt(8)\n\t"
    "v_mfma_f32_16x16x32_bf16 v[220:223], a[200:203], a[88:91], v[220:223]\n\t"
    "ds_read_b64_tr_b16 a[224:225], v9 offset:64\n\t"
    "v_mfma_f32_16x16x32_bf16 v[220:223], a[204:207], a[92:95], v[220:223]\n\t"
    "v_mfma_f32_16x16x32_bf16 v[216:219], a[200:203], a[80:83], v[216:219]\n\t"
    "ds_read_b64_tr_b16 a[226:227], v9 offset:3648\n\t"
    "v_mfma_f32_16x16x32_bf16 v[216:219], a[204:207], a[84:87], v[216:219]\n\t"
    "buffer_load_dwordx4 a[160:163], v7, s[40:43], s66 offen offset:0 sc1\n\t"
    "v_mfma_f32_16x16x32_bf16 v[212:215], a[200:203], a[72:75], v[212:215]\n\t"
    "ds_read_b64_tr_b16 a[228:229], v9 offset:7232\n\t"
    "v_mfma_f32_16x16x32_bf16 v[212:215], a[204:207], a[76:79], v[212:215]\n\t"
    "s_add_u32 m0, s68, 124928\n\t"
    "v_mfma_f32_16x16x32_bf16 v[208:211], a[200:203], a[64:67], v[208:211]\n\t"
    "ds_read_b64_tr_b16 a[230:231], v9 offset:10816\n\t"
    "v_mfma_f32_16x16x32_bf16 v[208:211], a[204:207], a[68:71], v[208:211]\n\t"
    "buffer_load_dwordx4 v2, s[48:51], s64 offen lds\n\t"
    "s_waitcnt lgkmcnt(8)\n\t"
    "v_mfma_f32_16x16x32_bf16 v[176:179], a[208:211], a[64:67], v[176:179]\n\t"
    "ds_read_b64_tr_b16 a[232:233], v9 offset:14400\n\t"
    "v_mfma_f32_16x16x32_bf16 v[176:179], a[212:215], a[68:71], v[176:179]\n\t"
    "v_mfma_f32_16x16x32_bf16 v[180:183], a[208:211], a[72:75], v[180:183]\n\t"
    "ds_read_b64_tr_b16 a[234:235], v9 offset:17984\n\t"
    "v_mfma_f32_16x16x32_bf16 v[180:183], a[212:215], a[76:79], v[180:183]\n\t"
    "buffer_load_dwordx4 a[164:167], v8, s[40:43], s66 offen offset:0 sc1\n\t"
    "v_mfma_f32_16x16x32_bf16 v[184:187], a[208:211], a[80:83], v[184:187]\n\t"
    "ds_read_b64_tr_b16 a[236:237], v9 offset:21568\n\t"
    "v_mfma_f32_16x16x32_bf16 v[184:187], a[212:215], a[84:87], v[184:187]\n\t"
    "s_add_u32 m0, s68, 114688\n\t"
    "v_mfma_f32_16x16x32_bf16 v[188:191], a[208:211], a[88:91], v[188:191]\n\t"
    "ds_read_b64_tr_b16 a[238:239], v9 offset:25152\n\t"
    "v_mfma_f32_16x16x32_bf16 v[188:191], a[212:215], a[92:95], v[188:191]\n\t"
    "buffer_load_dwordx4 v3, s[44:47], s64 offen lds\n\t"
    "s_waitcnt lgkmcnt(8)\n\t"
    "v_mfma_f32_16x16x32_bf16 v[236:239], a[216:219], a[88:91], v[236:239]\n\t"
    "ds_read_b64_tr_b16 a[192:193], v9 offset:96\n\t"
    "v_mfma_f32_16x16x32_bf16 v[236:239], a[220:223], a[92:95], v[236:239]\n\t"
    "v_mfma_f32_16x16x32_bf16 v[232:235], a[216:219], a[80:83], v[232:235]\n\t"
    "ds_read_b64_tr_b16 a[194:195], v9 offset:3680\n\t"
    "v_mfma_f32_16x16x32_bf16 v[232:235], a[220:223], a[84:87], v[232:235]\n\t"
    "buffer_load_dwordx4 a[168:171], v7, s[40:43], s66 offen offset:1024 sc1\n\t"
    "v_mfma_f32_16x16x32_bf16 v[228:231], a[216:219], a[72:75], v[228:231]\n\t"
    "ds_read_b64_tr_b16 a[196:197], v9 offset:7264\n\t"
    "v_mfma_f32_16x16x32_bf16 v[228:231], a[220:223], a[76:79], v[228:231]\n\t"
    "s_add_u32 m0, s68, 129024\n\t"
    "v_mfma_f32_16x16x32_bf16 v[224:227], a[216:219], a[64:67], v[224:227]\n\t"
    "ds_read_b64_tr_b16 a[198:199], v9 offset:10848\n\t"
    "v_mfma_f32_16x16x32_bf16 v[224:227], a[220:223], a[68:71], v[224:227]\n\t"
    "buffer_load_dwordx4 v3, s[48:51], s64 offen lds\n\t"
    "s_waitcnt lgkmcnt(8)\n\t"
    "v_mfma_f32_16x16x32_bf16 v[160:163], a[224:227], a[64:67], v[160:163]\n\t"
    "ds_read_b64_tr_b16 a[200:201], v9 offset:14432\n\t"
    "v_mfma_f32_16x16x32_bf16 v[160:163], a[228:231], a[68:71], v[160:163]\n\t"
    "v_mfma_f32_16x16x32_bf16 v[164:167], a[224:227], a[72:75], v[164:167]\n\t"
    "ds_read_b64_tr_b16 a[202:203], v9 offset:18016\n\t"
    "v_mfma_f32_16x16x32_bf16 v[164:167], a[228:231], a[76:79], v[164:167]\n\t"
    "buffer_load_dwordx4 a[172:175], v8, s[40:43], s66 offen offset:1024 sc1\n\t"
    "v_mfma_f32_16x16x32_bf16 v[168:171], a[224:227], a[80:83], v[168:171]\n\t"
    "ds_read_b64_tr_b16 a[204:205], v9 offset:21600\n\t"
    "v_mfma_f32_16x16x32_bf16 v[168:171], a[228:231], a[84:87], v[168:171]\n\t"
    "s_add_u32 m0, s68, 118784\n\t"
    "v_mfma_f32_16x16x32_bf16 v[172:175], a[224:227], a[88:91], v[172:175]\n\t"
    "ds_read_b64_tr_b16 a[206:207], v9 offset:25184\n\t"
    "v_mfma_f32_16x16x32_bf16 v[172:175], a[228:231], a[92:95], v[172:175]\n\t"
    "buffer_load_dwordx4 v4, s[44:47], s64 offen lds\n\t"
    "s_waitcnt lgkmcnt(8)\n\t"
    "v_mfma_f32_16x16x32_bf16 v[252:255], a[232:235], a[88:91], v[252:255]\n\t"
    "ds_read_b64_tr_b16 a[208:209], v9 offset:128\n\t"
    "v_mfma_f32_16x16x32_bf16 v[252:255], a[236:239], a[92:95], v[252:255]\n\t"
    "buffer_load_dwordx4 a[176:179], v7, s[40:43], s66 offen offset:2048 sc1\n\t"
    "v_mfma_f32_16x16x32_bf16 v[248:251], a[232:235], a[80:83], v[248:251]\n\t"
    "ds_read_b64_tr_b16 a[210:211], v9 offset:3712\n\t"
    "v_mfma_f32_16x16x32_bf16 v[248:251], a[236:239], a[84:87], v[248:251]\n\t"
    "s_add_u32 m0, s68, 133120\n\t"
    "v_mfma_f32_16x16x32_bf16 v[244:247], a[232:235], a[72:75], v[244:247]\n\t"
    "ds_read_b64_tr_b16 a[212:213], v9 offset:7296\n\t"
    "v_mfma_f32_16x16x32_bf16 v[244:247], a[236:239], a[76:79], v[244:247]\n\t"
    "buffer_load_dwordx4 v4, s[48:51], s64 offen lds\n\t"
    "v_mfma_f32_16x16x32_bf16 v[240:243], a[232:235], a[64:67], v[240:243]\n\t"
    "ds_read_b64_tr_b16 a[214:215], v9 offset:10880\n\t"
    "v_mfma_f32_16x16x32_bf16 v[240:243], a[236:239], a[68:71], v[240:243]\n\t"
    "s_waitcnt lgkmcnt(8)\n\t"
    "v_mfma_f32_16x16x32_bf16 v[144:147], a[192:195], a[64:67], v[144:147]\n\t"
    "ds_read_b64_tr_b16 a[216:217], v9 offset:14464\n\t"
    "v_mfma_f32_16x16x32_bf16 v[144:147], a[196:199], a[68:71], v[144:147]\n\t"
    "buffer_load_dwordx4 a[180:183], v8, s[40:43], s66 offen offset:2048 sc1\n\t"
    "v_mfma_f32_16x16x32_bf16 v[148:151], a[192:195], a[72:75], v[148:151]\n\t"
    "ds_read_b64_tr_b16 a[218:219], v9 offset:18048\n\t"
    "v_mfma_f32_16x16x32_bf16 v[148:151], a[196:199], a[76:79], v[148:151]\n\t"
    "s_add_u32 m0, s69, 110592\n\t"
    "v_mfma_f32_16x16x32_bf16 v[152:155], a[192:195], a[80:83], v[152:155]\n\t"
    "ds_read_b64_tr_b16 a[220:221], v9 offset:21632\n\t"
    "v_mfma_f32_16x16x32_bf16 v[152:155], a[196:199], a[84:87], v[152:155]\n\t"
    "buffer_load_dwordx4 v5, s[52:55], s64 offen lds\n\t"
    "v_mfma_f32_16x16x32_bf16 v[156:159], a[192:195], a[88:91], v[156:159]\n\t"
    "ds_read_b64_tr_b16 a[222:223], v9 offset:25216\n\t"
    "v_mfma_f32_16x16x32_bf16 v[156:159], a[196:199], a[92:95], v[156:159]\n\t"
    "s_waitcnt lgkmcnt(8)\n\t"
    "v_mfma_f32_16x16x32_bf16 a[12:15], a[200:203], a[88:91], a[12:15]\n\t"
    "ds_read_b64_tr_b16 a[224:225], v9 offset:160\n\t"
    "v_mfma_f32_16x16x32_bf16 a[12:15], a[204:207], a[92:95], a[12:15]\n\t"
    "buffer_load_dwordx4 a[184:187], v7, s[40:43], s66 offen offset:3072 sc1\n\t"
    "v_mfma_f32_16x16x32_bf16 a[8:11], a[200:203], a[80:83], a[8:11]\n\t"
    "ds_read_b64_tr_b16 a[226:227], v9 offset:3744\n\t"
    "v_mfma_f32_16x16x32_bf16 a[8:11], a[204:207], a[84:87], a[8:11]\n\t"
    "s_add_u32 m0, s68, 139264\n\t"
    "v_mfma_f32_16x16x32_bf16 a[4:7], a[200:203], a[72:75], a[4:7]\n\t"
    "ds_read_b64_tr_b16 a[228:229], v9 offset:7328\n\t"
    "v_mfma_f32_16x16x32_bf16 a[4:7], a[204:207], a[76:79], a[4:7]\n\t"
    "buffer_load_dwordx4 v6, s[56:59], s65 offen lds\n\t"
    "v_mfma_f32_16x16x32_bf16 a[0:3], a[200:203], a[64:67], a[0:3]\n\t"
    "ds_read_b64_tr_b16 a[230:231], v9 offset:10912\n\t"
    "v_mfma_f32_16x16x32_bf16 a[0:3], a[204:207], a[68:71], a[0:3]\n\t"
    "s_waitcnt lgkmcnt(8)\n\t"
    "v_mfma_f32_16x16x32_bf16 v[128:131], a[208:211], a[64:67], v[128:131]\n\t"
    "ds_read_b64_tr_b16 a[232:233], v9 offset:14496\n\t"
    "v_mfma_f32_16x16x32_bf16 v[128:131], a[212:215], a[68:71], v[128:131]\n\t"
    "buffer_load_dwordx4 a[188:191], v8, s[40:43], s66 offen offset:3072 sc1\n\t"
    "v_mfma_f32_16x16x32_bf16 v[132:135], a[208:211], a[72:75], v[132:135]\n\t"
    "ds_read_b64_tr_b16 a[234:235], v9 offset:18080\n\t"
    "v_mfma_f32_16x16x32_bf16 v[132:135], a[212:215], a[76:79], v[132:135]\n\t"
    "s_add_u32 m0, s68, 143360\n\t"
    "v_mfma_f32_16x16x32_bf16 v[136:139], a[208:211], a[80:83], v[136:139]\n\t"
    "ds_read_b64_tr_b16 a[236:237], v9 offset:21664\n\t"
    "v_mfma_f32_16x16x32_bf16 v[136:139], a[212:215], a[84:87], v[136:139]\n\t"
    "buffer_load_dwordx4 v6, s[60:63], s65 offen lds\n\t"
    "v_mfma_f32_16x16x32_bf16 v[140:143], a[208:211], a[88:91], v[140:143]\n\t"
    "ds_read_b64_tr_b16 a[238:239], v9 offset:25248\n\t"
    "v_mfma_f32_16x16x32_bf16 v[140:143], a[212:215], a[92:95], v[140:143]\n\t"
    "s_waitcnt lgkmcnt(8)\n\t"
    "v_mfma_f32_16x16x32_bf16 a[28:31], a[216:219], a[88:91], a[28:31]\n\t"
    "ds_read_b64_tr_b16 a[192:193], v9 offset:192\n\t"
    "v_mfma_f32_16x16x32_bf16 a[28:31], a[220:223], a[92:95], a[28:31]\n\t"
    "v_mfma_f32_16x16x32_bf16 a[24:27], a[216:219], a[80:83], a[24:27]\n\t"
    "ds_read_b64_tr_b16 a[194:195], v9 offset:3776\n\t"
    "v_mfma_f32_16x16x32_bf16 a[24:27], a[220:223], a[84:87], a[24:27]\n\t"
    "v_mfma_f32_16x16x32_bf16 a[20:23], a[216:219], a[72:75], a[20:23]\n\t"
    "ds_read_b64_tr_b16 a[196:197], v9 offset:7360\n\t"
    "v_mfma_f32_16x16x32_bf16 a[20:23], a[220:223], a[76:79], a[20:23]\n\t"
    "v_mfma_f32_16x16x32_bf16 a[16:19], a[216:219], a[64:67], a[16:19]\n\t"
    "ds_read_b64_tr_b16 a[198:199], v9 offset:10944\n\t"
    "v_mfma_f32_16x16x32_bf16 a[16:19], a[220:223], a[68:71], a[16:19]\n\t"
    "s_lshr_b32 s95, s38, 3\n\t"
    "s_waitcnt lgkmcnt(8)\n\t"
    "v_mfma_f32_16x16x32_bf16 v[112:115], a[224:227], a[64:67], v[112:115]\n\t"
    "ds_read_b64_tr_b16 a[200:201], v9 offset:14528\n\t"
    "s_lshl_b32 s95, s95, 1\n\t"
    "v_mfma_f32_16x16x32_bf16 v[112:115], a[228:231], a[68:71], v[112:115]\n\t"
    "s_and_b32 s90, s87, 1\n\t"
    "v_mfma_f32_16x16x32_bf16 v[116:119], a[224:227], a[72:75], v[116:119]\n\t"
    "ds_read_b64_tr_b16 a[202:203], v9 offset:18112\n\t"
    "s_add_u32 s95, s95, s90\n\t"
    "v_mfma_f32_16x16x32_bf16 v[116:119], a[228:231], a[76:79], v[116:119]\n\t"
    "s_lshl_b32 s95, s95, 9\n\t"
    "v_mfma_f32_16x16x32_bf16 v[120:123], a[224:227], a[80:83], v[120:123]\n\t"
    "ds_read_b64_tr_b16 a[204:205], v9 offset:21696\n\t"
    "s_add_u32 s95, s95, 35704832\n\t"
    "v_mfma_f32_16x16x32_bf16 v[120:123], a[228:231], a[84:87], v[120:123]\n\t"
    "v_and_b32_e32 v30, 0x70, v31\n\t"
    "v_mfma_f32_16x16x32_bf16 v[124:127], a[224:227], a[88:91], v[124:127]\n\t"
    "ds_read_b64_tr_b16 a[206:207], v9 offset:25280\n\t"
    "v_lshlrev_b32_e32 v30, 2, v30\n\t"
    "v_mfma_f32_16x16x32_bf16 v[124:127], a[228:231], a[92:95], v[124:127]\n\t"
    "buffer_load_dwordx2 v[24:25], v30, s[16:19], s95 offen sc1\n\t"
    "s_waitcnt lgkmcnt(8)\n\t"
    "v_mfma_f32_16x16x32_bf16 a[44:47], a[232:235], a[88:91], a[44:47]\n\t"
    "ds_read_b128 a[208:211], v11 offset:0\n\t"
    "s_lshl_b32 s90, s87, 10\n\t"
    "v_mfma_f32_16x16x32_bf16 a[44:47], a[236:239], a[92:95], a[44:47]\n\t"
    "s_add_u32 s90, s90, 147456\n\t"
    "v_mfma_f32_16x16x32_bf16 a[40:43], a[232:235], a[80:83], a[40:43]\n\t"
    "ds_read_b128 a[212:215], v12 offset:0\n\t"
    "v_add_u32_e32 v15, s90, v31\n\t"
    "v_mfma_f32_16x16x32_bf16 a[40:43], a[236:239], a[84:87], a[40:43]\n\t"
    "v_mfma_f32_16x16x32_bf16 a[36:39], a[232:235], a[72:75], a[36:39]\n\t"
    "s_mov_b64 s[24:25], s[70:71]\n\t"
    "v_mfma_f32_16x16x32_bf16 a[36:39], a[236:239], a[76:79], a[36:39]\n\t"
    "s_mov_b32 s26, 7340032\n\t"
    "v_mfma_f32_16x16x32_bf16 a[32:35], a[232:235], a[64:67], a[32:35]\n\t"
    "s_lshr_b32 s28, %[bid], 3\n\t"
    "v_mfma_f32_16x16x32_bf16 a[32:35], a[236:239], a[68:71], a[32:35]\n\t"
    "s_lshl_b32 s28, s28, 2\n\t"
    "s_waitcnt lgkmcnt(6)\n\t"
    "v_mfma_f32_16x16x32_bf16 v[96:99], a[192:195], a[64:67], v[96:99]\n\t"
    "ds_read_b128 a[216:219], v11 offset:4096\n\t"
    "s_add_u32 s28, s28, s87\n\t"
    "v_mfma_f32_16x16x32_bf16 v[96:99], a[196:199], a[68:71], v[96:99]\n\t"
    "s_cmp_eq_u32 s73, 0\n\t"
    "v_mfma_f32_16x16x32_bf16 v[100:103], a[192:195], a[72:75], v[100:103]\n\t"
    "ds_read_b128 a[220:223], v12 offset:4096\n\t"
    "s_cselect_b32 s29, 12, 16\n\t"
    "v_mfma_f32_16x16x32_bf16 v[100:103], a[196:199], a[76:79], v[100:103]\n\t"
    "s_cselect_b32 s33, 2048, 0\n\t"
    "v_mfma_f32_16x16x32_bf16 v[104:107], a[192:195], a[80:83], v[104:107]\n\t"
    "s_mul_i32 s28, s28, s29\n\t"
    "v_mfma_f32_16x16x32_bf16 v[104:107], a[196:199], a[84:87], v[104:107]\n\t"
    "s_add_u32 s28, s28, s33\n\t"
    "v_mfma_f32_16x16x32_bf16 v[108:111], a[192:195], a[88:91], v[108:111]\n\t"
    "s_lshl_b32 s28, s28, 11\n\t"
    "v_mfma_f32_16x16x32_bf16 v[108:111], a[196:199], a[92:95], v[108:111]\n\t"
    "s_lshr_b32 s33, s70, 7\n\t"
    "s_waitcnt lgkmcnt(4)\n\t"
    "v_mfma_f32_16x16x32_bf16 a[60:63], a[200:203], a[88:91], a[60:63]\n\t"
    "ds_read_b128 a[224:227], v11 offset:2048\n\t"
    "s_sub_u32 s33, 3, s33\n\t"
    "v_mfma_f32_16x16x32_bf16 a[60:63], a[204:207], a[92:95], a[60:63]\n\t"
    "s_and_b32 s33, s33, 7\n\t"
    "v_mfma_f32_16x16x32_bf16 a[56:59], a[200:203], a[80:83], a[56:59]\n\t"
    "ds_read_b128 a[228:231], v12 offset:2048\n\t"
    "s_lshl_b32 s33, s33, 7\n\t"
    "v_mfma_f32_16x16x32_bf16 a[56:59], a[204:207], a[84:87], a[56:59]\n\t"
    "s_add_u32 s28, s28, s33\n\t"
    "v_mfma_f32_16x16x32_bf16 a[52:55], a[200:203], a[72:75], a[52:55]\n\t"
    "v_lshlrev_b32_e32 v0, 6, v31\n\t"
    "v_mfma_f32_16x16x32_bf16 a[52:55], a[204:207], a[76:79], a[52:55]\n\t"
    "s_lshl_b32 s29, s29, 11\n\t"
    "v_mfma_f32_16x16x32_bf16 a[48:51], a[200:203], a[64:67], a[48:51]\n\t"
    "s_sub_u32 s29, s29, 1\n\t"
    "v_mfma_f32_16x16x32_bf16 a[48:51], a[204:207], a[68:71], a[48:51]\n\t"
    "s_brev_b32 s33, 1\n\t"
    "s_waitcnt lgkmcnt(4)\n\t"
    "v_mfma_f32_16x16x32_bf16 v[32:35], a[64:67], a[208:211], v[32:35]\n\t"
    "ds_read_b128 a[232:235], v11 offset:6144\n\t"
    "v_sub_u32_e32 v28, s29, v0\n\t"
    "v_mfma_f32_16x16x32_bf16 v[32:35], a[68:71], a[212:215], v[32:35]\n\t"
    "v_and_or_b32 v0, v28, s33, v0\n\t"
    "v_mfma_f32_16x16x32_bf16 v[36:39], a[72:75], a[208:211], v[36:39]\n\t"
    "ds_read_b128 a[236:239], v12 offset:6144\n\t"
    "buffer_load_dword v28, v0, s[24:27], s28 offen\n\t"
    "v_mfma_f32_16x16x32_bf16 v[36:39], a[76:79], a[212:215], v[36:39]\n\t"
    "v_mfma_f32_16x16x32_bf16 v[40:43], a[80:83], a[208:211], v[40:43]\n\t"
    "v_mfma_f32_16x16x32_bf16 v[40:43], a[84:87], a[212:215], v[40:43]\n\t"
    "v_mfma_f32_16x16x32_bf16 v[44:47], a[88:91], a[208:211], v[44:47]\n\t"
    "v_mfma_f32_16x16x32_bf16 v[44:47], a[92:95], a[212:215], v[44:47]\n\t"
    "s_waitcnt lgkmcnt(4)\n\t"
    "v_mfma_f32_16x16x32_bf16 v[76:79], a[88:91], a[216:219], v[76:79]\n\t"
    "v_mfma_f32_16x16x32_bf16 v[76:79], a[92:95], a[220:223], v[76:79]\n\t"
    "v_mfma_f32_16x16x32_bf16 v[72:75], a[80:83], a[216:219], v[72:75]\n\t"
    "v_mfma_f32_16x16x32_bf16 v[72:75], a[84:87], a[220:223], v[72:75]\n\t"
    "v_mfma_f32_16x16x32_bf16 v[68:71], a[72:75], a[216:219], v[68:71]\n\t"
    "v_mfma_f32_16x16x32_bf16 v[68:71], a[76:79], a[220:223], v[68:71]\n\t"
    "v_mfma_f32_16x16x32_bf16 v[64:67], a[64:67], a[216:219], v[64:67]\n\t"
    "v_mfma_f32_16x16x32_bf16 v[64:67], a[68:71], a[220:223], v[64:67]\n\t"
    "s_waitcnt vmcnt(1) lgkmcnt(0)\n\t"
    "s_barrier\n\t"
    "s_mov_b32 s90, 0\n\t"
    "L_spX_%=:\n\t"
    "v_cmp_eq_u64_e32 vcc, s[82:83], v[24:25]\n\t"
    "s_cmp_eq_u64 vcc, -1\n\t"
    "s_cbranch_scc1 L_spXok_%=\n\t"
    "buffer_load_dwordx2 v[24:25], v30, s[16:19], s95 offen sc1\n\t"
    "s_add_u32 s90, s90, 1\n\t"
    "s_waitcnt vmcnt(0)\n\t"
    "s_cmp_lt_u32 s90, 0x40000\n\t"
    "s_cbranch_scc1 L_spX_%=\n\t"
    "L_spXok_%=:\n\t"
    "ds_read_b128 v[24:27], v15\n\t"
    "v_mfma_f32_16x16x32_bf16 v[48:51], a[64:67], a[224:227], v[48:51]\n\t"
    "ds_read_b64_tr_b16 a[192:193], v9 offset:36864\n\t"
    "v_mfma_f32_16x16x32_bf16 v[48:51], a[68:71], a[228:231], v[48:51]\n\t"
    "ds_read_b64_tr_b16 a[194:195], v9 offset:40448\n\t"
    "v_mfma_f32_16x16x32_bf16 v[52:55], a[72:75], a[224:227], v[52:55]\n\t"
    "ds_read_b64_tr_b16 a[196:197], v9 offset:44032\n\t"
    "v_mfma_f32_16x16x32_bf16 v[52:55], a[76:79], a[228:231], v[52:55]\n\t"
    "ds_read_b64_tr_b16 a[198:199], v9 offset:47616\n\t"
    "v_mfma_f32_16x16x32_bf16 v[56:59], a[80:83], a[224:227], v[56:59]\n\t"
    "ds_read_b64_tr_b16 a[200:201], v10 offset:0\n\t"
    "v_mfma_f32_16x16x32_bf16 v[56:59], a[84:87], a[228:231], v[56:59]\n\t"
    "ds_read_b64_tr_b16 a[202:203], v10 offset:3584\n\t"
    "v_mfma_f32_16x16x32_bf16 v[60:63], a[88:91], a[224:227], v[60:63]\n\t"
    "ds_read_b64_tr_b16 a[204:205], v10 offset:7168\n\t"
    "v_mfma_f32_16x16x32_bf16 v[60:63], a[92:95], a[228:231], v[60:63]\n\t"
    "ds_read_b64_tr_b16 a[206:207], v10 offset:10752\n\t"
    "v_mfma_f32_16x16x32_bf16 v[92:95], a[88:91], a[232:235], v[92:95]\n\t"
    "ds_read_b64_tr_b16 a[208:209], v10 offset:36864\n\t"
    "v_mfma_f32_16x16x32_bf16 v[92:95], a[92:95], a[236:239], v[92:95]\n\t"
    "ds_read_b64_tr_b16 a[210:211], v10 offset:40448\n\t"
    "v_mfma_f32_16x16x32_bf16 v[88:91], a[80:83], a[232:235], v[88:91]\n\t"
    "ds_read_b64_tr_b16 a[212:213], v10 offset:44032\n\t"
    "v_mfma_f32_16x16x32_bf16 v[88:91], a[84:87], a[236:239], v[88:91]\n\t"
    "ds_read_b64_tr_b16 a[214:215], v10 offset:47616\n\t"
    "v_mfma_f32_16x16x32_bf16 v[84:87], a[72:75], a[232:235], v[84:87]\n\t"
    "v_mfma_f32_16x16x32_bf16 v[84:87], a[76:79], a[236:239], v[84:87]\n\t"
    "v_mfma_f32_16x16x32_bf16 v[80:83], a[64:67], a[232:235], v[80:83]\n\t"
    "v_mfma_f32_16x16x32_bf16 v[80:83], a[68:71], a[236:239], v[80:83]\n\t"
    "s_waitcnt lgkmcnt(8)\n\t"
    "v_mfma_f32_16x16x32_bf16 v[192:195], a[192:195], a[96:99], v[192:195]\n\t"
    "s_mov_b64 s[44:45], s[70:71]\n\t"
    "ds_read_b64_tr_b16 a[216:217], v9 offset:51200\n\t"
    "v_mfma_f32_16x16x32_bf16 v[192:195], a[196:199], a[100:103], v[192:195]\n\t"
    "s_mov_b32 s46, 7340032\n\t"
    "ds_read_b64_tr_b16 a[218:219], v9 offset:54784\n\t"
    "s_waitcnt lgkmcnt(6)\n\t"
    "v_mfma_f32_16x16x32_bf16 v[192:195], a[200:203], a[128:131], v[192:195]\n\t"
    "s_mov_b64 s[48:49], s[78:79]\n\t"
    "v_mfma_f32_16x16x32_bf16 v[192:195], a[204:207], a[132:135], v[192:195]\n\t"
    "s_mov_b32 s50, 2097152\n\t"
    "ds_read_b64_tr_b16 a[220:221], v9 offset:58368\n\t"
    "s_waitcnt lgkmcnt(3)\n\t"
    "v_mfma_f32_16x16x32_bf16 v[192:195], a[208:211], a[160:163], v[192:195]\n\t"
    "s_mov_b64 s[52:53], s[80:81]\n\t"
    "ds_read_b64_tr_b16 a[222:223], v9 offset:61952\n\t"
    "v_mfma_f32_16x16x32_bf16 v[192:195], a[212:215], a[164:167], v[192:195]\n\t"
    "s_mov_b32 s54, 2097152\n\t"
    "v_mfma_f32_16x16x32_bf16 v[196:199], a[192:195], a[104:107], v[196:199]\n\t"
    "s_add_u32 s40, s16, 4194304\n\t"
    "ds_read_b64_tr_b16 a[224:225], v10 offset:14336\n\t"
    "v_mfma_f32_16x16x32_bf16 v[196:199], a[196:199], a[108:111], v[196:199]\n\t"
    "s_addc_u32 s41, s17, 0\n\t"
    "s_mov_b32 s94, 37748736\n\t"
    "ds_read_b64_tr_b16 a[226:227], v10 offset:17920\n\t"
    "v_mfma_f32_16x16x32_bf16 v[196:199], a[200:203], a[136:139], v[196:199]\n\t"
    "s_mov_b32 s42, 23068672\n\t"
    "buffer_load_dwordx2 a[64:65], v27, s[16:19], s94 offen offset:0 sc1\n\t"
    "v_mfma_f32_16x16x32_bf16 v[196:199], a[204:207], a[140:143], v[196:199]\n\t"
    "s_mov_b64 s[24:25], s[30:31]\n\t"
    "ds_read_b64_tr_b16 a[228:229], v10 offset:21504\n\t"
    "v_mfma_f32_16x16x32_bf16 v[196:199], a[208:211], a[168:171], v[196:199]\n\t"
    "s_mov_b32 s26, 2048\n\t"
    "ds_read_b64_tr_b16 a[230:231], v10 offset:25088\n\t"
    "v_mfma_f32_16x16x32_bf16 v[196:199], a[212:215], a[172:175], v[196:199]\n\t"
    "s_mov_b64 s[56:57], s[34:35]\n\t"
    "buffer_load_dwordx2 a[66:67], v27, s[16:19], s94 offen offset:1024 sc1\n\t"
    "v_mfma_f32_16x16x32_bf16 v[200:203], a[192:195], a[112:115], v[200:203]\n\t"
    "s_mov_b32 s58, 8388608\n\t"
    "ds_read_b64_tr_b16 a[232:233], v10 offset:51200\n\t"
    "v_mfma_f32_16x16x32_bf16 v[200:203], a[196:199], a[116:119], v[200:203]\n\t"
    "s_mul_i32 s68, s87, 4224\n\t"
    "ds_read_b64_tr_b16 a[234:235], v10 offset:54784\n\t"
    "v_mfma_f32_16x16x32_bf16 v[200:203], a[200:203], a[144:147], v[200:203]\n\t"
    "s_add_u32 s69, s68, 1056\n\t"
    "buffer_load_dwordx2 a[68:69], v27, s[16:19], s94 offen offset:2048 sc1\n\t"
    "v_mfma_f32_16x16x32_bf16 v[200:203], a[204:207], a[148:151], v[200:203]\n\t"
    "s_add_u32 s70, s68, 2112\n\t"
    "ds_read_b64_tr_b16 a[236:237], v10 offset:58368\n\t"
    "v_mfma_f32_16x16x32_bf16 v[200:203], a[208:211], a[176:179], v[200:203]\n\t"
    "s_add_u32 s71, s68, 3168\n\t"
    "ds_read_b64_tr_b16 a[238:239], v10 offset:61952\n\t"
    "v_mfma_f32_16x16x32_bf16 v[200:203], a[212:215], a[180:183], v[200:203]\n\t"
    "s_lshl_b32 s78, s87, 12\n\t"
    "buffer_load_dwordx2 a[70:71], v27, s[16:19], s94 offen offset:3072 sc1\n\t"
    "v_mfma_f32_16x16x32_bf16 v[204:207], a[192:195], a[120:123], v[204:207]\n\t"
    "v_mfma_f32_16x16x32_bf16 v[204:207], a[196:199], a[124:127], v[204:207]\n\t"
    "s_add_u32 s79, s78, 1024\n\t"
    "v_mfma_f32_16x16x32_bf16 v[204:207], a[200:203], a[152:155], v[204:207]\n\t"
    "s_add_u32 s80, s78, 2048\n\t"
    "buffer_load_dwordx2 a[72:73], v27, s[16:19], s94 offen offset:512 sc1\n\t"
    "v_mfma_f32_16x16x32_bf16 v[204:207], a[204:207], a[156:159], v[204:207]\n\t"
    "s_add_u32 s81, s78, 3072\n\t"
    "v_mfma_f32_16x16x32_bf16 v[204:207], a[208:211], a[184:187], v[204:207]\n\t"
    "v_mfma_f32_16x16x32_bf16 v[204:207], a[212:215], a[188:191], v[204:207]\n\t"
    "s_cmp_eq_u32 s73, 0\n\t"
    "buffer_load_dwordx2 a[74:75], v27, s[16:19], s94 offen offset:1536 sc1\n\t"
    "s_waitcnt lgkmcnt(8)\n\t"
    "v_mfma_f32_16x16x32_bf16 v[208:211], a[216:219], a[96:99], v[208:211]\n\t"
    "s_cselect_b32 s64, 32, 0\n\t"
    "ds_read_b64_tr_b16 a[192:193], v9 offset:36896\n\t"
    "v_mfma_f32_16x16x32_bf16 v[208:211], a[220:223], a[100:103], v[208:211]\n\t"
    "s_cselect_b32 s75, 2, 3\n\t"
    "ds_read_b64_tr_b16 a[194:195], v9 offset:40480\n\t"
    "s_waitcnt lgkmcnt(6)\n\t"
    "v_mfma_f32_16x16x32_bf16 v[208:211], a[224:227], a[128:131], v[208:211]\n\t"
    "buffer_load_dwordx2 a[76:77], v27, s[16:19], s94 offen offset:2560 sc1\n\t"
    "v_mfma_f32_16x16x32_bf16 v[208:211], a[228:231], a[132:135], v[208:211]\n\t"
    "s_cselect_b32 s76, 3, 4\n\t"
    "ds_read_b64_tr_b16 a[196:197], v9 offset:44064\n\t"
    "s_waitcnt lgkmcnt(3)\n\t"
    "v_mfma_f32_16x16x32_bf16 v[208:211], a[232:235], a[160:163], v[208:211]\n\t"
    "s_lshr_b32 s90, s87, 1\n\t"
    "ds_read_b64_tr_b16 a[198:199], v9 offset:47648\n\t"
    "v_mfma_f32_16x16x32_bf16 v[208:211], a[236:239], a[164:167], v[208:211]\n\t"
    "buffer_load_dwordx2 a[78:79], v27, s[16:19], s94 offen offset:3584 sc1\n\t"
    "v_mfma_f32_16x16x32_bf16 v[212:215], a[216:219], a[104:107], v[212:215]\n\t"
    "s_add_u32 s66, s64, s90\n\t"
    "ds_read_b64_tr_b16 a[200:201], v10 offset:32\n\t"
    "v_mfma_f32_16x16x32_bf16 v[212:215], a[220:223], a[108:111], v[212:215]\n\t"
    "s_lshl_b32 s66, s66, 18\n\t"
    "ds_read_b64_tr_b16 a[202:203], v10 offset:3616\n\t"
    "v_mfma_f32_16x16x32_bf16 v[212:215], a[224:227], a[136:139], v[212:215]\n\t"
    "s_lshl_b32 s64, s64, 17\n\t"
    "buffer_load_dwordx2 a[80:81], v27, s[16:19], 0 offen offset:0 sc1\n\t"
    "v_mfma_f32_16x16x32_bf16 v[212:215], a[228:231], a[140:143], v[212:215]\n\t"
    "ds_read_b64_tr_b16 a[204:205], v10 offset:7200\n\t"
    "v_mfma_f32_16x16x32_bf16 v[212:215], a[232:235], a[168:171], v[212:215]\n\t"
    "s_add_u32 s65, s64, 131072\n\t"
    "ds_read_b64_tr_b16 a[206:207], v10 offset:10784\n\t"
    "v_mfma_f32_16x16x32_bf16 v[212:215], a[236:239], a[172:175], v[212:215]\n\t"
    "s_mov_b32 s72, 262144\n\t"
    "buffer_load_dwordx2 a[82:83], v27, s[16:19], 0 offen offset:1024 sc1\n\t"
    "v_mfma_f32_16x16x32_bf16 v[216:219], a[216:219], a[112:115], v[216:219]\n\t"
    "s_mov_b32 s74, 0\n\t"
    "ds_read_b64_tr_b16 a[208:209], v10 offset:36896\n\t"
    "v_mfma_f32_16x16x32_bf16 v[216:219], a[220:223], a[116:119], v[216:219]\n\t"
    "ds_read_b64_tr_b16 a[210:211], v10 offset:40480\n\t"
    "v_mfma_f32_16x16x32_bf16 v[216:219], a[224:227], a[144:147], v[216:219]\n\t"
    "buffer_load_dwordx2 a[84:85], v27, s[16:19], 0 offen offset:2048 sc1\n\t"
    "v_mov_b32_e32 v2, v16\n\t"
    "v_mfma_f32_16x16x32_bf16 v[216:219], a[228:231], a[148:151], v[216:219]\n\t"
    "ds_read_b64_tr_b16 a[212:213], v10 offset:44064\n\t"
    "v_add_u32_e32 v3, 0x800, v16\n\t"
    "v_mfma_f32_16x16x32_bf16 v[216:219], a[232:235], a[176:179], v[216:219]\n\t"
    "ds_read_b64_tr_b16 a[214:215], v10 offset:47648\n\t"
    "v_add_u32_e32 v4, 0x1000, v16\n\t"
    "v_mfma_f32_16x16x32_bf16 v[216:219], a[236:239], a[180:183], v[216:219]\n\t"
    "buffer_load_dwordx2 a[86:87], v27, s[16:19], 0 offen offset:3072 sc1\n\t"
    "v_mfma_f32_16x16x32_bf16 v[220:223], a[216:219], a[120:123], v[220:223]\n\t"
    "v_add_u32_e32 v5, 0x1800, v16\n\t"
    "v_mfma_f32_16x16x32_bf16 v[220:223], a[220:223], a[124:127], v[220:223]\n\t"
    "v_add_u32_e32 v6, 0x20000, v18\n\t"
    "v_mfma_f32_16x16x32_bf16 v[220:223], a[224:227], a[152:155], v[220:223]\n\t"
    "buffer_load_dwordx2 a[88:89], v27, s[16:19], 0 offen offset:512 sc1\n\t"
    "v_mfma_f32_16x16x32_bf16 v[220:223], a[228:231], a[156:159], v[220:223]\n\t"
    "v_mfma_f32_16x16x32_bf16 v[220:223], a[232:235], a[184:187], v[220:223]\n\t"
    "v_mfma_f32_16x16x32_bf16 v[220:223], a[236:239], a[188:191], v[220:223]\n\t"
    "buffer_load_dwordx2 a[90:91], v27, s[16:19], 0 offen offset:1536 sc1\n\t"
    "s_waitcnt lgkmcnt(8)\n\t"
    "v_mfma_f32_16x16x32_bf16 v[176:179], a[192:195], a[96:99], v[176:179]\n\t"
    "ds_read_b64_tr_b16 a[216:217], v9 offset:51232\n\t"
    "v_mfma_f32_16x16x32_bf16 v[176:179], a[196:199], a[100:103], v[176:179]\n\t"
    "ds_read_b64_tr_b16 a[218:219], v9 offset:54816\n\t"
    "s_waitcnt lgkmcnt(6)\n\t"
    "v_mfma_f32_16x16x32_bf16 v[176:179], a[200:203], a[128:131], v[176:179]\n\t"
    "buffer_load_dwordx2 a[92:93], v27, s[16:19], 0 offen offset:2560 sc1\n\t"
    "v_mul_f32_e32 v192, s92, v192\n\t"
    "v_mul_f32_e32 v193, s92, v193\n\t"
    "v_mul_f32_e32 v194, s92, v194\n\t"
    "v_mfma_f32_16x16x32_bf16 v[176:179], a[204:207], a[132:135], v[176:179]\n\t"
    "ds_read_b64_tr_b16 a[220:221], v9 offset:58400\n\t"
    "v_mul_f32_e32 v195, s92, v195\n\t"
    "v_mul_f32_e32 v196, s92, v196\n\t"
    "v_mul_f32_e32 v197, s92, v197\n\t"
    "s_waitcnt lgkmcnt(3)\n\t"
    "v_mfma_f32_16x16x32_bf16 v[176:179], a[208:211], a[160:163], v[176:179]\n\t"
    "ds_read_b64_tr_b16 a[222:223], v9 offset:61984\n\t"
    "v_mul_f32_e32 v198, s92, v198\n\t"
    "v_mul_f32_e32 v199, s92, v199\n\t"
    "v_mfma_f32_16x16x32_bf16 v[176:179], a[212:215], a[164:167], v[176:179]\n\t"
    "buffer_load_dwordx2 a[94:95], v27, s[16:19], 0 offen offset:3584 sc1\n\t"
    "v_exp_f32_e32 v192, v192\n\t"
    "v_exp_f32_e32 v193, v193\n\t"
    "v_mfma_f32_16x16x32_bf16 v[180:183], a[192:195], a[104:107], v[180:183]\n\t"
    "ds_read_b64_tr_b16 a[224:225], v10 offset:14368\n\t"
    "v_exp_f32_e32 v194, v194\n\t"
    "v_mfma_f32_16x16x32_bf16 v[180:183], a[196:199], a[108:111], v[180:183]\n\t"
    "s_add_u32 m0, s68, 0\n\t"
    "ds_read_b64_tr_b16 a[226:227], v10 offset:17952\n\t"
    "v_exp_f32_e32 v195, v195\n\t"
    "v_exp_f32_e32 v196, v196\n\t"
    "v_mfma_f32_16x16x32_bf16 v[180:183], a[200:203], a[136:139], v[180:183]\n\t"
    "buffer_load_dwordx4 v2, s[44:47], s64 offen lds\n\t"
    "v_exp_f32_e32 v197, v197\n\t"
    "v_mfma_f32_16x16x32_bf16 v[180:183], a[204:207], a[140:143], v[180:183]\n\t"
    "ds_read_b64_tr_b16 a[228:229], v10 offset:21536\n\t"
    "v_exp_f32_e32 v198, v198\n\t"
    "v_exp_f32_e32 v199, v199\n\t"
    "v_mfma_f32_16x16x32_bf16 v[180:183], a[208:211], a[168:171], v[180:183]\n\t"
    "ds_read_b64_tr_b16 a[230:231], v10 offset:25120\n\t"
    "v_add_f32_e32 v192, 1.0, v192\n\t"
    "v_add_f32_e32 v193, 1.0, v193\n\t"
    "v_add_f32_e32 v194, 1.0, v194\n\t"
    "v_mfma_f32_16x16x32_bf16 v[180:183], a[212:215], a[172:175], v[180:183]\n\t"
    "v_add_f32_e32 v195, 1.0, v195\n\t"
    "v_add_f32_e32 v196, 1.0, v196\n\t"
    "v_add_f32_e32 v197, 1.0, v197\n\t"
    "v_add_f32_e32 v198, 1.0, v198\n\t"
    "v_mfma_f32_16x16x32_bf16 v[184:187], a[192:195], a[112:115], v[184:187]\n\t"
    "ds_read_b64_tr_b16 a[232:233], v10 offset:51232\n\t"
    "v_add_f32_e32 v199, 1.0, v199\n\t"
    "v_rcp_f32_e32 v192, v192\n\t"
    "v_mfma_f32_16x16x32_bf16 v[184:187], a[196:199], a[116:119], v[184:187]\n\t"
    "s_add_u32 m0, s69, 0\n\t"
    "ds_read_b64_tr_b16 a[234:235], v10 offset:54816\n\t"
    "v_rcp_f32_e32 v193, v193\n\t"
    "v_mfma_f32_16x16x32_bf16 v[184:187], a[200:203], a[144:147], v[184:187]\n\t"
    "buffer_load_dwordx4 v3, s[44:47], s64 offen lds\n\t"
    "v_rcp_f32_e32 v194, v194\n\t"
    "v_rcp_f32_e32 v195, v195\n\t"
    "v_mfma_f32_16x16x32_bf16 v[184:187], a[204:207], a[148:151], v[184:187]\n\t"
    "ds_read_b64_tr_b16 a[236:237], v10 offset:58400\n\t"
    "v_rcp_f32_e32 v196, v196\n\t"
    "v_mfma_f32_16x16x32_bf16 v[184:187], a[208:211], a[176:179], v[184:187]\n\t"
    "ds_read_b64_tr_b16 a[238:239], v10 offset:61984\n\t"
    "v_rcp_f32_e32 v197, v197\n\t"
    "v_rcp_f32_e32 v198, v198\n\t"
    "v_mfma_f32_16x16x32_bf16 v[184:187], a[212:215], a[180:183], v[184:187]\n\t"
    "v_rcp_f32_e32 v199, v199\n\t"
    "v_mul_f32_e32 v192, v192, v208\n\t"
    "v_mul_f32_e32 v193, v193, v209\n\t"
    "v_mfma_f32_16x16x32_bf16 v[188:191], a[192:195], a[120:123], v[188:191]\n\t"
    "v_mul_f32_e32 v194, v194, v210\n\t"
    "v_mul_f32_e32 v195, v195, v211\n\t"
    "v_mul_f32_e32 v196, v196, v212\n\t"
    "v_mul_f32_e32 v197, v197, v213\n\t"
    "v_mfma_f32_16x16x32_bf16 v[188:191], a[196:199], a[124:127], v[188:191]\n\t"
    "s_add_u32 m0, s70, 0\n\t"
    "v_mul_f32_e32 v198, v198, v214\n\t"
    "v_mul_f32_e32 v199, v199, v215\n\t"
    "v_cvt_pk_bf16_f32 v192, v192, v193\n\t"
    "v_cvt_pk_bf16_f32 v193, v194, v195\n\t"
    "v_mfma_f32_16x16x32_bf16 v[188:191], a[200:203], a[152:155], v[188:191]\n\t"
    "buffer_load_dwordx4 v4, s[44:47], s64 offen lds\n\t"
    "v_cvt_pk_bf16_f32 v194, v196, v197\n\t"
    "v_cvt_pk_bf16_f32 v195, v198, v199\n\t"
    "v_mfma_f32_16x16x32_bf16 v[188:191], a[204:207], a[156:159], v[188:191]\n\t"
    "v_permlane16_swap_b32_e32 v192, v194\n\t"
    "v_permlane16_swap_b32_e32 v193, v195\n\t"
    "v_mfma_f32_16x16x32_bf16 v[188:191], a[208:211], a[184:187], v[188:191]\n\t"
    "buffer_store_dwordx4 v[192:195], v22, s[16:19], s96 offen offset:0 sc1\n\t"
    "v_mul_f32_e32 v200, s92, v200\n\t"
    "v_mul_f32_e32 v201, s92, v201\n\t"
    "v_mfma_f32_16x16x32_bf16 v[188:191], a[212:215], a[188:191], v[188:191]\n\t"
    "v_mul_f32_e32 v202, s92, v202\n\t"
    "v_mul_f32_e32 v203, s92, v203\n\t"
    "v_mul_f32_e32 v204, s92, v204\n\t"
    "v_mul_f32_e32 v205, s92, v205\n\t"
    "s_waitcnt lgkmcnt(8)\n\t"
    "v_mfma_f32_16x16x32_bf16 v[224:227], a[216:219], a[96:99], v[224:227]\n\t"
    "ds_read_b64_tr_b16 a[192:193], v9 offset:36928\n\t"
    "v_mul_f32_e32 v206, s92, v206\n\t"
    "v_mul_f32_e32 v207, s92, v207\n\t"
    "v_mfma_f32_16x16x32_bf16 v[224:227], a[220:223], a[100:103], v[224:227]\n\t"
    "s_add_u32 m0, s71, 0\n\t"
    "ds_read_b64_tr_b16 a[194:195], v9 offset:40512\n\t"
    "v_exp_f32_e32 v200, v200\n\t"
    "v_exp_f32_e32 v201, v201\n\t"
    "s_waitcnt lgkmcnt(6)\n\t"
    "v_mfma_f32_16x16x32_bf16 v[224:227], a[224:227], a[128:131], v[224:227]\n\t"
    "buffer_load_dwordx4 v5, s[44:47], s64 offen lds\n\t"
    "v_exp_f32_e32 v202, v202\n\t"
    "v_mfma_f32_16x16x32_bf16 v[224:227], a[228:231], a[132:135], v[224:227]\n\t"
    "ds_read_b64_tr_b16 a[196:197], v9 offset:44096\n\t"
    "v_exp_f32_e32 v203, v203\n\t"
    "v_exp_f32_e32 v204, v204\n\t"
    "s_waitcnt lgkmcnt(3)\n\t"
    "v_mfma_f32_16x16x32_bf16 v[224:227], a[232:235], a[160:163], v[224:227]\n\t"
    "ds_read_b64_tr_b16 a[198:199], v9 offset:47680\n\t"
    "v_exp_f32_e32 v205, v205\n\t"
    "v_mfma_f32_16x16x32_bf16 v[224:227], a[236:239], a[164:167], v[224:227]\n\t"
    "v_exp_f32_e32 v206, v206\n\t"
    "v_exp_f32_e32 v207, v207\n\t"
    "v_add_f32_e32 v200, 1.0, v200\n\t"
    "v_mfma_f32_16x16x32_bf16 v[228:231], a[216:219], a[104:107], v[228:231]\n\t"
    "ds_read_b64_tr_b16 a[200:201], v10 offset:64\n\t"
    "v_add_f32_e32 v201, 1.0, v201\n\t"
    "v_add_f32_e32 v202, 1.0, v202\n\t"
    "v_add_f32_e32 v203, 1.0, v203\n\t"
    "v_mfma_f32_16x16x32_bf16 v[228:231], a[220:223], a[108:111], v[228:231]\n\t"
    "s_add_u32 m0, s68, 16896\n\t"
    "ds_read_b64_tr_b16 a[202:203], v10 offset:3648\n\t"
    "v_add_f32_e32 v204, 1.0, v204\n\t"
    "v_add_f32_e32 v205, 1.0, v205\n\t"
    "v_add_f32_e32 v206, 1.0, v206\n\t"
    "v_mfma_f32_16x16x32_bf16 v[228:231], a[224:227], a[136:139], v[228:231]\n\t"
    "buffer_load_dwordx4 v2, s[44:47], s65 offen lds\n\t"
    "v_add_f32_e32 v207, 1.0, v207\n\t"
    "v_rcp_f32_e32 v200, v200\n\t"
    "v_mfma_f32_16x16x32_bf16 v[228:231], a[228:231], a[140:143], v[228:231]\n\t"
    "ds_read_b64_tr_b16 a[204:205], v10 offset:7232\n\t"
    "v_rcp_f32_e32 v201, v201\n\t"
    "v_mfma_f32_16x16x32_bf16 v[228:231], a[232:235], a[168:171], v[228:231]\n\t"
    "ds_read_b64_tr_b16 a[206:207], v10 offset:10816\n\t"
    "v_rcp_f32_e32 v202, v202\n\t"
    "v_rcp_f32_e32 v203, v203\n\t"
    "v_mfma_f32_16x16x32_bf16 v[228:231], a[236:239], a[172:175], v[228:231]\n\t"
    "v_rcp_f32_e32 v204, v204\n\t"
    "v_mfma_f32_16x16x32_bf16 v[232:235], a[216:219], a[112:115], v[232:235]\n\t"
    "ds_read_b64_tr_b16 a[208:209], v10 offset:36928\n\t"
    "v_rcp_f32_e32 v205, v205\n\t"
    "v_mfma_f32_16x16x32_bf16 v[232:235], a[220:223], a[116:119], v[232:235]\n\t"
    "s_add_u32 m0, s69, 16896\n\t"
    "ds_read_b64_tr_b16 a[210:211], v10 offset:40512\n\t"
    "v_rcp_f32_e32 v206, v206\n\t"
    "v_mfma_f32_16x16x32_bf16 v[232:235], a[224:227], a[144:147], v[232:235]\n\t"
    "buffer_load_dwordx4 v3, s[44:47], s65 offen lds\n\t"
    "v_rcp_f32_e32 v207, v207\n\t"
    "v_mul_f32_e32 v200, v200, v216\n\t"
    "v_mfma_f32_16x16x32_bf16 v[232:235], a[228:231], a[148:151], v[232:235]\n\t"
    "ds_read_b64_tr_b16 a[212:213], v10 offset:44096\n\t"
    "v_mul_f32_e32 v201, v201, v217\n\t"
    "v_mul_f32_e32 v202, v202, v218\n\t"
    "v_mfma_f32_16x16x32_bf16 v[232:235], a[232:235], a[176:179], v[232:235]\n\t"
    "ds_read_b64_tr_b16 a[214:215], v10 offset:47680\n\t"
    "v_mul_f32_e32 v203, v203, v219\n\t"
    "v_mul_f32_e32 v204, v204, v220\n\t"
    "v_mfma_f32_16x16x32_bf16 v[232:235], a[236:239], a[180:183], v[232:235]\n\t"
    "v_mul_f32_e32 v205, v205, v221\n\t"
    "v_mul_f32_e32 v206, v206, v222\n\t"
    "v_mul_f32_e32 v207, v207, v223\n\t"
    "v_mfma_f32_16x16x32_bf16 v[236:239], a[216:219], a[120:123], v[236:239]\n\t"
    "v_cvt_pk_bf16_f32 v200, v200, v201\n\t"
    "v_cvt_pk_bf16_f32 v201, v202, v203\n\t"
    "v_cvt_pk_bf16_f32 v202, v204, v205\n\t"
    "v_mfma_f32_16x16x32_bf16 v[236:239], a[220:223], a[124:127], v[236:239]\n\t"
    "s_add_u32 m0, s70, 16896\n\t"
    "v_cvt_pk_bf16_f32 v203, v206, v207\n\t"
    "v_permlane16_swap_b32_e32 v200, v202\n\t"
    "v_mfma_f32_16x16x32_bf16 v[236:239], a[224:227], a[152:155], v[236:239]\n\t"
    "buffer_load_dwordx4 v4, s[44:47], s65 offen lds\n\t"
    "v_permlane16_swap_b32_e32 v201, v203\n\t"
    "v_mfma_f32_16x16x32_bf16 v[236:239], a[228:231], a[156:159], v[236:239]\n\t"
    "buffer_store_dwordx4 v[200:203], v22, s[16:19], s96 offen offset:2048 sc1\n\t"
    "s_add_u32 s96, s96, s97\n\t"
    "v_mfma_f32_16x16x32_bf16 v[236:239], a[232:235], a[184:187], v[236:239]\n\t"
    "v_mfma_f32_16x16x32_bf16 v[236:239], a[236:239], a[188:191], v[236:239]\n\t"
    "s_waitcnt lgkmcnt(8)\n\t"
    "v_mfma_f32_16x16x32_bf16 v[160:163], a[192:195], a[96:99], v[160:163]\n\t"
    "ds_read_b64_tr_b16 a[216:217], v9 offset:51264\n\t"
    "v_mfma_f32_16x16x32_bf16 v[160:163], a[196:199], a[100:103], v[160:163]\n\t"
    "s_add_u32 m0, s71, 16896\n\t"
    "ds_read_b64_tr_b16 a[218:219], v9 offset:54848\n\t"
    "s_waitcnt lgkmcnt(6)\n\t"
    "v_mfma_f32_16x16x32_bf16 v[160:163], a[200:203], a[128:131], v[160:163]\n\t"
    "buffer_load_dwordx4 v5, s[44:47], s65 offen lds\n\t"
    "v_mul_f32_e32 v176, s92, v176\n\t"
    "v_mul_f32_e32 v177, s92, v177\n\t"
    "v_mul_f32_e32 v178, s92, v178\n\t"
    "v_mfma_f32_16x16x32_bf16 v[160:163], a[204:207], a[132:135], v[160:163]\n\t"
    "ds_read_b64_tr_b16 a[220:221], v9 offset:58432\n\t"
    "v_mul_f32_e32 v179, s92, v179\n\t"
    "v_mul_f32_e32 v180, s92, v180\n\t"
    "v_mul_f32_e32 v181, s92, v181\n\t"
    "s_waitcnt lgkmcnt(3)\n\t"
    "v_mfma_f32_16x16x32_bf16 v[160:163], a[208:211], a[160:163], v[160:163]\n\t"
    "ds_read_b64_tr_b16 a[222:223], v9 offset:62016\n\t"
    "v_mul_f32_e32 v182, s92, v182\n\t"
    "v_mul_f32_e32 v183, s92, v183\n\t"
    "v_mfma_f32_16x16x32_bf16 v[160:163], a[212:215], a[164:167], v[160:163]\n\t"
    "v_exp_f32_e32 v176, v176\n\t"
    "v_exp_f32_e32 v177, v177\n\t"
    "v_mfma_f32_16x16x32_bf16 v[164:167], a[192:195], a[104:107], v[164:167]\n\t"
    "ds_read_b64_tr_b16 a[224:225], v10 offset:14400\n\t"
    "v_exp_f32_e32 v178, v178\n\t"
    "v_exp_f32_e32 v179, v179\n\t"
    "v_mfma_f32_16x16x32_bf16 v[164:167], a[196:199], a[108:111], v[164:167]\n\t"
    "ds_read_b64_tr_b16 a[226:227], v10 offset:17984\n\t"
    "v_exp_f32_e32 v180, v180\n\t"
    "v_mfma_f32_16x16x32_bf16 v[164:167], a[200:203], a[136:139], v[164:167]\n\t"
    "s_add_u32 s64, s64, s72\n\t"
    "v_exp_f32_e32 v181, v181\n\t"
    "v_exp_f32_e32 v182, v182\n\t"
    "v_mfma_f32_16x16x32_bf16 v[164:167], a[204:207], a[140:143], v[164:167]\n\t"
    "s_add_u32 s65, s65, s72\n\t"
    "ds_read_b64_tr_b16 a[228:229], v10 offset:21568\n\t"
    "v_exp_f32_e32 v183, v183\n\t"
    "v_add_f32_e32 v176, 1.0, v176\n\t"
    "v_add_f32_e32 v177, 1.0, v177\n\t"
    "v_mfma_f32_16x16x32_bf16 v[164:167], a[208:211], a[168:171], v[164:167]\n\t"
    "ds_read_b64_tr_b16 a[230:231], v10 offset:25152\n\t"
    "v_add_f32_e32 v178, 1.0, v178\n\t"
    "v_add_f32_e32 v179, 1.0, v179\n\t"
    "v_add_f32_e32 v180, 1.0, v180\n\t"
    "v_mfma_f32_16x16x32_bf16 v[164:167], a[212:215], a[172:175], v[164:167]\n\t"
    "v_add_f32_e32 v181, 1.0, v181\n\t"
    "v_add_f32_e32 v182, 1.0, v182\n\t"
    "v_add_f32_e32 v183, 1.0, v183\n\t"
    "v_mfma_f32_16x16x32_bf16 v[168:171], a[192:195], a[112:115], v[168:171]\n\t"
    "ds_read_b64_tr_b16 a[232:233], v10 offset:51264\n\t"
    "v_rcp_f32_e32 v176, v176\n\t"
    "v_rcp_f32_e32 v177, v177\n\t"
    "v_mfma_f32_16x16x32_bf16 v[168:171], a[196:199], a[116:119], v[168:171]\n\t"
    "ds_read_b64_tr_b16 a[234:235], v10 offset:54848\n\t"
    "v_rcp_f32_e32 v178, v178\n\t"
    "v_mfma_f32_16x16x32_bf16 v[168:171], a[200:203], a[144:147], v[168:171]\n\t"
    "v_rcp_f32_e32 v179, v179\n\t"
    "v_rcp_f32_e32 v180, v180\n\t"
    "v_mfma_f32_16x16x32_bf16 v[168:171], a[204:207], a[148:151], v[168:171]\n\t"
    "ds_read_b64_tr_b16 a[236:237], v10 offset:58432\n\t"
    "v_rcp_f32_e32 v181, v181\n\t"
    "v_rcp_f32_e32 v182, v182\n\t"
    "v_mfma_f32_16x16x32_bf16 v[168:171], a[208:211], a[176:179], v[168:171]\n\t"
    "ds_read_b64_tr_b16 a[238:239], v10 offset:62016\n\t"
    "v_rcp_f32_e32 v183, v183\n\t"
    "v_mul_f32_e32 v176, v176, v224\n\t"
    "v_mfma_f32_16x16x32_bf16 v[168:171], a[212:215], a[180:183], v[168:171]\n\t"
    "v_mul_f32_e32 v177, v177, v225\n\t"
    "v_mul_f32_e32 v178, v178, v226\n\t"
    "v_mul_f32_e32 v179, v179, v227\n\t"
    "v_mul_f32_e32 v180, v180, v228\n\t"
    "v_mfma_f32_16x16x32_bf16 v[172:175], a[192:195], a[120:123], v[172:175]\n\t"
    "v_mul_f32_e32 v181, v181, v229\n\t"
    "v_mul_f32_e32 v182, v182, v230\n\t"
    "v_mul_f32_e32 v183, v183, v231\n\t"
    "v_cvt_pk_bf16_f32 v176, v176, v177\n\t"
    "v_mfma_f32_16x16x32_bf16 v[172:175], a[196:199], a[124:127], v[172:175]\n\t"
    "v_cvt_pk_bf16_f32 v177, v178, v179\n\t"
    "v_cvt_pk_bf16_f32 v178, v180, v181\n\t"
    "v_cvt_pk_bf16_f32 v179, v182, v183\n\t"
    "v_mfma_f32_16x16x32_bf16 v[172:175], a[200:203], a[152:155], v[172:175]\n\t"
    "v_permlane16_swap_b32_e32 v176, v178\n\t"
    "v_permlane16_swap_b32_e32 v177, v179\n\t"
    "v_mfma_f32_16x16x32_bf16 v[172:175], a[204:207], a[156:159], v[172:175]\n\t"
    "buffer_store_dwordx4 v[176:179], v22, s[16:19], s96 offen offset:0 sc1\n\t"
    "v_mul_f32_e32 v184, s92, v184\n\t"
    "v_mul_f32_e32 v185, s92, v185\n\t"
    "v_mfma_f32_16x16x32_bf16 v[172:175], a[208:211], a[184:187], v[172:175]\n\t"
    "v_mul_f32_e32 v186, s92, v186\n\t"
    "v_mul_f32_e32 v187, s92, v187\n\t"
    "v_mul_f32_e32 v188, s92, v188\n\t"
    "v_mul_f32_e32 v189, s92, v189\n\t"
    "v_mfma_f32_16x16x32_bf16 v[172:175], a[212:215], a[188:191], v[172:175]\n\t"
    "v_mul_f32_e32 v190, s92, v190\n\t"
    "v_mul_f32_e32 v191, s92, v191\n\t"
    "v_exp_f32_e32 v184, v184\n\t"
    "s_waitcnt lgkmcnt(8)\n\t"
    "v_mfma_f32_16x16x32_bf16 v[240:243], a[216:219], a[96:99], v[240:243]\n\t"
    "ds_read_b64_tr_b16 a[192:193], v9 offset:36960\n\t"
    "v_exp_f32_e32 v185, v185\n\t"
    "v_mfma_f32_16x16x32_bf16 v[240:243], a[220:223], a[100:103], v[240:243]\n\t"
    "ds_read_b64_tr_b16 a[194:195], v9 offset:40544\n\t"
    "v_exp_f32_e32 v186, v186\n\t"
    "v_exp_f32_e32 v187, v187\n\t"
    "s_waitcnt lgkmcnt(6)\n\t"
    "v_mfma_f32_16x16x32_bf16 v[240:243], a[224:227], a[128:131], v[240:243]\n\t"
    "v_exp_f32_e32 v188, v188\n\t"
    "v_exp_f32_e32 v189, v189\n\t"
    "v_mfma_f32_16x16x32_bf16 v[240:243], a[228:231], a[132:135], v[240:243]\n\t"
    "ds_read_b64_tr_b16 a[196:197], v9 offset:44128\n\t"
    "v_exp_f32_e32 v190, v190\n\t"
    "s_waitcnt lgkmcnt(3)\n\t"
    "v_mfma_f32_16x16x32_bf16 v[240:243], a[232:235], a[160:163], v[240:243]\n\t"
    "ds_read_b64_tr_b16 a[198:199], v9 offset:47712\n\t"
    "v_exp_f32_e32 v191, v191\n\t"
    "v_mfma_f32_16x16x32_bf16 v[240:243], a[236:239], a[164:167], v[240:243]\n\t"
    "v_add_f32_e32 v184, 1.0, v184\n\t"
    "v_add_f32_e32 v185, 1.0, v185\n\t"
    "v_add_f32_e32 v186, 1.0, v186\n\t"
    "v_mfma_f32_16x16x32_bf16 v[244:247], a[216:219], a[104:107], v[244:247]\n\t"
    "ds_read_b64_tr_b16 a[200:201], v10 offset:96\n\t"
    "v_add_f32_e32 v187, 1.0, v187\n\t"
    "v_add_f32_e32 v188, 1.0, v188\n\t"
    "v_mfma_f32_16x16x32_bf16 v[244:247], a[220:223], a[108:111], v[244:247]\n\t"
    "ds_read_b64_tr_b16 a[202:203], v10 offset:3680\n\t"
    "v_add_f32_e32 v189, 1.0, v189\n\t"
    "v_add_f32_e32 v190, 1.0, v190\n\t"
    "v_mfma_f32_16x16x32_bf16 v[244:247], a[224:227], a[136:139], v[244:247]\n\t"
    "v_add_f32_e32 v191, 1.0, v191\n\t"
    "v_rcp_f32_e32 v184, v184\n\t"
    "v_mfma_f32_16x16x32_bf16 v[244:247], a[228:231], a[140:143], v[244:247]\n\t"
    "ds_read_b64_tr_b16 a[204:205], v10 offset:7264\n\t"
    "v_rcp_f32_e32 v185, v185\n\t"
    "v_mfma_f32_16x16x32_bf16 v[244:247], a[232:235], a[168:171], v[244:247]\n\t"
    "ds_read_b64_tr_b16 a[206:207], v10 offset:10848\n\t"
    "v_rcp_f32_e32 v186, v186\n\t"
    "v_mfma_f32_16x16x32_bf16 v[244:247], a[236:239], a[172:175], v[244:247]\n\t"
    "v_rcp_f32_e32 v187, v187\n\t"
    "v_mfma_f32_16x16x32_bf16 v[248:251], a[216:219], a[112:115], v[248:251]\n\t"
    "ds_read_b64_tr_b16 a[208:209], v10 offset:36960\n\t"
    "v_rcp_f32_e32 v188, v188\n\t"
    "v_mfma_f32_16x16x32_bf16 v[248:251], a[220:223], a[116:119], v[248:251]\n\t"
    "ds_read_b64_tr_b16 a[210:211], v10 offset:40544\n\t"
    "v_rcp_f32_e32 v189, v189\n\t"
    "v_mfma_f32_16x16x32_bf16 v[248:251], a[224:227], a[144:147], v[248:251]\n\t"
    "v_rcp_f32_e32 v190, v190\n\t"
    "v_rcp_f32_e32 v191, v191\n\t"
    "v_mfma_f32_16x16x32_bf16 v[248:251], a[228:231], a[148:151], v[248:251]\n\t"
    "ds_read_b64_tr_b16 a[212:213], v10 offset:44128\n\t"
    "v_mul_f32_e32 v184, v184, v232\n\t"
    "v_mul_f32_e32 v185, v185, v233\n\t"
    "v_mfma_f32_16x16x32_bf16 v[248:251], a[232:235], a[176:179], v[248:251]\n\t"
    "ds_read_b64_tr_b16 a[214:215], v10 offset:47712\n\t"
    "v_mul_f32_e32 v186, v186, v234\n\t"
    "v_mul_f32_e32 v187, v187, v235\n\t"
    "v_mfma_f32_16x16x32_bf16 v[248:251], a[236:239], a[180:183], v[248:251]\n\t"
    "v_mul_f32_e32 v188, v188, v236\n\t"
    "v_mul_f32_e32 v189, v189, v237\n\t"
    "v_mul_f32_e32 v190, v190, v238\n\t"
    "v_mfma_f32_16x16x32_bf16 v[252:255], a[216:219], a[120:123], v[252:255]\n\t"
    "v_mul_f32_e32 v191, v191, v239\n\t"
    "v_cvt_pk_bf16_f32 v184, v184, v185\n\t"
    "v_cvt_pk_bf16_f32 v185, v186, v187\n\t"
    "v_mfma_f32_16x16x32_bf16 v[252:255], a[220:223], a[124:127], v[252:255]\n\t"
    "v_cvt_pk_bf16_f32 v186, v188, v189\n\t"
    "v_cvt_pk_bf16_f32 v187, v190, v191\n\t"
    "v_mfma_f32_16x16x32_bf16 v[252:255], a[224:227], a[152:155], v[252:255]\n\t"
    "v_permlane16_swap_b32_e32 v184, v186\n\t"
    "v_permlane16_swap_b32_e32 v185, v187\n\t"
    "v_mfma_f32_16x16x32_bf16 v[252:255], a[228:231], a[156:159], v[252:255]\n\t"
    "buffer_store_dwordx4 v[184:187], v22, s[16:19], s96 offen offset:2048 sc1\n\t"
    "s_add_u32 s96, s96, s98\n\t"
    "v_mfma_f32_16x16x32_bf16 v[252:255], a[232:235], a[184:187], v[252:255]\n\t"
    "v_mfma_f32_16x16x32_bf16 v[252:255], a[236:239], a[188:191], v[252:255]\n\t"
    "s_waitcnt lgkmcnt(8)\n\t"
    "v_mfma_f32_16x16x32_bf16 v[144:147], a[192:195], a[96:99], v[144:147]\n\t"
    "ds_read_b64_tr_b16 a[216:217], v9 offset:51296\n\t"
    "v_mfma_f32_16x16x32_bf16 v[144:147], a[196:199], a[100:103], v[144:147]\n\t"
    "ds_read_b64_tr_b16 a[218:219], v9 offset:54880\n\t"
    "s_waitcnt lgkmcnt(6)\n\t"
    "v_mfma_f32_16x16x32_bf16 v[144:147], a[200:203], a[128:131], v[144:147]\n\t"
    "v_mul_f32_e32 v160, s92, v160\n\t"
    "v_mul_f32_e32 v161, s92, v161\n\t"
    "v_mul_f32_e32 v162, s92, v162\n\t"
    "v_mul_f32_e32 v163, s92, v163\n\t"
    "v_mfma_f32_16x16x32_bf16 v[144:147], a[204:207], a[132:135], v[144:147]\n\t"
    "ds_read_b64_tr_b16 a[220:221], v9 offset:58464\n\t"
    "v_mul_f32_e32 v164, s92, v164\n\t"
    "v_mul_f32_e32 v165, s92, v165\n\t"
    "v_mul_f32_e32 v166, s92, v166\n\t"
    "s_waitcnt lgkmcnt(3)\n\t"
    "v_mfma_f32_16x16x32_bf16 v[144:147], a[208:211], a[160:163], v[144:147]\n\t"
    "ds_read_b64_tr_b16 a[222:223], v9 offset:62048\n\t"
    "v_mul_f32_e32 v167, s92, v167\n\t"
    "v_exp_f32_e32 v160, v160\n\t"
    "v_mfma_f32_16x16x32_bf16 v[144:147], a[212:215], a[164:167], v[144:147]\n\t"
    "v_exp_f32_e32 v161, v161\n\t"
    "v_exp_f32_e32 v162, v162\n\t"
    "v_mfma_f32_16x16x32_bf16 v[148:151], a[192:195], a[104:107], v[148:151]\n\t"
    "ds_read_b64_tr_b16 a[224:225], v10 offset:14432\n\t"
    "v_exp_f32_e32 v163, v163\n\t"
    "v_mfma_f32_16x16x32_bf16 v[148:151], a[196:199], a[108:111], v[148:151]\n\t"
    "ds_read_b64_tr_b16 a[226:227], v10 offset:18016\n\t"
    "v_exp_f32_e32 v164, v164\n\t"
    "v_exp_f32_e32 v165, v165\n\t"
    "v_mfma_f32_16x16x32_bf16 v[148:151], a[200:203], a[136:139], v[148:151]\n\t"
    "v_exp_f32_e32 v166, v166\n\t"
    "v_exp_f32_e32 v167, v167\n\t"
    "v_mfma_f32_16x16x32_bf16 v[148:151], a[204:207], a[140:143], v[148:151]\n\t"
    "ds_read_b64_tr_b16 a[228:229], v10 offset:21600\n\t"
    "v_add_f32_e32 v160, 1.0, v160\n\t"
    "v_add_f32_e32 v161, 1.0, v161\n\t"
    "v_add_f32_e32 v162, 1.0, v162\n\t"
    "v_mfma_f32_16x16x32_bf16 v[148:151], a[208:211], a[168:171], v[148:151]\n\t"
    "ds_read_b64_tr_b16 a[230:231], v10 offset:25184\n\t"
    "v_add_f32_e32 v163, 1.0, v163\n\t"
    "v_add_f32_e32 v164, 1.0, v164\n\t"
    "v_add_f32_e32 v165, 1.0, v165\n\t"
    "v_mfma_f32_16x16x32_bf16 v[148:151], a[212:215], a[172:175], v[148:151]\n\t"
    "v_add_f32_e32 v166, 1.0, v166\n\t"
    "v_add_f32_e32 v167, 1.0, v167\n\t"
    "v_rcp_f32_e32 v160, v160\n\t"
    "v_mfma_f32_16x16x32_bf16 v[152:155], a[192:195], a[112:115], v[152:155]\n\t"
    "ds_read_b64_tr_b16 a[232:233], v10 offset:51296\n\t"
    "v_rcp_f32_e32 v161, v161\n\t"
    "v_mfma_f32_16x16x32_bf16 v[152:155], a[196:199], a[116:119], v[152:155]\n\t"
    "ds_read_b64_tr_b16 a[234:235], v10 offset:54880\n\t"
    "v_rcp_f32_e32 v162, v162\n\t"
    "v_rcp_f32_e32 v163, v163\n\t"
    "v_mfma_f32_16x16x32_bf16 v[152:155], a[200:203], a[144:147], v[152:155]\n\t"
    "v_rcp_f32_e32 v164, v164\n\t"
    "v_rcp_f32_e32 v165, v165\n\t"
    "v_mfma_f32_16x16x32_bf16 v[152:155], a[204:207], a[148:151], v[152:155]\n\t"
    "ds_read_b64_tr_b16 a[236:237], v10 offset:58464\n\t"
    "v_rcp_f32_e32 v166, v166\n\t"
    "v_mfma_f32_16x16x32_bf16 v[152:155], a[208:211], a[176:179], v[152:155]\n\t"
    "ds_read_b64_tr_b16 a[238:239], v10 offset:62048\n\t"
    "v_rcp_f32_e32 v167, v167\n\t"
    "v_mul_f32_e32 v160, v160, v240\n\t"
    "v_mul_f32_e32 v161, v161, v241\n\t"
    "v_mfma_f32_16x16x32_bf16 v[152:155], a[212:215], a[180:183], v[152:155]\n\t"
    "v_mul_f32_e32 v162, v162, v242\n\t"
    "v_mul_f32_e32 v163, v163, v243\n\t"
    "v_mul_f32_e32 v164, v164, v244\n\t"
    "v_mul_f32_e32 v165, v165, v245\n\t"
    "v_mfma_f32_16x16x32_bf16 v[156:159], a[192:195], a[120:123], v[156:159]\n\t"
    "v_mul_f32_e32 v166, v166, v246\n\t"
    "v_mul_f32_e32 v167, v167, v247\n\t"
    "v_cvt_pk_bf16_f32 v160, v160, v161\n\t"
    "v_cvt_pk_bf16_f32 v161, v162, v163\n\t"
    "v_mfma_f32_16x16x32_bf16 v[156:159], a[196:199], a[124:127], v[156:159]\n\t"
    "v_cvt_pk_bf16_f32 v162, v164, v165\n\t"
    "v_cvt_pk_bf16_f32 v163, v166, v167\n\t"
    "s_nop 0\n\t"
    "v_permlane16_swap_b32_e32 v160, v162\n\t"
    "v_mfma_f32_16x16x32_bf16 v[156:159], a[200:203], a[152:155], v[156:159]\n\t"
    "v_permlane16_swap_b32_e32 v161, v163\n\t"
    "v_mfma_f32_16x16x32_bf16 v[156:159], a[204:207], a[156:159], v[156:159]\n\t"
    "buffer_store_dwordx4 v[160:163], v22, s[16:19], s96 offen offset:0 sc1\n\t"
    "v_mul_f32_e32 v168, s92, v168\n\t"
    "v_mul_f32_e32 v169, s92, v169\n\t"
    "v_mfma_f32_16x16x32_bf16 v[156:159], a[208:211], a[184:187], v[156:159]\n\t"
    "v_mul_f32_e32 v170, s92, v170\n\t"
    "v_mul_f32_e32 v171, s92, v171\n\t"
    "v_mul_f32_e32 v172, s92, v172\n\t"
    "v_mul_f32_e32 v173, s92, v173\n\t"
    "v_mfma_f32_16x16x32_bf16 v[156:159], a[212:215], a[188:191], v[156:159]\n\t"
    "v_mul_f32_e32 v174, s92, v174\n\t"
    "v_mul_f32_e32 v175, s92, v175\n\t"
    "v_exp_f32_e32 v168, v168\n\t"
    "s_waitcnt lgkmcnt(8)\n\t"
    "v_mfma_f32_16x16x32_bf16 a[0:3], a[216:219], a[96:99], a[0:3]\n\t"
    "ds_read_b64_tr_b16 a[192:193], v9 offset:36992\n\t"
    "v_exp_f32_e32 v169, v169\n\t"
    "v_mfma_f32_16x16x32_bf16 a[0:3], a[220:223], a[100:103], a[0:3]\n\t"
    "ds_read_b64_tr_b16 a[194:195], v9 offset:40576\n\t"
    "v_exp_f32_e32 v170, v170\n\t"
    "v_exp_f32_e32 v171, v171\n\t"
    "s_waitcnt lgkmcnt(6)\n\t"
    "v_mfma_f32_16x16x32_bf16 a[0:3], a[224:227], a[128:131], a[0:3]\n\t"
    "v_exp_f32_e32 v172, v172\n\t"
    "v_exp_f32_e32 v173, v173\n\t"
    "v_mfma_f32_16x16x32_bf16 a[0:3], a[228:231], a[132:135], a[0:3]\n\t"
    "ds_read_b64_tr_b16 a[196:197], v9 offset:44160\n\t"
    "v_exp_f32_e32 v174, v174\n\t"
    "s_waitcnt lgkmcnt(3)\n\t"
    "v_mfma_f32_16x16x32_bf16 a[0:3], a[232:235], a[160:163], a[0:3]\n\t"
    "ds_read_b64_tr_b16 a[198:199], v9 offset:47744\n\t"
    "v_exp_f32_e32 v175, v175\n\t"
    "v_add_f32_e32 v168, 1.0, v168\n\t"
    "v_mfma_f32_16x16x32_bf16 a[0:3], a[236:239], a[164:167], a[0:3]\n\t"
    "v_add_f32_e32 v169, 1.0, v169\n\t"
    "v_add_f32_e32 v170, 1.0, v170\n\t"
    "v_add_f32_e32 v171, 1.0, v171\n\t"
    "v_mfma_f32_16x16x32_bf16 a[4:7], a[216:219], a[104:107], a[4:7]\n\t"
    "ds_read_b64_tr_b16 a[200:201], v10 offset:128\n\t"
    "v_add_f32_e32 v172, 1.0, v172\n\t"
    "v_add_f32_e32 v173, 1.0, v173\n\t"
    "v_mfma_f32_16x16x32_bf16 a[4:7], a[220:223], a[108:111], a[4:7]\n\t"
    "ds_read_b64_tr_b16 a[202:203], v10 offset:3712\n\t"
    "v_add_f32_e32 v174, 1.0, v174\n\t"
    "v_add_f32_e32 v175, 1.0, v175\n\t"
    "v_mfma_f32_16x16x32_bf16 a[4:7], a[224:227], a[136:139], a[4:7]\n\t"
    "buffer_load_dwordx2 v[224:225], v26, s[24:27], 0 offen offset:0\n\t"
    "v_rcp_f32_e32 v168, v168\n\t"
    "v_mfma_f32_16x16x32_bf16 a[4:7], a[228:231], a[140:143], a[4:7]\n\t"
    "ds_read_b64_tr_b16 a[204:205], v10 offset:7296\n\t"
    "v_rcp_f32_e32 v169, v169\n\t"
    "v_mfma_f32_16x16x32_bf16 a[4:7], a[232:235], a[168:171], a[4:7]\n\t"
    "ds_read_b64_tr_b16 a[206:207], v10 offset:10880\n\t"
    "v_rcp_f32_e32 v170, v170\n\t"
    "v_mfma_f32_16x16x32_bf16 a[4:7], a[236:239], a[172:175], a[4:7]\n\t"
    "v_rcp_f32_e32 v171, v171\n\t"
    "v_mfma_f32_16x16x32_bf16 a[8:11], a[216:219], a[112:115], a[8:11]\n\t"
    "ds_read_b64_tr_b16 a[208:209], v10 offset:36992\n\t"
    "v_rcp_f32_e32 v172, v172\n\t"
    "v_mfma_f32_16x16x32_bf16 a[8:11], a[220:223], a[116:119], a[8:11]\n\t"
    "ds_read_b64_tr_b16 a[210:211], v10 offset:40576\n\t"
    "v_rcp_f32_e32 v173, v173\n\t"
    "v_mfma_f32_16x16x32_bf16 a[8:11], a[224:227], a[144:147], a[8:11]\n\t"
    "buffer_load_dwordx2 v[226:227], v26, s[24:27], 0 offen offset:32\n\t"
    "v_rcp_f32_e32 v174, v174\n\t"
    "v_mfma_f32_16x16x32_bf16 a[8:11], a[228:231], a[148:151], a[8:11]\n\t"
    "ds_read_b64_tr_b16 a[212:213], v10 offset:44160\n\t"
    "v_rcp_f32_e32 v175, v175\n\t"
    "v_mul_f32_e32 v168, v168, v248\n\t"
    "v_mfma_f32_16x16x32_bf16 a[8:11], a[232:235], a[176:179], a[8:11]\n\t"
    "ds_read_b64_tr_b16 a[214:215], v10 offset:47744\n\t"
    "v_mul_f32_e32 v169, v169, v249\n\t"
    "v_mul_f32_e32 v170, v170, v250\n\t"
    "v_mfma_f32_16x16x32_bf16 a[8:11], a[236:239], a[180:183], a[8:11]\n\t"
    "v_mul_f32_e32 v171, v171, v251\n\t"
    "v_mul_f32_e32 v172, v172, v252\n\t"
    "v_mul_f32_e32 v173, v173, v253\n\t"
    "v_mfma_f32_16x16x32_bf16 a[12:15], a[216:219], a[120:123], a[12:15]\n\t"
    "v_mul_f32_e32 v174, v174, v254\n\t"
    "v_mul_f32_e32 v175, v175, v255\n\t"
    "v_cvt_pk_bf16_f32 v168, v168, v169\n\t"
    "v_mfma_f32_16x16x32_bf16 a[12:15], a[220:223], a[124:127], a[12:15]\n\t"
    "v_cvt_pk_bf16_f32 v169, v170, v171\n\t"
    "v_cvt_pk_bf16_f32 v170, v172, v173\n\t"
    "v_cvt_pk_bf16_f32 v171, v174, v175\n\t"
    "v_mfma_f32_16x16x32_bf16 a[12:15], a[224:227], a[152:155], a[12:15]\n\t"
    "v_permlane16_swap_b32_e32 v168, v170\n\t"
    "v_mfma_f32_16x16x32_bf16 a[12:15], a[228:231], a[156:159], a[12:15]\n\t"
    "v_permlane16_swap_b32_e32 v169, v171\n\t"
    "v_mfma_f32_16x16x32_bf16 a[12:15], a[232:235], a[184:187], a[12:15]\n\t"
    "buffer_store_dwordx4 v[168:171], v22, s[16:19], s96 offen offset:2048 sc1\n\t"
    "s_add_u32 s96, s96, s97\n\t"
    "v_mfma_f32_16x16x32_bf16 a[12:15], a[236:239], a[188:191], a[12:15]\n\t"
    "s_waitcnt lgkmcnt(8)\n\t"
    "v_mfma_f32_16x16x32_bf16 v[128:131], a[192:195], a[96:99], v[128:131]\n\t"
    "ds_read_b64_tr_b16 a[216:217], v9 offset:51328\n\t"
    "v_mfma_f32_16x16x32_bf16 v[128:131], a[196:199], a[100:103], v[128:131]\n\t"
    "ds_read_b64_tr_b16 a[218:219], v9 offset:54912\n\t"
    "s_waitcnt lgkmcnt(6)\n\t"
    "v_mfma_f32_16x16x32_bf16 v[128:131], a[200:203], a[128:131], v[128:131]\n\t"
    "v_mul_f32_e32 v144, s92, v144\n\t"
    "v_mul_f32_e32 v145, s92, v145\n\t"
    "v_mul_f32_e32 v146, s92, v146\n\t"
    "v_mul_f32_e32 v147, s92, v147\n\t"
    "v_mfma_f32_16x16x32_bf16 v[128:131], a[204:207], a[132:135], v[128:131]\n\t"
    "ds_read_b64_tr_b16 a[220:221], v9 offset:58496\n\t"
    "v_mul_f32_e32 v148, s92, v148\n\t"
    "v_mul_f32_e32 v149, s92, v149\n\t"
    "v_mul_f32_e32 v150, s92, v150\n\t"
    "s_waitcnt lgkmcnt(3)\n\t"
    "v_mfma_f32_16x16x32_bf16 v[128:131], a[208:211], a[160:163], v[128:131]\n\t"
    "ds_read_b64_tr_b16 a[222:223], v9 offset:62080\n\t"
    "v_mul_f32_e32 v151, s92, v151\n\t"
    "v_exp_f32_e32 v144, v144\n\t"
    "v_mfma_f32_16x16x32_bf16 v[128:131], a[212:215], a[164:167], v[128:131]\n\t"
    "v_exp_f32_e32 v145, v145\n\t"
    "v_exp_f32_e32 v146, v146\n\t"
    "v_mfma_f32_16x16x32_bf16 v[132:135], a[192:195], a[104:107], v[132:135]\n\t"
    "ds_read_b64_tr_b16 a[224:225], v10 offset:14464\n\t"
    "v_exp_f32_e32 v147, v147\n\t"
    "v_mfma_f32_16x16x32_bf16 v[132:135], a[196:199], a[108:111], v[132:135]\n\t"
    "ds_read_b64_tr_b16 a[226:227], v10 offset:18048\n\t"
    "v_exp_f32_e32 v148, v148\n\t"
    "v_exp_f32_e32 v149, v149\n\t"
    "v_mfma_f32_16x16x32_bf16 v[132:135], a[200:203], a[136:139], v[132:135]\n\t"
    "v_exp_f32_e32 v150, v150\n\t"
    "v_exp_f32_e32 v151, v151\n\t"
    "v_mfma_f32_16x16x32_bf16 v[132:135], a[204:207], a[140:143], v[132:135]\n\t"
    "ds_read_b64_tr_b16 a[228:229], v10 offset:21632\n\t"
    "v_add_f32_e32 v144, 1.0, v144\n\t"
    "v_add_f32_e32 v145, 1.0, v145\n\t"
    "v_add_f32_e32 v146, 1.0, v146\n\t"
    "v_mfma_f32_16x16x32_bf16 v[132:135], a[208:211], a[168:171], v[132:135]\n\t"
    "ds_read_b64_tr_b16 a[230:231], v10 offset:25216\n\t"
    "v_add_f32_e32 v147, 1.0, v147\n\t"
    "v_add_f32_e32 v148, 1.0, v148\n\t"
    "v_add_f32_e32 v149, 1.0, v149\n\t"
    "v_mfma_f32_16x16x32_bf16 v[132:135], a[212:215], a[172:175], v[132:135]\n\t"
    "v_add_f32_e32 v150, 1.0, v150\n\t"
    "v_add_f32_e32 v151, 1.0, v151\n\t"
    "v_accvgpr_read_b32 v176, a0\n\t"
    "v_accvgpr_read_b32 v177, a1\n\t"
    "v_mfma_f32_16x16x32_bf16 v[136:139], a[192:195], a[112:115], v[136:139]\n\t"
    "ds_read_b64_tr_b16 a[232:233], v10 offset:51328\n\t"
    "v_accvgpr_read_b32 v178, a2\n\t"
    "v_accvgpr_read_b32 v179, a3\n\t"
    "v_accvgpr_read_b32 v180, a4\n\t"
    "v_mfma_f32_16x16x32_bf16 v[136:139], a[196:199], a[116:119], v[136:139]\n\t"
    "ds_read_b64_tr_b16 a[234:235], v10 offset:54912\n\t"
    "v_accvgpr_read_b32 v181, a5\n\t"
    "v_accvgpr_read_b32 v182, a6\n\t"
    "v_accvgpr_read_b32 v183, a7\n\t"
    "v_mfma_f32_16x16x32_bf16 v[136:139], a[200:203], a[144:147], v[136:139]\n\t"
    "v_rcp_f32_e32 v144, v144\n\t"
    "v_rcp_f32_e32 v145, v145\n\t"
    "v_mfma_f32_16x16x32_bf16 v[136:139], a[204:207], a[148:151], v[136:139]\n\t"
    "ds_read_b64_tr_b16 a[236:237], v10 offset:58496\n\t"
    "v_rcp_f32_e32 v146, v146\n\t"
    "v_mfma_f32_16x16x32_bf16 v[136:139], a[208:211], a[176:179], v[136:139]\n\t"
    "ds_read_b64_tr_b16 a[238:239], v10 offset:62080\n\t"
    "v_rcp_f32_e32 v147, v147\n\t"
    "v_rcp_f32_e32 v148, v148\n\t"
    "v_mfma_f32_16x16x32_bf16 v[136:139], a[212:215], a[180:183], v[136:139]\n\t"
    "v_rcp_f32_e32 v149, v149\n\t"
    "v_rcp_f32_e32 v150, v150\n\t"
    "v_mfma_f32_16x16x32_bf16 v[140:143], a[192:195], a[120:123], v[140:143]\n\t"
    "v_rcp_f32_e32 v151, v151\n\t"
    "v_mul_f32_e32 v144, v144, v176\n\t"
    "v_mul_f32_e32 v145, v145, v177\n\t"
    "v_mfma_f32_16x16x32_bf16 v[140:143], a[196:199], a[124:127], v[140:143]\n\t"
    "v_mul_f32_e32 v146, v146, v178\n\t"
    "v_mul_f32_e32 v147, v147, v179\n\t"
    "v_mul_f32_e32 v148, v148, v180\n\t"
    "v_mul_f32_e32 v149, v149, v181\n\t"
    "v_mfma_f32_16x16x32_bf16 v[140:143], a[200:203], a[152:155], v[140:143]\n\t"
    "v_mul_f32_e32 v150, v150, v182\n\t"
    "v_mul_f32_e32 v151, v151, v183\n\t"
    "v_cvt_pk_bf16_f32 v144, v144, v145\n\t"
    "v_cvt_pk_bf16_f32 v145, v146, v147\n\t"
    "v_mfma_f32_16x16x32_bf16 v[140:143], a[204:207], a[156:159], v[140:143]\n\t"
    "v_cvt_pk_bf16_f32 v146, v148, v149\n\t"
    "v_cvt_pk_bf16_f32 v147, v150, v151\n\t"
    "s_nop 0\n\t"
    "v_permlane16_swap_b32_e32 v144, v146\n\t"
    "v_mfma_f32_16x16x32_bf16 v[140:143], a[208:211], a[184:187], v[140:143]\n\t"
    "v_permlane16_swap_b32_e32 v145, v147\n\t"
    "v_mfma_f32_16x16x32_bf16 v[140:143], a[212:215], a[188:191], v[140:143]\n\t"
    "buffer_store_dwordx4 v[144:147], v22, s[16:19], s96 offen offset:0 sc1\n\t"
    "v_mul_f32_e32 v152, s92, v152\n\t"
    "v_mul_f32_e32 v153, s92, v153\n\t"
    "s_waitcnt lgkmcnt(8)\n\t"
    "v_mfma_f32_16x16x32_bf16 a[16:19], a[216:219], a[96:99], a[16:19]\n\t"
    "ds_read_b64_tr_b16 a[192:193], v9 offset:37024\n\t"
    "v_mul_f32_e32 v154, s92, v154\n\t"
    "v_mul_f32_e32 v155, s92, v155\n\t"
    "v_mul_f32_e32 v156, s92, v156\n\t"
    "v_mfma_f32_16x16x32_bf16 a[16:19], a[220:223], a[100:103], a[16:19]\n\t"
    "ds_read_b64_tr_b16 a[194:195], v9 offset:40608\n\t"
    "v_mul_f32_e32 v157, s92, v157\n\t"
    "v_mul_f32_e32 v158, s92, v158\n\t"
    "v_mul_f32_e32 v159, s92, v159\n\t"
    "s_waitcnt lgkmcnt(6)\n\t"
    "v_mfma_f32_16x16x32_bf16 a[16:19], a[224:227], a[128:131], a[16:19]\n\t"
    "v_exp_f32_e32 v152, v152\n\t"
    "v_exp_f32_e32 v153, v153\n\t"
    "v_mfma_f32_16x16x32_bf16 a[16:19], a[228:231], a[132:135], a[16:19]\n\t"
    "ds_read_b64_tr_b16 a[196:197], v9 offset:44192\n\t"
    "v_exp_f32_e32 v154, v154\n\t"
    "s_waitcnt lgkmcnt(3)\n\t"
    "v_mfma_f32_16x16x32_bf16 a[16:19], a[232:235], a[160:163], a[16:19]\n\t"
    "ds_read_b64_tr_b16 a[198:199], v9 offset:47776\n\t"
    "v_exp_f32_e32 v155, v155\n\t"
    "v_exp_f32_e32 v156, v156\n\t"
    "v_mfma_f32_16x16x32_bf16 a[16:19], a[236:239], a[164:167], a[16:19]\n\t"
    "v_exp_f32_e32 v157, v157\n\t"
    "v_exp_f32_e32 v158, v158\n\t"
    "v_mfma_f32_16x16x32_bf16 a[20:23], a[216:219], a[104:107], a[20:23]\n\t"
    "ds_read_b64_tr_b16 a[200:201], v10 offset:160\n\t"
    "v_exp_f32_e32 v159, v159\n\t"
    "v_add_f32_e32 v152, 1.0, v152\n\t"
    "v_mfma_f32_16x16x32_bf16 a[20:23], a[220:223], a[108:111], a[20:23]\n\t"
    "ds_read_b64_tr_b16 a[202:203], v10 offset:3744\n\t"
    "v_add_f32_e32 v153, 1.0, v153\n\t"
    "v_add_f32_e32 v154, 1.0, v154\n\t"
    "v_add_f32_e32 v155, 1.0, v155\n\t"
    "v_mfma_f32_16x16x32_bf16 a[20:23], a[224:227], a[136:139], a[20:23]\n\t"
    "v_add_f32_e32 v156, 1.0, v156\n\t"
    "v_add_f32_e32 v157, 1.0, v157\n\t"
    "v_add_f32_e32 v158, 1.0, v158\n\t"
    "v_add_f32_e32 v159, 1.0, v159\n\t"
    "v_mfma_f32_16x16x32_bf16 a[20:23], a[228:231], a[140:143], a[20:23]\n\t"
    "ds_read_b64_tr_b16 a[204:205], v10 offset:7328\n\t"
    "v_accvgpr_read_b32 v184, a8\n\t"
    "v_accvgpr_read_b32 v185, a9\n\t"
    "v_accvgpr_read_b32 v186, a10\n\t"
    "v_mfma_f32_16x16x32_bf16 a[20:23], a[232:235], a[168:171], a[20:23]\n\t"
    "ds_read_b64_tr_b16 a[206:207], v10 offset:10912\n\t"
    "v_accvgpr_read_b32 v187, a11\n\t"
    "v_accvgpr_read_b32 v188, a12\n\t"
    "v_accvgpr_read_b32 v189, a13\n\t"
    "v_mfma_f32_16x16x32_bf16 a[20:23], a[236:239], a[172:175], a[20:23]\n\t"
    "v_accvgpr_read_b32 v190, a14\n\t"
    "v_accvgpr_read_b32 v191, a15\n\t"
    "v_rcp_f32_e32 v152, v152\n\t"
    "v_mfma_f32_16x16x32_bf16 a[24:27], a[216:219], a[112:115], a[24:27]\n\t"
    "ds_read_b64_tr_b16 a[208:209], v10 offset:37024\n\t"
    "v_rcp_f32_e32 v153, v153\n\t"
    "v_mfma_f32_16x16x32_bf16 a[24:27], a[220:223], a[116:119], a[24:27]\n\t"
    "ds_read_b64_tr_b16 a[210:211], v10 offset:40608\n\t"
    "v_rcp_f32_e32 v154, v154\n\t"
    "v_rcp_f32_e32 v155, v155\n\t"
    "v_mfma_f32_16x16x32_bf16 a[24:27], a[224:227], a[144:147], a[24:27]\n\t"
    "v_rcp_f32_e32 v156, v156\n\t"
    "v_rcp_f32_e32 v157, v157\n\t"
    "v_mfma_f32_16x16x32_bf16 a[24:27], a[228:231], a[148:151], a[24:27]\n\t"
    "ds_read_b64_tr_b16 a[212:213], v10 offset:44192\n\t"
    "v_rcp_f32_e32 v158, v158\n\t"
    "v_mfma_f32_16x16x32_bf16 a[24:27], a[232:235], a[176:179], a[24:27]\n\t"
    "ds_read_b64_tr_b16 a[214:215], v10 offset:47776\n\t"
    "v_rcp_f32_e32 v159, v159\n\t"
    "v_mul_f32_e32 v152, v152, v184\n\t"
    "v_mul_f32_e32 v153, v153, v185\n\t"
    "v_mfma_f32_16x16x32_bf16 a[24:27], a[236:239], a[180:183], a[24:27]\n\t"
    "v_mul_f32_e32 v154, v154, v186\n\t"
    "v_mul_f32_e32 v155, v155, v187\n\t"
    "v_mul_f32_e32 v156, v156, v188\n\t"
    "v_mul_f32_e32 v157, v157, v189\n\t"
    "v_mfma_f32_16x16x32_bf16 a[28:31], a[216:219], a[120:123], a[28:31]\n\t"
    "v_mul_f32_e32 v158, v158, v190\n\t"
    "v_mul_f32_e32 v159, v159, v191\n\t"
    "v_cvt_pk_bf16_f32 v152, v152, v153\n\t"
    "v_mfma_f32_16x16x32_bf16 a[28:31], a[220:223], a[124:127], a[28:31]\n\t"
    "v_cvt_pk_bf16_f32 v153, v154, v155\n\t"
    "v_cvt_pk_bf16_f32 v154, v156, v157\n\t"
    "v_cvt_pk_bf16_f32 v155, v158, v159\n\t"
    "v_mfma_f32_16x16x32_bf16 a[28:31], a[224:227], a[152:155], a[28:31]\n\t"
    "v_permlane16_swap_b32_e32 v152, v154\n\t"
    "v_mfma_f32_16x16x32_bf16 a[28:31], a[228:231], a[156:159], a[28:31]\n\t"
    "v_permlane16_swap_b32_e32 v153, v155\n\t"
    "v_mfma_f32_16x16x32_bf16 a[28:31], a[232:235], a[184:187], a[28:31]\n\t"
    "buffer_store_dwordx4 v[152:155], v22, s[16:19], s96 offen offset:2048 sc1\n\t"
    "s_add_u32 s96, s96, s98\n\t"
    "v_mfma_f32_16x16x32_bf16 a[28:31], a[236:239], a[188:191], a[28:31]\n\t"
    "s_waitcnt lgkmcnt(8)\n\t"
    "v_mfma_f32_16x16x32_bf16 v[112:115], a[192:195], a[96:99], v[112:115]\n\t"
    "ds_read_b64_tr_b16 a[216:217], v9 offset:51360\n\t"
    "v_mfma_f32_16x16x32_bf16 v[112:115], a[196:199], a[100:103], v[112:115]\n\t"
    "ds_read_b64_tr_b16 a[218:219], v9 offset:54944\n\t"
    "s_waitcnt lgkmcnt(6)\n\t"
    "v_mfma_f32_16x16x32_bf16 v[112:115], a[200:203], a[128:131], v[112:115]\n\t"
    "v_mul_f32_e32 v128, s92, v128\n\t"
    "v_mul_f32_e32 v129, s92, v129\n\t"
    "v_mul_f32_e32 v130, s92, v130\n\t"
    "v_mul_f32_e32 v131, s92, v131\n\t"
    "v_mfma_f32_16x16x32_bf16 v[112:115], a[204:207], a[132:135], v[112:115]\n\t"
    "ds_read_b64_tr_b16 a[220:221], v9 offset:58528\n\t"
    "v_mul_f32_e32 v132, s92, v132\n\t"
    "v_mul_f32_e32 v133, s92, v133\n\t"
    "v_mul_f32_e32 v134, s92, v134\n\t"
    "s_waitcnt lgkmcnt(3)\n\t"
    "v_mfma_f32_16x16x32_bf16 v[112:115], a[208:211], a[160:163], v[112:115]\n\t"
    "ds_read_b64_tr_b16 a[222:223], v9 offset:62112\n\t"
    "v_mul_f32_e32 v135, s92, v135\n\t"
    "v_exp_f32_e32 v128, v128\n\t"
    "v_mfma_f32_16x16x32_bf16 v[112:115], a[212:215], a[164:167], v[112:115]\n\t"
    "v_exp_f32_e32 v129, v129\n\t"
    "v_exp_f32_e32 v130, v130\n\t"
    "v_mfma_f32_16x16x32_bf16 v[116:119], a[192:195], a[104:107], v[116:119]\n\t"
    "ds_read_b64_tr_b16 a[224:225], v10 offset:14496\n\t"
    "v_exp_f32_e32 v131, v131\n\t"
    "v_mfma_f32_16x16x32_bf16 v[116:119], a[196:199], a[108:111], v[116:119]\n\t"
    "ds_read_b64_tr_b16 a[226:227], v10 offset:18080\n\t"
    "v_exp_f32_e32 v132, v132\n\t"
    "v_exp_f32_e32 v133, v133\n\t"
    "v_mfma_f32_16x16x32_bf16 v[116:119], a[200:203], a[136:139], v[116:119]\n\t"
    "v_exp_f32_e32 v134, v134\n\t"
    "v_exp_f32_e32 v135, v135\n\t"
    "v_mfma_f32_16x16x32_bf16 v[116:119], a[204:207], a[140:143], v[116:119]\n\t"
    "ds_read_b64_tr_b16 a[228:229], v10 offset:21664\n\t"
    "v_add_f32_e32 v128, 1.0, v128\n\t"
    "v_add_f32_e32 v129, 1.0, v129\n\t"
    "v_add_f32_e32 v130, 1.0, v130\n\t"
    "v_mfma_f32_16x16x32_bf16 v[116:119], a[208:211], a[168:171], v[116:119]\n\t"
    "ds_read_b64_tr_b16 a[230:231], v10 offset:25248\n\t"
    "v_add_f32_e32 v131, 1.0, v131\n\t"
    "v_add_f32_e32 v132, 1.0, v132\n\t"
    "v_add_f32_e32 v133, 1.0, v133\n\t"
    "v_mfma_f32_16x16x32_bf16 v[116:119], a[212:215], a[172:175], v[116:119]\n\t"
    "v_add_f32_e32 v134, 1.0, v134\n\t"
    "v_add_f32_e32 v135, 1.0, v135\n\t"
    "v_accvgpr_read_b32 v176, a16\n\t"
    "v_accvgpr_read_b32 v177, a17\n\t"
    "v_mfma_f32_16x16x32_bf16 v[120:123], a[192:195], a[112:115], v[120:123]\n\t"
    "ds_read_b64_tr_b16 a[232:233], v10 offset:51360\n\t"
    "v_accvgpr_read_b32 v178, a18\n\t"
    "v_accvgpr_read_b32 v179, a19\n\t"
    "v_accvgpr_read_b32 v180, a20\n\t"
    "v_mfma_f32_16x16x32_bf16 v[120:123], a[196:199], a[116:119], v[120:123]\n\t"
    "ds_read_b64_tr_b16 a[234:235], v10 offset:54944\n\t"
    "v_accvgpr_read_b32 v181, a21\n\t"
    "v_accvgpr_read_b32 v182, a22\n\t"
    "v_accvgpr_read_b32 v183, a23\n\t"
    "v_mfma_f32_16x16x32_bf16 v[120:123], a[200:203], a[144:147], v[120:123]\n\t"
    "v_rcp_f32_e32 v128, v128\n\t"
    "v_rcp_f32_e32 v129, v129\n\t"
    "v_mfma_f32_16x16x32_bf16 v[120:123], a[204:207], a[148:151], v[120:123]\n\t"
    "ds_read_b64_tr_b16 a[236:237], v10 offset:58528\n\t"
    "v_rcp_f32_e32 v130, v130\n\t"
    "v_mfma_f32_16x16x32_bf16 v[120:123], a[208:211], a[176:179], v[120:123]\n\t"
    "ds_read_b64_tr_b16 a[238:239], v10 offset:62112\n\t"
    "v_rcp_f32_e32 v131, v131\n\t"
    "v_rcp_f32_e32 v132, v132\n\t"
    "v_mfma_f32_16x16x32_bf16 v[120:123], a[212:215], a[180:183], v[120:123]\n\t"
    "v_rcp_f32_e32 v133, v133\n\t"
    "v_rcp_f32_e32 v134, v134\n\t"
    "v_mfma_f32_16x16x32_bf16 v[124:127], a[192:195], a[120:123], v[124:127]\n\t"
    "v_rcp_f32_e32 v135, v135\n\t"
    "v_mul_f32_e32 v128, v128, v176\n\t"
    "v_mul_f32_e32 v129, v129, v177\n\t"
    "v_mfma_f32_16x16x32_bf16 v[124:127], a[196:199], a[124:127], v[124:127]\n\t"
    "v_mul_f32_e32 v130, v130, v178\n\t"
    "v_mul_f32_e32 v131, v131, v179\n\t"
    "v_mul_f32_e32 v132, v132, v180\n\t"
    "v_mul_f32_e32 v133, v133, v181\n\t"
    "v_mfma_f32_16x16x32_bf16 v[124:127], a[200:203], a[152:155], v[124:127]\n\t"
    "v_mul_f32_e32 v134, v134, v182\n\t"
    "v_mul_f32_e32 v135, v135, v183\n\t"
    "v_cvt_pk_bf16_f32 v128, v128, v129\n\t"
    "v_cvt_pk_bf16_f32 v129, v130, v131\n\t"
    "v_mfma_f32_16x16x32_bf16 v[124:127], a[204:207], a[156:159], v[124:127]\n\t"
    "v_cvt_pk_bf16_f32 v130, v132, v133\n\t"
    "v_cvt_pk_bf16_f32 v131, v134, v135\n\t"
    "s_nop 0\n\t"
    "v_permlane16_swap_b32_e32 v128, v130\n\t"
    "v_mfma_f32_16x16x32_bf16 v[124:127], a[208:211], a[184:187], v[124:127]\n\t"
    "v_permlane16_swap_b32_e32 v129, v131\n\t"
    "v_mfma_f32_16x16x32_bf16 v[124:127], a[212:215], a[188:191], v[124:127]\n\t"
    "buffer_store_dwordx4 v[128:131], v22, s[16:19], s96 offen offset:0 sc1\n\t"
    "v_mul_f32_e32 v136, s92, v136\n\t"
    "v_mul_f32_e32 v137, s92, v137\n\t"
    "s_waitcnt lgkmcnt(8)\n\t"
    "v_mfma_f32_16x16x32_bf16 a[32:35], a[216:219], a[96:99], a[32:35]\n\t"
    "ds_read_b64_tr_b16 a[192:193], v9 offset:37056\n\t"
    "v_mul_f32_e32 v138, s92, v138\n\t"
    "v_mul_f32_e32 v139, s92, v139\n\t"
    "v_mul_f32_e32 v140, s92, v140\n\t"
    "v_mfma_f32_16x16x32_bf16 a[32:35], a[220:223], a[100:103], a[32:35]\n\t"
    "ds_read_b64_tr_b16 a[194:195], v9 offset:40640\n\t"
    "v_mul_f32_e32 v141, s92, v141\n\t"
    "v_mul_f32_e32 v142, s92, v142\n\t"
    "v_mul_f32_e32 v143, s92, v143\n\t"
    "s_waitcnt lgkmcnt(6)\n\t"
    "v_mfma_f32_16x16x32_bf16 a[32:35], a[224:227], a[128:131], a[32:35]\n\t"
    "v_exp_f32_e32 v136, v136\n\t"
    "v_exp_f32_e32 v137, v137\n\t"
    "v_mfma_f32_16x16x32_bf16 a[32:35], a[228:231], a[132:135], a[32:35]\n\t"
    "ds_read_b64_tr_b16 a[196:197], v9 offset:44224\n\t"
    "v_exp_f32_e32 v138, v138\n\t"
    "s_waitcnt lgkmcnt(3)\n\t"
    "v_mfma_f32_16x16x32_bf16 a[32:35], a[232:235], a[160:163], a[32:35]\n\t"
    "ds_read_b64_tr_b16 a[198:199], v9 offset:47808\n\t"
    "v_exp_f32_e32 v139, v139\n\t"
    "v_exp_f32_e32 v140, v140\n\t"
    "v_mfma_f32_16x16x32_bf16 a[32:35], a[236:239], a[164:167], a[32:35]\n\t"
    "v_exp_f32_e32 v141, v141\n\t"
    "v_exp_f32_e32 v142, v142\n\t"
    "v_mfma_f32_16x16x32_bf16 a[36:39], a[216:219], a[104:107], a[36:39]\n\t"
    "ds_read_b64_tr_b16 a[200:201], v10 offset:192\n\t"
    "v_exp_f32_e32 v143, v143\n\t"
    "v_add_f32_e32 v136, 1.0, v136\n\t"
    "v_mfma_f32_16x16x32_bf16 a[36:39], a[220:223], a[108:111], a[36:39]\n\t"
    "ds_read_b64_tr_b16 a[202:203], v10 offset:3776\n\t"
    "v_add_f32_e32 v137, 1.0, v137\n\t"
    "v_add_f32_e32 v138, 1.0, v138\n\t"
    "v_add_f32_e32 v139, 1.0, v139\n\t"
    "v_mfma_f32_16x16x32_bf16 a[36:39], a[224:227], a[136:139], a[36:39]\n\t"
    "v_add_f32_e32 v140, 1.0, v140\n\t"
    "v_add_f32_e32 v141, 1.0, v141\n\t"
    "v_add_f32_e32 v142, 1.0, v142\n\t"
    "v_add_f32_e32 v143, 1.0, v143\n\t"
    "v_mfma_f32_16x16x32_bf16 a[36:39], a[228:231], a[140:143], a[36:39]\n\t"
    "ds_read_b64_tr_b16 a[204:205], v10 offset:7360\n\t"
    "v_accvgpr_read_b32 v184, a24\n\t"
    "v_accvgpr_read_b32 v185, a25\n\t"
    "v_accvgpr_read_b32 v186, a26\n\t"
    "v_mfma_f32_16x16x32_bf16 a[36:39], a[232:235], a[168:171], a[36:39]\n\t"
    "ds_read_b64_tr_b16 a[206:207], v10 offset:10944\n\t"
    "v_accvgpr_read_b32 v187, a27\n\t"
    "v_accvgpr_read_b32 v188, a28\n\t"
    "v_accvgpr_read_b32 v189, a29\n\t"
    "v_mfma_f32_16x16x32_bf16 a[36:39], a[236:239], a[172:175], a[36:39]\n\t"
    "v_accvgpr_read_b32 v190, a30\n\t"
    "v_accvgpr_read_b32 v191, a31\n\t"
    "v_rcp_f32_e32 v136, v136\n\t"
    "v_mfma_f32_16x16x32_bf16 a[40:43], a[216:219], a[112:115], a[40:43]\n\t"
    "ds_read_b64_tr_b16 a[208:209], v10 offset:37056\n\t"
    "v_rcp_f32_e32 v137, v137\n\t"
    "v_mfma_f32_16x16x32_bf16 a[40:43], a[220:223], a[116:119], a[40:43]\n\t"
    "ds_read_b64_tr_b16 a[210:211], v10 offset:40640\n\t"
    "v_rcp_f32_e32 v138, v138\n\t"
    "v_rcp_f32_e32 v139, v139\n\t"
    "v_mfma_f32_16x16x32_bf16 a[40:43], a[224:227], a[144:147], a[40:43]\n\t"
    "v_rcp_f32_e32 v140, v140\n\t"
    "v_rcp_f32_e32 v141, v141\n\t"
    "v_mfma_f32_16x16x32_bf16 a[40:43], a[228:231], a[148:151], a[40:43]\n\t"
    "ds_read_b64_tr_b16 a[212:213], v10 offset:44224\n\t"
    "v_rcp_f32_e32 v142, v142\n\t"
    "v_mfma_f32_16x16x32_bf16 a[40:43], a[232:235], a[176:179], a[40:43]\n\t"
    "ds_read_b64_tr_b16 a[214:215], v10 offset:47808\n\t"
    "v_rcp_f32_e32 v143, v143\n\t"
    "v_mul_f32_e32 v136, v136, v184\n\t"
    "v_mul_f32_e32 v137, v137, v185\n\t"
    "v_mfma_f32_16x16x32_bf16 a[40:43], a[236:239], a[180:183], a[40:43]\n\t"
    "v_mul_f32_e32 v138, v138, v186\n\t"
    "v_mul_f32_e32 v139, v139, v187\n\t"
    "v_mul_f32_e32 v140, v140, v188\n\t"
    "v_mul_f32_e32 v141, v141, v189\n\t"
    "v_mfma_f32_16x16x32_bf16 a[44:47], a[216:219], a[120:123], a[44:47]\n\t"
    "v_mul_f32_e32 v142, v142, v190\n\t"
    "v_mul_f32_e32 v143, v143, v191\n\t"
    "v_cvt_pk_bf16_f32 v136, v136, v137\n\t"
    "v_mfma_f32_16x16x32_bf16 a[44:47], a[220:223], a[124:127], a[44:47]\n\t"
    "v_cvt_pk_bf16_f32 v137, v138, v139\n\t"
    "v_cvt_pk_bf16_f32 v138, v140, v141\n\t"
    "v_cvt_pk_bf16_f32 v139, v142, v143\n\t"
    "v_mfma_f32_16x16x32_bf16 a[44:47], a[224:227], a[152:155], a[44:47]\n\t"
    "v_permlane16_swap_b32_e32 v136, v138\n\t"
    "v_mfma_f32_16x16x32_bf16 a[44:47], a[228:231], a[156:159], a[44:47]\n\t"
    "v_permlane16_swap_b32_e32 v137, v139\n\t"
    "v_mfma_f32_16x16x32_bf16 a[44:47], a[232:235], a[184:187], a[44:47]\n\t"
    "buffer_store_dwordx4 v[136:139], v22, s[16:19], s96 offen offset:2048 sc1\n\t"
    "s_add_u32 s96, s96, s97\n\t"
    "v_mfma_f32_16x16x32_bf16 a[44:47], a[236:239], a[188:191], a[44:47]\n\t"
    "s_waitcnt lgkmcnt(8)\n\t"
    "v_mfma_f32_16x16x32_bf16 v[96:99], a[192:195], a[96:99], v[96:99]\n\t"
    "ds_read_b64_tr_b16 a[216:217], v9 offset:51392\n\t"
    "v_mfma_f32_16x16x32_bf16 v[96:99], a[196:199], a[100:103], v[96:99]\n\t"
    "ds_read_b64_tr_b16 a[218:219], v9 offset:54976\n\t"
    "s_waitcnt lgkmcnt(6)\n\t"
    "v_mfma_f32_16x16x32_bf16 v[96:99], a[200:203], a[128:131], v[96:99]\n\t"
    "v_mul_f32_e32 v112, s92, v112\n\t"
    "v_mul_f32_e32 v113, s92, v113\n\t"
    "v_mul_f32_e32 v114, s92, v114\n\t"
    "v_mul_f32_e32 v115, s92, v115\n\t"
    "v_mfma_f32_16x16x32_bf16 v[96:99], a[204:207], a[132:135], v[96:99]\n\t"
    "ds_read_b64_tr_b16 a[220:221], v9 offset:58560\n\t"
    "v_mul_f32_e32 v116, s92, v116\n\t"
    "v_mul_f32_e32 v117, s92, v117\n\t"
    "v_mul_f32_e32 v118, s92, v118\n\t"
    "s_waitcnt lgkmcnt(3)\n\t"
    "v_mfma_f32_16x16x32_bf16 v[96:99], a[208:211], a[160:163], v[96:99]\n\t"
    "ds_read_b64_tr_b16 a[222:223], v9 offset:62144\n\t"
    "v_mul_f32_e32 v119, s92, v119\n\t"
    "v_exp_f32_e32 v112, v112\n\t"
    "v_mfma_f32_16x16x32_bf16 v[96:99], a[212:215], a[164:167], v[96:99]\n\t"
    "v_exp_f32_e32 v113, v113\n\t"
    "v_exp_f32_e32 v114, v114\n\t"
    "v_mfma_f32_16x16x32_bf16 v[100:103], a[192:195], a[104:107], v[100:103]\n\t"
    "ds_read_b64_tr_b16 a[224:225], v10 offset:14528\n\t"
    "v_exp_f32_e32 v115, v115\n\t"
    "v_mfma_f32_16x16x32_bf16 v[100:103], a[196:199], a[108:111], v[100:103]\n\t"
    "ds_read_b64_tr_b16 a[226:227], v10 offset:18112\n\t"
    "v_exp_f32_e32 v116, v116\n\t"
    "v_exp_f32_e32 v117, v117\n\t"
    "v_mfma_f32_16x16x32_bf16 v[100:103], a[200:203], a[136:139], v[100:103]\n\t"
    "v_exp_f32_e32 v118, v118\n\t"
    "v_exp_f32_e32 v119, v119\n\t"
    "v_mfma_f32_16x16x32_bf16 v[100:103], a[204:207], a[140:143], v[100:103]\n\t"
    "ds_read_b64_tr_b16 a[228:229], v10 offset:21696\n\t"
    "v_add_f32_e32 v112, 1.0, v112\n\t"
    "v_add_f32_e32 v113, 1.0, v113\n\t"
    "v_add_f32_e32 v114, 1.0, v114\n\t"
    "v_mfma_f32_16x16x32_bf16 v[100:103], a[208:211], a[168:171], v[100:103]\n\t"
    "ds_read_b64_tr_b16 a[230:231], v10 offset:25280\n\t"
    "v_add_f32_e32 v115, 1.0, v115\n\t"
    "v_add_f32_e32 v116, 1.0, v116\n\t"
    "v_add_f32_e32 v117, 1.0, v117\n\t"
    "v_mfma_f32_16x16x32_bf16 v[100:103], a[212:215], a[172:175], v[100:103]\n\t"
    "v_add_f32_e32 v118, 1.0, v118\n\t"
    "v_add_f32_e32 v119, 1.0, v119\n\t"
    "v_accvgpr_read_b32 v176, a32\n\t"
    "v_accvgpr_read_b32 v177, a33\n\t"
    "v_mfma_f32_16x16x32_bf16 v[104:107], a[192:195], a[112:115], v[104:107]\n\t"
    "ds_read_b64_tr_b16 a[232:233], v10 offset:51392\n\t"
    "v_accvgpr_read_b32 v178, a34\n\t"
    "v_accvgpr_read_b32 v179, a35\n\t"
    "v_accvgpr_read_b32 v180, a36\n\t"
    "v_mfma_f32_16x16x32_bf16 v[104:107], a[196:199], a[116:119], v[104:107]\n\t"
    "ds_read_b64_tr_b16 a[234:235], v10 offset:54976\n\t"
    "v_accvgpr_read_b32 v181, a37\n\t"
    "v_accvgpr_read_b32 v182, a38\n\t"
    "v_accvgpr_read_b32 v183, a39\n\t"
    "v_mfma_f32_16x16x32_bf16 v[104:107], a[200:203], a[144:147], v[104:107]\n\t"
    "v_rcp_f32_e32 v112, v112\n\t"
    "v_rcp_f32_e32 v113, v113\n\t"
    "v_mfma_f32_16x16x32_bf16 v[104:107], a[204:207], a[148:151], v[104:107]\n\t"
    "ds_read_b64_tr_b16 a[236:237], v10 offset:58560\n\t"
    "v_rcp_f32_e32 v114, v114\n\t"
    "v_mfma_f32_16x16x32_bf16 v[104:107], a[208:211], a[176:179], v[104:107]\n\t"
    "ds_read_b64_tr_b16 a[238:239], v10 offset:62144\n\t"
    "v_rcp_f32_e32 v115, v115\n\t"
    "v_rcp_f32_e32 v116, v116\n\t"
    "v_mfma_f32_16x16x32_bf16 v[104:107], a[212:215], a[180:183], v[104:107]\n\t"
    "v_rcp_f32_e32 v117, v117\n\t"
    "v_rcp_f32_e32 v118, v118\n\t"
    "v_mfma_f32_16x16x32_bf16 v[108:111], a[192:195], a[120:123], v[108:111]\n\t"
    "v_rcp_f32_e32 v119, v119\n\t"
    "v_mul_f32_e32 v112, v112, v176\n\t"
    "v_mul_f32_e32 v113, v113, v177\n\t"
    "v_mfma_f32_16x16x32_bf16 v[108:111], a[196:199], a[124:127], v[108:111]\n\t"
    "v_mul_f32_e32 v114, v114, v178\n\t"
    "v_mul_f32_e32 v115, v115, v179\n\t"
    "v_mul_f32_e32 v116, v116, v180\n\t"
    "v_mul_f32_e32 v117, v117, v181\n\t"
    "v_mfma_f32_16x16x32_bf16 v[108:111], a[200:203], a[152:155], v[108:111]\n\t"
    "v_mul_f32_e32 v118, v118, v182\n\t"
    "v_mul_f32_e32 v119, v119, v183\n\t"
    "v_cvt_pk_bf16_f32 v112, v112, v113\n\t"
    "v_cvt_pk_bf16_f32 v113, v114, v115\n\t"
    "v_mfma_f32_16x16x32_bf16 v[108:111], a[204:207], a[156:159], v[108:111]\n\t"
    "v_cvt_pk_bf16_f32 v114, v116, v117\n\t"
    "v_cvt_pk_bf16_f32 v115, v118, v119\n\t"
    "s_nop 0\n\t"
    "v_permlane16_swap_b32_e32 v112, v114\n\t"
    "v_mfma_f32_16x16x32_bf16 v[108:111], a[208:211], a[184:187], v[108:111]\n\t"
    "v_permlane16_swap_b32_e32 v113, v115\n\t"
    "v_mfma_f32_16x16x32_bf16 v[108:111], a[212:215], a[188:191], v[108:111]\n\t"
    "buffer_store_dwordx4 v[112:115], v22, s[16:19], s96 offen offset:0 sc1\n\t"
    "v_mul_f32_e32 v120, s92, v120\n\t"
    "v_mul_f32_e32 v121, s92, v121\n\t"
    "s_waitcnt lgkmcnt(8)\n\t"
    "v_mfma_f32_16x16x32_bf16 a[48:51], a[216:219], a[96:99], a[48:51]\n\t"
    "ds_read_b128 a[192:195], v11 offset:36864\n\t"
    "v_mul_f32_e32 v122, s92, v122\n\t"
    "v_mul_f32_e32 v123, s92, v123\n\t"
    "v_mul_f32_e32 v124, s92, v124\n\t"
    "v_mfma_f32_16x16x32_bf16 a[48:51], a[220:223], a[100:103], a[48:51]\n\t"
    "v_mul_f32_e32 v125, s92, v125\n\t"
    "v_mul_f32_e32 v126, s92, v126\n\t"
    "v_mul_f32_e32 v127, s92, v127\n\t"
    "s_waitcnt lgkmcnt(5)\n\t"
    "v_mfma_f32_16x16x32_bf16 a[48:51], a[224:227], a[128:131], a[48:51]\n\t"
    "v_exp_f32_e32 v120, v120\n\t"
    "v_exp_f32_e32 v121, v121\n\t"
    "v_mfma_f32_16x16x32_bf16 a[48:51], a[228:231], a[132:135], a[48:51]\n\t"
    "ds_read_b128 a[196:199], v12 offset:36864\n\t"
    "v_exp_f32_e32 v122, v122\n\t"
    "v_exp_f32_e32 v123, v123\n\t"
    "s_waitcnt lgkmcnt(2)\n\t"
    "v_mfma_f32_16x16x32_bf16 a[48:51], a[232:235], a[160:163], a[48:51]\n\t"
    "v_exp_f32_e32 v124, v124\n\t"
    "v_exp_f32_e32 v125, v125\n\t"
    "v_mfma_f32_16x16x32_bf16 a[48:51], a[236:239], a[164:167], a[48:51]\n\t"
    "v_exp_f32_e32 v126, v126\n\t"
    "v_exp_f32_e32 v127, v127\n\t"
    "v_mfma_f32_16x16x32_bf16 a[52:55], a[216:219], a[104:107], a[52:55]\n\t"
    "ds_read_b128 a[200:203], v13 offset:0\n\t"
    "v_add_f32_e32 v120, 1.0, v120\n\t"
    "v_add_f32_e32 v121, 1.0, v121\n\t"
    "v_add_f32_e32 v122, 1.0, v122\n\t"
    "v_mfma_f32_16x16x32_bf16 a[52:55], a[220:223], a[108:111], a[52:55]\n\t"
    "v_add_f32_e32 v123, 1.0, v123\n\t"
    "v_add_f32_e32 v124, 1.0, v124\n\t"
    "v_add_f32_e32 v125, 1.0, v125\n\t"
    "v_add_f32_e32 v126, 1.0, v126\n\t"
    "v_mfma_f32_16x16x32_bf16 a[52:55], a[224:227], a[136:139], a[52:55]\n\t"
    "v_add_f32_e32 v127, 1.0, v127\n\t"
    "v_accvgpr_read_b32 v184, a40\n\t"
    "v_accvgpr_read_b32 v185, a41\n\t"
    "v_accvgpr_read_b32 v186, a42\n\t"
    "v_mfma_f32_16x16x32_bf16 a[52:55], a[228:231], a[140:143], a[52:55]\n\t"
    "ds_read_b128 a[204:207], v14 offset:0\n\t"
    "v_accvgpr_read_b32 v187, a43\n\t"
    "v_accvgpr_read_b32 v188, a44\n\t"
    "v_accvgpr_read_b32 v189, a45\n\t"
    "v_mfma_f32_16x16x32_bf16 a[52:55], a[232:235], a[168:171], a[52:55]\n\t"
    "v_accvgpr_read_b32 v190, a46\n\t"
    "v_accvgpr_read_b32 v191, a47\n\t"
    "v_rcp_f32_e32 v120, v120\n\t"
    "v_mfma_f32_16x16x32_bf16 a[52:55], a[236:239], a[172:175], a[52:55]\n\t"
    "v_rcp_f32_e32 v121, v121\n\t"
    "v_rcp_f32_e32 v122, v122\n\t"
    "v_mfma_f32_16x16x32_bf16 a[56:59], a[216:219], a[112:115], a[56:59]\n\t"
    "ds_read_b128 a[208:211], v13 offset:36864\n\t"
    "v_rcp_f32_e32 v123, v123\n\t"
    "v_mfma_f32_16x16x32_bf16 a[56:59], a[220:223], a[116:119], a[56:59]\n\t"
    "v_rcp_f32_e32 v124, v124\n\t"
    "v_mfma_f32_16x16x32_bf16 a[56:59], a[224:227], a[144:147], a[56:59]\n\t"
    "v_rcp_f32_e32 v125, v125\n\t"
    "v_rcp_f32_e32 v126, v126\n\t"
    "v_mfma_f32_16x16x32_bf16 a[56:59], a[228:231], a[148:151], a[56:59]\n\t"
    "ds_read_b128 a[212:215], v14 offset:36864\n\t"
    "v_rcp_f32_e32 v127, v127\n\t"
    "v_mfma_f32_16x16x32_bf16 a[56:59], a[232:235], a[176:179], a[56:59]\n\t"
    "v_mul_f32_e32 v120, v120, v184\n\t"
    "v_mul_f32_e32 v121, v121, v185\n\t"
    "v_mul_f32_e32 v122, v122, v186\n\t"
    "v_mfma_f32_16x16x32_bf16 a[56:59], a[236:239], a[180:183], a[56:59]\n\t"
    "v_mul_f32_e32 v123, v123, v187\n\t"
    "v_mul_f32_e32 v124, v124, v188\n\t"
    "v_mul_f32_e32 v125, v125, v189\n\t"
    "v_mfma_f32_16x16x32_bf16 a[60:63], a[216:219], a[120:123], a[60:63]\n\t"
    "v_mul_f32_e32 v126, v126, v190\n\t"
    "v_mul_f32_e32 v127, v127, v191\n\t"
    "v_cvt_pk_bf16_f32 v120, v120, v121\n\t"
    "v_mfma_f32_16x16x32_bf16 a[60:63], a[220:223], a[124:127], a[60:63]\n\t"
    "v_cvt_pk_bf16_f32 v121, v122, v123\n\t"
    "v_cvt_pk_bf16_f32 v122, v124, v125\n\t"
    "v_cvt_pk_bf16_f32 v123, v126, v127\n\t"
    "v_mfma_f32_16x16x32_bf16 a[60:63], a[224:227], a[152:155], a[60:63]\n\t"
    "v_permlane16_swap_b32_e32 v120, v122\n\t"
    "v_mfma_f32_16x16x32_bf16 a[60:63], a[228:231], a[156:159], a[60:63]\n\t"
    "v_permlane16_swap_b32_e32 v121, v123\n\t"
    "v_mfma_f32_16x16x32_bf16 a[60:63], a[232:235], a[184:187], a[60:63]\n\t"
    "buffer_store_dwordx4 v[120:123], v22, s[16:19], s96 offen offset:2048 sc1\n\t"
    "s_add_u32 s96, s96, s98\n\t"
    "v_mfma_f32_16x16x32_bf16 a[60:63], a[236:239], a[188:191], a[60:63]\n\t"
    "s_waitcnt lgkmcnt(4)\n\t"
    "v_mfma_f32_16x16x32_bf16 v[32:35], a[96:99], a[192:195], v[32:35]\n\t"
    "ds_read_b128 a[216:219], v11 offset:40960\n\t"
    "v_mfma_f32_16x16x32_bf16 v[32:35], a[100:103], a[196:199], v[32:35]\n\t"
    "s_waitcnt lgkmcnt(3)\n\t"
    "v_mfma_f32_16x16x32_bf16 v[32:35], a[128:131], a[200:203], v[32:35]\n\t"
    "v_mul_f32_e32 v96, s92, v96\n\t"
    "v_mul_f32_e32 v97, s92, v97\n\t"
    "v_mul_f32_e32 v98, s92, v98\n\t"
    "v_mul_f32_e32 v99, s92, v99\n\t"
    "v_mfma_f32_16x16x32_bf16 v[32:35], a[132:135], a[204:207], v[32:35]\n\t"
    "ds_read_b128 a[220:223], v12 offset:40960\n\t"
    "v_mul_f32_e32 v100, s92, v100\n\t"
    "v_mul_f32_e32 v101, s92, v101\n\t"
    "v_mul_f32_e32 v102, s92, v102\n\t"
    "s_waitcnt lgkmcnt(2)\n\t"
    "v_mfma_f32_16x16x32_bf16 v[32:35], a[160:163], a[208:211], v[32:35]\n\t"
    "v_mul_f32_e32 v103, s92, v103\n\t"
    "v_exp_f32_e32 v96, v96\n\t"
    "v_mfma_f32_16x16x32_bf16 v[32:35], a[164:167], a[212:215], v[32:35]\n\t"
    "v_exp_f32_e32 v97, v97\n\t"
    "v_exp_f32_e32 v98, v98\n\t"
    "v_mfma_f32_16x16x32_bf16 v[36:39], a[104:107], a[192:195], v[36:39]\n\t"
    "ds_read_b128 a[224:227], v13 offset:4096\n\t"
    "v_exp_f32_e32 v99, v99\n\t"
    "v_exp_f32_e32 v100, v100\n\t"
    "v_mfma_f32_16x16x32_bf16 v[36:39], a[108:111], a[196:199], v[36:39]\n\t"
    "v_exp_f32_e32 v101, v101\n\t"
    "v_exp_f32_e32 v102, v102\n\t"
    "v_mfma_f32_16x16x32_bf16 v[36:39], a[136:139], a[200:203], v[36:39]\n\t"
    "v_exp_f32_e32 v103, v103\n\t"
    "v_add_f32_e32 v96, 1.0, v96\n\t"
    "v_add_f32_e32 v97, 1.0, v97\n\t"
    "v_mfma_f32_16x16x32_bf16 v[36:39], a[140:143], a[204:207], v[36:39]\n\t"
    "ds_read_b128 a[228:231], v14 offset:4096\n\t"
    "v_add_f32_e32 v98, 1.0, v98\n\t"
    "v_add_f32_e32 v99, 1.0, v99\n\t"
    "v_add_f32_e32 v100, 1.0, v100\n\t"
    "v_mfma_f32_16x16x32_bf16 v[36:39], a[168:171], a[208:211], v[36:39]\n\t"
    "v_add_f32_e32 v101, 1.0, v101\n\t"
    "v_add_f32_e32 v102, 1.0, v102\n\t"
    "v_add_f32_e32 v103, 1.0, v103\n\t"
    "v_accvgpr_read_b32 v176, a48\n\t"
    "v_mfma_f32_16x16x32_bf16 v[36:39], a[172:175], a[212:215], v[36:39]\n\t"
    "v_accvgpr_read_b32 v177, a49\n\t"
    "v_accvgpr_read_b32 v178, a50\n\t"
    "v_accvgpr_read_b32 v179, a51\n\t"
    "v_accvgpr_read_b32 v180, a52\n\t"
    "v_mfma_f32_16x16x32_bf16 v[40:43], a[112:115], a[192:195], v[40:43]\n\t"
    "ds_read_b128 a[232:235], v13 offset:40960\n\t"
    "v_accvgpr_read_b32 v181, a53\n\t"
    "v_accvgpr_read_b32 v182, a54\n\t"
    "v_accvgpr_read_b32 v183, a55\n\t"
    "v_mfma_f32_16x16x32_bf16 v[40:43], a[116:119], a[196:199], v[40:43]\n\t"
    "v_rcp_f32_e32 v96, v96\n\t"
    "v_rcp_f32_e32 v97, v97\n\t"
    "v_mfma_f32_16x16x32_bf16 v[40:43], a[144:147], a[200:203], v[40:43]\n\t"
    "v_rcp_f32_e32 v98, v98\n\t"
    "v_rcp_f32_e32 v99, v99\n\t"
    "v_mfma_f32_16x16x32_bf16 v[40:43], a[148:151], a[204:207], v[40:43]\n\t"
    "ds_read_b128 a[236:239], v14 offset:40960\n\t"
    "v_rcp_f32_e32 v100, v100\n\t"
    "v_mfma_f32_16x16x32_bf16 v[40:43], a[176:179], a[208:211], v[40:43]\n\t"
    "v_rcp_f32_e32 v101, v101\n\t"
    "v_rcp_f32_e32 v102, v102\n\t"
    "v_mfma_f32_16x16x32_bf16 v[40:43], a[180:183], a[212:215], v[40:43]\n\t"
    "v_rcp_f32_e32 v103, v103\n\t"
    "v_mul_f32_e32 v96, v96, v176\n\t"
    "v_mul_f32_e32 v97, v97, v177\n\t"
    "v_mul_f32_e32 v98, v98, v178\n\t"
    "v_mfma_f32_16x16x32_bf16 v[44:47], a[120:123], a[192:195], v[44:47]\n\t"
    "v_mul_f32_e32 v99, v99, v179\n\t"
    "v_mul_f32_e32 v100, v100, v180\n\t"
    "v_mul_f32_e32 v101, v101, v181\n\t"
    "v_mul_f32_e32 v102, v102, v182\n\t"
    "v_mfma_f32_16x16x32_bf16 v[44:47], a[124:127], a[196:199], v[44:47]\n\t"
    "v_mul_f32_e32 v103, v103, v183\n\t"
    "v_cvt_pk_bf16_f32 v96, v96, v97\n\t"
    "v_cvt_pk_bf16_f32 v97, v98, v99\n\t"
    "v_cvt_pk_bf16_f32 v98, v100, v101\n\t"
    "v_mfma_f32_16x16x32_bf16 v[44:47], a[152:155], a[200:203], v[44:47]\n\t"
    "v_cvt_pk_bf16_f32 v99, v102, v103\n\t"
    "v_permlane16_swap_b32_e32 v96, v98\n\t"
    "v_mfma_f32_16x16x32_bf16 v[44:47], a[156:159], a[204:207], v[44:47]\n\t"
    "v_permlane16_swap_b32_e32 v97, v99\n\t"
    "v_mfma_f32_16x16x32_bf16 v[44:47], a[184:187], a[208:211], v[44:47]\n\t"
    "buffer_store_dwordx4 v[96:99], v22, s[16:19], s96 offen offset:0 sc1\n\t"
    "v_mul_f32_e32 v104, s92, v104\n\t"
    "v_mul_f32_e32 v105, s92, v105\n\t"
    "v_mfma_f32_16x16x32_bf16 v[44:47], a[188:191], a[212:215], v[44:47]\n\t"
    "v_mul_f32_e32 v106, s92, v106\n\t"
    "v_mul_f32_e32 v107, s92, v107\n\t"
    "v_mul_f32_e32 v108, s92, v108\n\t"
    "v_mul_f32_e32 v109, s92, v109\n\t"
    "s_waitcnt lgkmcnt(4)\n\t"
    "v_mfma_f32_16x16x32_bf16 v[64:67], a[96:99], a[216:219], v[64:67]\n\t"
    "ds_read_b128 a[192:195], v11 offset:38912\n\t"
    "v_mul_f32_e32 v110, s92, v110\n\t"
    "v_mul_f32_e32 v111, s92, v111\n\t"
    "v_mfma_f32_16x16x32_bf16 v[64:67], a[100:103], a[220:223], v[64:67]\n\t"
    "v_exp_f32_e32 v104, v104\n\t"
    "v_exp_f32_e32 v105, v105\n\t"
    "s_waitcnt lgkmcnt(3)\n\t"
    "v_mfma_f32_16x16x32_bf16 v[64:67], a[128:131], a[224:227], v[64:67]\n\t"
    "v_exp_f32_e32 v106, v106\n\t"
    "v_exp_f32_e32 v107, v107\n\t"
    "v_mfma_f32_16x16x32_bf16 v[64:67], a[132:135], a[228:231], v[64:67]\n\t"
    "ds_read_b128 a[196:199], v12 offset:38912\n\t"
    "v_exp_f32_e32 v108, v108\n\t"
    "v_exp_f32_e32 v109, v109\n\t"
    "s_waitcnt lgkmcnt(2)\n\t"
    "v_mfma_f32_16x16x32_bf16 v[64:67], a[160:163], a[232:235], v[64:67]\n\t"
    "v_exp_f32_e32 v110, v110\n\t"
    "v_exp_f32_e32 v111, v111\n\t"
    "v_mfma_f32_16x16x32_bf16 v[64:67], a[164:167], a[236:239], v[64:67]\n\t"
    "v_add_f32_e32 v104, 1.0, v104\n\t"
    "v_add_f32_e32 v105, 1.0, v105\n\t"
    "v_add_f32_e32 v106, 1.0, v106\n\t"
    "v_add_f32_e32 v107, 1.0, v107\n\t"
    "v_mfma_f32_16x16x32_bf16 v[68:71], a[104:107], a[216:219], v[68:71]\n\t"
    "ds_read_b128 a[200:203], v13 offset:2048\n\t"
    "v_add_f32_e32 v108, 1.0, v108\n\t"
    "v_add_f32_e32 v109, 1.0, v109\n\t"
    "v_add_f32_e32 v110, 1.0, v110\n\t"
    "v_mfma_f32_16x16x32_bf16 v[68:71], a[108:111], a[220:223], v[68:71]\n\t"
    "v_add_f32_e32 v111, 1.0, v111\n\t"
    "v_accvgpr_read_b32 v184, a56\n\t"
    "v_accvgpr_read_b32 v185, a57\n\t"
    "v_accvgpr_read_b32 v186, a58\n\t"
    "v_mfma_f32_16x16x32_bf16 v[68:71], a[136:139], a[224:227], v[68:71]\n\t"
    "v_accvgpr_read_b32 v187, a59\n\t"
    "v_accvgpr_read_b32 v188, a60\n\t"
    "v_accvgpr_read_b32 v189, a61\n\t"
    "v_accvgpr_read_b32 v190, a62\n\t"
    "v_mfma_f32_16x16x32_bf16 v[68:71], a[140:143], a[228:231], v[68:71]\n\t"
    "ds_read_b128 a[204:207], v14 offset:2048\n\t"
    "v_accvgpr_read_b32 v191, a63\n\t"
    "v_rcp_f32_e32 v104, v104\n\t"
    "v_mfma_f32_16x16x32_bf16 v[68:71], a[168:171], a[232:235], v[68:71]\n\t"
    "v_rcp_f32_e32 v105, v105\n\t"
    "v_rcp_f32_e32 v106, v106\n\t"
    "v_mfma_f32_16x16x32_bf16 v[68:71], a[172:175], a[236:239], v[68:71]\n\t"
    "v_rcp_f32_e32 v107, v107\n\t"
    "v_rcp_f32_e32 v108, v108\n\t"
    "v_mfma_f32_16x16x32_bf16 v[72:75], a[112:115], a[216:219], v[72:75]\n\t"
    "ds_read_b128 a[208:211], v13 offset:38912\n\t"
    "v_rcp_f32_e32 v109, v109\n\t"
    "v_mfma_f32_16x16x32_bf16 v[72:75], a[116:119], a[220:223], v[72:75]\n\t"
    "v_rcp_f32_e32 v110, v110\n\t"
    "v_rcp_f32_e32 v111, v111\n\t"
    "v_mfma_f32_16x16x32_bf16 v[72:75], a[144:147], a[224:227], v[72:75]\n\t"
    "v_mul_f32_e32 v104, v104, v184\n\t"
    "v_mul_f32_e32 v105, v105, v185\n\t"
    "v_mul_f32_e32 v106, v106, v186\n\t"
    "v_mfma_f32_16x16x32_bf16 v[72:75], a[148:151], a[228:231], v[72:75]\n\t"
    "ds_read_b128 a[212:215], v14 offset:38912\n\t"
    "v_mul_f32_e32 v107, v107, v187\n\t"
    "v_mul_f32_e32 v108, v108, v188\n\t"
    "v_mfma_f32_16x16x32_bf16 v[72:75], a[176:179], a[232:235], v[72:75]\n\t"
    "v_mul_f32_e32 v109, v109, v189\n\t"
    "v_mul_f32_e32 v110, v110, v190\n\t"
    "v_mul_f32_e32 v111, v111, v191\n\t"
    "v_mfma_f32_16x16x32_bf16 v[72:75], a[180:183], a[236:239], v[72:75]\n\t"
    "v_cvt_pk_bf16_f32 v104, v104, v105\n\t"
    "v_cvt_pk_bf16_f32 v105, v106, v107\n\t"
    "v_cvt_pk_bf16_f32 v106, v108, v109\n\t"
    "v_mfma_f32_16x16x32_bf16 v[76:79], a[120:123], a[216:219], v[76:79]\n\t"
    "v_cvt_pk_bf16_f32 v107, v110, v111\n\t"
    "v_permlane16_swap_b32_e32 v104, v106\n\t"
    "v_mfma_f32_16x16x32_bf16 v[76:79], a[124:127], a[220:223], v[76:79]\n\t"
    "v_permlane16_swap_b32_e32 v105, v107\n\t"
    "v_mfma_f32_16x16x32_bf16 v[76:79], a[152:155], a[224:227], v[76:79]\n\t"
    "buffer_store_dwordx4 v[104:107], v22, s[16:19], s96 offen offset:2048 sc1\n\t"
    "s_add_u32 s96, s96, s97\n\t"
    "v_mfma_f32_16x16x32_bf16 v[76:79], a[156:159], a[228:231], v[76:79]\n\t"
    "v_mfma_f32_16x16x32_bf16 v[76:79], a[184:187], a[232:235], v[76:79]\n\t"
    "v_mov_b32_e32 v238, s82\n\t"
    "v_mov_b32_e32 v239, s83\n\t"
    "v_mfma_f32_16x16x32_bf16 v[76:79], a[188:191], a[236:239], v[76:79]\n\t"
    "v_lshl_or_b32 v0, s87, 10, v31\n\t"
    "v_cmp_eq_u32_e32 vcc, 0, v0\n\t"
    "s_waitcnt lgkmcnt(4)\n\t"
    "v_mfma_f32_16x16x32_bf16 v[48:51], a[96:99], a[192:195], v[48:51]\n\t"
    "ds_read_b128 a[216:219], v11 offset:43008\n\t"
    "v_bfrev_b32_e32 v0, 1\n\t"
    "s_nop 0\n\t"
    "v_mfma_f32_16x16x32_bf16 v[48:51], a[100:103], a[196:199], v[48:51]\n\t"
    "v_cndmask_b32_e64 v0, v0, 0, vcc\n\t"
    "s_add_u32 s90, s91, 35672064\n\t"
    "s_waitcnt lgkmcnt(3)\n\t"
    "v_mfma_f32_16x16x32_bf16 v[48:51], a[128:131], a[200:203], v[48:51]\n\t"
    "v_mfma_f32_16x16x32_bf16 v[48:51], a[132:135], a[204:207], v[48:51]\n\t"
    "ds_read_b128 a[220:223], v12 offset:43008\n\t"
    "s_waitcnt lgkmcnt(2)\n\t"
    "v_mfma_f32_16x16x32_bf16 v[48:51], a[160:163], a[208:211], v[48:51]\n\t"
    "v_mfma_f32_16x16x32_bf16 v[48:51], a[164:167], a[212:215], v[48:51]\n\t"
    "v_mfma_f32_16x16x32_bf16 v[52:55], a[104:107], a[192:195], v[52:55]\n\t"
    "ds_read_b128 a[224:227], v13 offset:6144\n\t"
    "v_mfma_f32_16x16x32_bf16 v[52:55], a[108:111], a[196:199], v[52:55]\n\t"
    "v_mfma_f32_16x16x32_bf16 v[52:55], a[136:139], a[200:203], v[52:55]\n\t"
    "v_mfma_f32_16x16x32_bf16 v[52:55], a[140:143], a[204:207], v[52:55]\n\t"
    "ds_read_b128 a[228:231], v14 offset:6144\n\t"
    "v_mfma_f32_16x16x32_bf16 v[52:55], a[168:171], a[208:211], v[52:55]\n\t"
    "v_mfma_f32_16x16x32_bf16 v[52:55], a[172:175], a[212:215], v[52:55]\n\t"
    "v_mfma_f32_16x16x32_bf16 v[56:59], a[112:115], a[192:195], v[56:59]\n\t"
    "ds_read_b128 a[232:235], v13 offset:43008\n\t"
    "v_mfma_f32_16x16x32_bf16 v[56:59], a[116:119], a[196:199], v[56:59]\n\t"
    "v_mfma_f32_16x16x32_bf16 v[56:59], a[144:147], a[200:203], v[56:59]\n\t"
    "v_mfma_f32_16x16x32_bf16 v[56:59], a[148:151], a[204:207], v[56:59]\n\t"
    "ds_read_b128 a[236:239], v14 offset:43008\n\t"
    "v_mfma_f32_16x16x32_bf16 v[56:59], a[176:179], a[208:211], v[56:59]\n\t"
    "v_mfma_f32_16x16x32_bf16 v[56:59], a[180:183], a[212:215], v[56:59]\n\t"
    "v_mfma_f32_16x16x32_bf16 v[60:63], a[120:123], a[192:195], v[60:63]\n\t"
    "v_mfma_f32_16x16x32_bf16 v[60:63], a[124:127], a[196:199], v[60:63]\n\t"
    "v_mfma_f32_16x16x32_bf16 v[60:63], a[152:155], a[200:203], v[60:63]\n\t"
    "v_mfma_f32_16x16x32_bf16 v[60:63], a[156:159], a[204:207], v[60:63]\n\t"
    "v_mfma_f32_16x16x32_bf16 v[60:63], a[184:187], a[208:211], v[60:63]\n\t"
    "v_mfma_f32_16x16x32_bf16 v[60:63], a[188:191], a[212:215], v[60:63]\n\t"
    "s_waitcnt lgkmcnt(4)\n\t"
    "v_mfma_f32_16x16x32_bf16 v[80:83], a[96:99], a[216:219], v[80:83]\n\t"
    "v_mfma_f32_16x16x32_bf16 v[80:83], a[100:103], a[220:223], v[80:83]\n\t"
    "s_waitcnt lgkmcnt(2)\n\t"
    "v_mfma_f32_16x16x32_bf16 v[80:83], a[128:131], a[224:227], v[80:83]\n\t"
    "v_mfma_f32_16x16x32_bf16 v[80:83], a[132:135], a[228:231], v[80:83]\n\t"
    "s_waitcnt lgkmcnt(0)\n\t"
    "v_mfma_f32_16x16x32_bf16 v[80:83], a[160:163], a[232:235], v[80:83]\n\t"
    "v_mfma_f32_16x16x32_bf16 v[80:83], a[164:167], a[236:239], v[80:83]\n\t"
    "v_mfma_f32_16x16x32_bf16 v[84:87], a[104:107], a[216:219], v[84:87]\n\t"
    "v_mfma_f32_16x16x32_bf16 v[84:87], a[108:111], a[220:223], v[84:87]\n\t"
    "v_mfma_f32_16x16x32_bf16 v[84:87], a[136:139], a[224:227], v[84:87]\n\t"
    "v_mfma_f32_16x16x32_bf16 v[84:87], a[140:143], a[228:231], v[84:87]\n\t"
    "v_mfma_f32_16x16x32_bf16 v[84:87], a[168:171], a[232:235], v[84:87]\n\t"
    "v_mfma_f32_16x16x32_bf16 v[84:87], a[172:175], a[236:239], v[84:87]\n\t"
    "v_mfma_f32_16x16x32_bf16 v[88:91], a[112:115], a[216:219], v[88:91]\n\t"
    "v_mfma_f32_16x16x32_bf16 v[88:91], a[116:119], a[220:223], v[88:91]\n\t"
    "v_mfma_f32_16x16x32_bf16 v[88:91], a[144:147], a[224:227], v[88:91]\n\t"
    "v_mfma_f32_16x16x32_bf16 v[88:91], a[148:151], a[228:231], v[88:91]\n\t"
    "v_mfma_f32_16x16x32_bf16 v[88:91], a[176:179], a[232:235], v[88:91]\n\t"
    "v_mfma_f32_16x16x32_bf16 v[88:91], a[180:183], a[236:239], v[88:91]\n\t"
    "v_mfma_f32_16x16x32_bf16 v[92:95], a[120:123], a[216:219], v[92:95]\n\t"
    "v_mfma_f32_16x16x32_bf16 v[92:95], a[124:127], a[220:223], v[92:95]\n\t"
    "v_mfma_f32_16x16x32_bf16 v[92:95], a[152:155], a[224:227], v[92:95]\n\t"
    "v_mfma_f32_16x16x32_bf16 v[92:95], a[156:159], a[228:231], v[92:95]\n\t"
    "v_mfma_f32_16x16x32_bf16 v[92:95], a[184:187], a[232:235], v[92:95]\n\t"
    "s_waitcnt vmcnt(0)\n\t"
    "s_barrier\n\t"
    "v_mfma_f32_16x16x32_bf16 v[92:95], a[188:191], a[236:239], v[92:95]\n\t"
    "buffer_store_dwordx2 v[238:239], v0, s[16:19], s90 offen sc1\n\t"
    "s_mov_b32 s98, s38\n\t"
    "v_accvgpr_read_b32 v96, a240\n\t"
    "v_accvgpr_read_b32 v97, a241\n\t"
    "v_accvgpr_read_b32 v98, a242\n\t"
    "v_accvgpr_read_b32 v99, a243\n\t"
    "v_pk_fma_f32 v[112:113], v[32:33], v[96:97], v[34:35] op_sel:[0,0,0] op_sel_hi:[1,0,1]\n\t"
    "v_pk_fma_f32 v[116:117], v[48:49], v[98:99], v[50:51] op_sel:[0,0,0] op_sel_hi:[1,0,1]\n\t"
    "v_pk_fma_f32 v[114:115], v[64:65], v[96:97], v[66:67] op_sel:[0,0,0] op_sel_hi:[1,0,1]\n\t"
    "v_pk_fma_f32 v[118:119], v[80:81], v[98:99], v[82:83] op_sel:[0,0,0] op_sel_hi:[1,0,1]\n\t"
    "v_pk_fma_f32 v[112:113], v[64:65], v[96:97], v[112:113] op_sel:[0,1,0] op_sel_hi:[1,1,1] neg_lo:[0,1,0] neg_hi:[0,1,0]\n\t"
    "v_pk_fma_f32 v[116:117], v[80:81], v[98:99], v[116:117] op_sel:[0,1,0] op_sel_hi:[1,1,1] neg_lo:[0,1,0] neg_hi:[0,1,0]\n\t"
    "v_pk_fma_f32 v[114:115], v[32:33], v[96:97], v[114:115] op_sel:[0,1,0] op_sel_hi:[1,1,1]\n\t"
    "v_pk_fma_f32 v[118:119], v[48:49], v[98:99], v[118:119] op_sel:[0,1,0] op_sel_hi:[1,1,1]\n\t"
    "v_accvgpr_read_b32 v104, a248\n\t"
    "v_accvgpr_read_b32 v105, a249\n\t"
    "v_accvgpr_read_b32 v106, a250\n\t"
    "v_accvgpr_read_b32 v107, a251\n\t"
    "v_pk_fma_f32 v[120:121], v[112:113], v[96:97], v[36:37] op_sel:[0,0,0] op_sel_hi:[1,0,1]\n\t"
    "v_pk_fma_f32 v[124:125], v[116:117], v[98:99], v[52:53] op_sel:[0,0,0] op_sel_hi:[1,0,1]\n\t"
    "v_pk_fma_f32 v[122:123], v[114:115], v[96:97], v[68:69] op_sel:[0,0,0] op_sel_hi:[1,0,1]\n\t"
    "v_pk_fma_f32 v[126:127], v[118:119], v[98:99], v[84:85] op_sel:[0,0,0] op_sel_hi:[1,0,1]\n\t"
    "v_pk_fma_f32 v[120:121], v[114:115], v[96:97], v[120:121] op_sel:[0,1,0] op_sel_hi:[1,1,1] neg_lo:[0,1,0] neg_hi:[0,1,0]\n\t"
    "v_pk_fma_f32 v[124:125], v[118:119], v[98:99], v[124:125] op_sel:[0,1,0] op_sel_hi:[1,1,1] neg_lo:[0,1,0] neg_hi:[0,1,0]\n\t"
    "v_pk_fma_f32 v[122:123], v[112:113], v[96:97], v[122:123] op_sel:[0,1,0] op_sel_hi:[1,1,1]\n\t"
    "v_pk_fma_f32 v[126:127], v[116:117], v[98:99], v[126:127] op_sel:[0,1,0] op_sel_hi:[1,1,1]\n\t"
    "v_pk_fma_f32 v[112:113], v[120:121], v[96:97], v[38:39] op_sel:[0,0,0] op_sel_hi:[1,0,1]\n\t"
    "v_pk_fma_f32 v[116:117], v[124:125], v[98:99], v[54:55] op_sel:[0,0,0] op_sel_hi:[1,0,1]\n\t"
    "v_pk_fma_f32 v[114:115], v[122:123], v[96:97], v[70:71] op_sel:[0,0,0] op_sel_hi:[1,0,1]\n\t"
    "v_pk_fma_f32 v[118:119], v[126:127], v[98:99], v[86:87] op_sel:[0,0,0] op_sel_hi:[1,0,1]\n\t"
    "v_pk_fma_f32 v[112:113], v[122:123], v[96:97], v[112:113] op_sel:[0,1,0] op_sel_hi:[1,1,1] neg_lo:[0,1,0] neg_hi:[0,1,0]\n\t"
    "v_pk_fma_f32 v[116:117], v[126:127], v[98:99], v[116:117] op_sel:[0,1,0] op_sel_hi:[1,1,1] neg_lo:[0,1,0] neg_hi:[0,1,0]\n\t"
    "v_pk_fma_f32 v[114:115], v[120:121], v[96:97], v[114:115] op_sel:[0,1,0] op_sel_hi:[1,1,1]\n\t"
    "v_pk_fma_f32 v[118:119], v[124:125], v[98:99], v[118:119] op_sel:[0,1,0] op_sel_hi:[1,1,1]\n\t"
    "v_pk_fma_f32 v[120:121], v[112:113], v[96:97], v[40:41] op_sel:[0,0,0] op_sel_hi:[1,0,1]\n\t"
    "v_pk_fma_f32 v[124:125], v[116:117], v[98:99], v[56:57] op_sel:[0,0,0] op_sel_hi:[1,0,1]\n\t"
    "v_pk_fma_f32 v[122:123], v[114:115], v[96:97], v[72:73] op_sel:[0,0,0] op_sel_hi:[1,0,1]\n\t"
    "v_pk_fma_f32 v[126:127], v[118:119], v[98:99], v[88:89] op_sel:[0,0,0] op_sel_hi:[1,0,1]\n\t"
    "v_pk_fma_f32 v[120:121], v[114:115], v[96:97], v[120:121] op_sel:[0,1,0] op_sel_hi:[1,1,1] neg_lo:[0,1,0] neg_hi:[0,1,0]\n\t"
    "v_pk_fma_f32 v[124:125], v[118:119], v[98:99], v[124:125] op_sel:[0,1,0] op_sel_hi:[1,1,1] neg_lo:[0,1,0] neg_hi:[0,1,0]\n\t"
    "v_pk_fma_f32 v[122:123], v[112:113], v[96:97], v[122:123] op_sel:[0,1,0] op_sel_hi:[1,1,1]\n\t"
    "v_pk_fma_f32 v[126:127], v[116:117], v[98:99], v[126:127] op_sel:[0,1,0] op_sel_hi:[1,1,1]\n\t"
    "v_pk_fma_f32 v[112:113], v[120:121], v[96:97], v[42:43] op_sel:[0,0,0] op_sel_hi:[1,0,1]\n\t"
    "v_pk_fma_f32 v[116:117], v[124:125], v[98:99], v[58:59] op_sel:[0,0,0] op_sel_hi:[1,0,1]\n\t"
    "v_pk_fma_f32 v[114:115], v[122:123], v[96:97], v[74:75] op_sel:[0,0,0] op_sel_hi:[1,0,1]\n\t"
    "v_pk_fma_f32 v[118:119], v[126:127], v[98:99], v[90:91] op_sel:[0,0,0] op_sel_hi:[1,0,1]\n\t"
    "v_pk_fma_f32 v[112:113], v[122:123], v[96:97], v[112:113] op_sel:[0,1,0] op_sel_hi:[1,1,1] neg_lo:[0,1,0] neg_hi:[0,1,0]\n\t"
    "v_pk_fma_f32 v[116:117], v[126:127], v[98:99], v[116:117] op_sel:[0,1,0] op_sel_hi:[1,1,1] neg_lo:[0,1,0] neg_hi:[0,1,0]\n\t"
    "v_pk_fma_f32 v[114:115], v[120:121], v[96:97], v[114:115] op_sel:[0,1,0] op_sel_hi:[1,1,1]\n\t"
    "v_pk_fma_f32 v[118:119], v[124:125], v[98:99], v[118:119] op_sel:[0,1,0] op_sel_hi:[1,1,1]\n\t"
    "v_pk_fma_f32 v[120:121], v[112:113], v[96:97], v[44:45] op_sel:[0,0,0] op_sel_hi:[1,0,1]\n\t"
    "v_pk_fma_f32 v[124:125], v[116:117], v[98:99], v[60:61] op_sel:[0,0,0] op_sel_hi:[1,0,1]\n\t"
    "v_pk_fma_f32 v[122:123], v[114:115], v[96:97], v[76:77] op_sel:[0,0,0] op_sel_hi:[1,0,1]\n\t"
    "v_pk_fma_f32 v[126:127], v[118:119], v[98:99], v[92:93] op_sel:[0,0,0] op_sel_hi:[1,0,1]\n\t"
    "v_pk_fma_f32 v[120:121], v[114:115], v[96:97], v[120:121] op_sel:[0,1,0] op_sel_hi:[1,1,1] neg_lo:[0,1,0] neg_hi:[0,1,0]\n\t"
    "v_pk_fma_f32 v[124:125], v[118:119], v[98:99], v[124:125] op_sel:[0,1,0] op_sel_hi:[1,1,1] neg_lo:[0,1,0] neg_hi:[0,1,0]\n\t"
    "v_pk_fma_f32 v[122:123], v[112:113], v[96:97], v[122:123] op_sel:[0,1,0] op_sel_hi:[1,1,1]\n\t"
    "v_pk_fma_f32 v[126:127], v[116:117], v[98:99], v[126:127] op_sel:[0,1,0] op_sel_hi:[1,1,1]\n\t"
    "v_pk_fma_f32 v[112:113], v[120:121], v[96:97], v[46:47] op_sel:[0,0,0] op_sel_hi:[1,0,1]\n\t"
    "v_pk_fma_f32 v[116:117], v[124:125], v[98:99], v[62:63] op_sel:[0,0,0] op_sel_hi:[1,0,1]\n\t"
    "v_pk_fma_f32 v[114:115], v[122:123], v[96:97], v[78:79] op_sel:[0,0,0] op_sel_hi:[1,0,1]\n\t"
    "v_pk_fma_f32 v[118:119], v[126:127], v[98:99], v[94:95] op_sel:[0,0,0] op_sel_hi:[1,0,1]\n\t"
    "v_pk_fma_f32 v[112:113], v[122:123], v[96:97], v[112:113] op_sel:[0,1,0] op_sel_hi:[1,1,1] neg_lo:[0,1,0] neg_hi:[0,1,0]\n\t"
    "v_pk_fma_f32 v[116:117], v[126:127], v[98:99], v[116:117] op_sel:[0,1,0] op_sel_hi:[1,1,1] neg_lo:[0,1,0] neg_hi:[0,1,0]\n\t"
    "v_pk_fma_f32 v[114:115], v[120:121], v[96:97], v[114:115] op_sel:[0,1,0] op_sel_hi:[1,1,1]\n\t"
    "v_pk_fma_f32 v[118:119], v[124:125], v[98:99], v[118:119] op_sel:[0,1,0] op_sel_hi:[1,1,1]\n\t"
    "v_fma_f32 v120, v112, v104, v113\n\t"
    "v_fma_f32 v122, v114, v104, v115\n\t"
    "v_fma_f32 v120, -v114, v106, v120\n\t"
    "v_fmac_f32_e32 v122, v112, v106\n\t"
    "v_fma_f32 v121, v116, v105, v117\n\t"
    "v_fma_f32 v123, v118, v105, v119\n\t"
    "v_fma_f32 v121, -v118, v107, v121\n\t"
    "v_fmac_f32_e32 v123, v116, v107\n\t"
    "ds_write_b128 v23, v[120:123]\n\t"
    "v_pk_mul_f32 v[124:125], v[104:105], v[104:105]\n\t"
    "v_pk_mul_f32 v[126:127], v[106:107], v[104:105]\n\t"
    "v_pk_fma_f32 v[124:125], v[106:107], v[106:107], v[124:125] neg_lo:[0,1,0] neg_hi:[0,1,0]\n\t"
    "v_pk_fma_f32 v[126:127], v[104:105], v[106:107], v[126:127]\n\t"
    "v_and_b32_e32 v30, 0xf0, v31\n\t"
    "v_lshlrev_b32_e32 v30, 1, v30\n\t"
    "v_lshlrev_b32_e32 v250, 2, v31\n\t"
    "v_and_b32_e32 v250, 0x7c0, v250\n\t"
    "v_subrev_u32_e32 v8, 3840, v23\n\t"
    "s_add_u32 s95, s99, 35672064\n\t"
    "s_waitcnt lgkmcnt(0)\n\t"
    "s_barrier\n\t"
    "ds_read_b128 v[128:131], v8 offset:0\n\t"
    "ds_read_b128 v[132:135], v8 offset:256\n\t"
    "ds_read_b128 v[136:139], v8 offset:512\n\t"
    "ds_read_b128 v[140:143], v8 offset:768\n\t"
    "ds_read_b128 v[144:147], v8 offset:1024\n\t"
    "ds_read_b128 v[148:151], v8 offset:1280\n\t"
    "ds_read_b128 v[152:155], v8 offset:1536\n\t"
    "ds_read_b128 v[156:159], v8 offset:1792\n\t"
    "ds_read_b128 v[160:163], v8 offset:2048\n\t"
    "ds_read_b128 v[164:167], v8 offset:2304\n\t"
    "ds_read_b128 v[168:171], v8 offset:2560\n\t"
    "ds_read_b128 v[172:175], v8 offset:2816\n\t"
    "ds_read_b128 v[176:179], v8 offset:3072\n\t"
    "ds_read_b128 v[180:183], v8 offset:3328\n\t"
    "ds_read_b128 v[184:187], v8 offset:3584\n\t"
    "s_add_u32 m0, s68, 33792\n\t"
    "v_accvgpr_read_b32 v100, a244\n\t"
    "buffer_load_dwordx4 v2, s[44:47], s64 offen lds\n\t"
    "v_accvgpr_read_b32 v101, a245\n\t"
    "v_accvgpr_read_b32 v102, a246\n\t"
    "v_accvgpr_read_b32 v103, a247\n\t"
    "v_accvgpr_read_b32 v108, a252\n\t"
    "v_accvgpr_read_b32 v109, a253\n\t"
    "v_accvgpr_read_b32 v110, a254\n\t"
    "v_accvgpr_read_b32 v111, a255\n\t"
    "v_lshlrev_b32_e32 v228, 16, v224\n\t"
    "v_and_b32_e32 v229, 0xffff0000, v224\n\t"
    "v_lshlrev_b32_e32 v230, 16, v225\n\t"
    "v_and_b32_e32 v231, 0xffff0000, v225\n\t"
    "v_lshlrev_b32_e32 v232, 16, v226\n\t"
    "v_and_b32_e32 v233, 0xffff0000, v226\n\t"
    "v_lshlrev_b32_e32 v234, 16, v227\n\t"
    "v_and_b32_e32 v235, 0xffff0000, v227\n\t"
    "s_add_u32 m0, s69, 33792\n\t"
    "s_waitcnt lgkmcnt(11)\n\t"
    "buffer_load_dwordx4 v3, s[44:47], s64 offen lds\n\t"
    "v_pk_fma_f32 v[132:133], v[128:129], v[124:125], v[132:133]\n\t"
    "v_pk_fma_f32 v[134:135], v[130:131], v[124:125], v[134:135]\n\t"
    "v_pk_fma_f32 v[132:133], v[130:131], v[126:127], v[132:133] neg_lo:[0,1,0] neg_hi:[0,1,0]\n\t"
    "v_pk_fma_f32 v[134:135], v[128:129], v[126:127], v[134:135]\n\t"
    "v_pk_fma_f32 v[136:137], v[132:133], v[124:125], v[136:137]\n\t"
    "v_pk_fma_f32 v[138:139], v[134:135], v[124:125], v[138:139]\n\t"
    "v_pk_fma_f32 v[136:137], v[134:135], v[126:127], v[136:137] neg_lo:[0,1,0] neg_hi:[0,1,0]\n\t"
    "v_pk_fma_f32 v[138:139], v[132:133], v[126:127], v[138:139]\n\t"
    "v_pk_fma_f32 v[140:141], v[136:137], v[124:125], v[140:141]\n\t"
    "v_pk_fma_f32 v[142:143], v[138:139], v[124:125], v[142:143]\n\t"
    "v_pk_fma_f32 v[140:141], v[138:139], v[126:127], v[140:141] neg_lo:[0,1,0] neg_hi:[0,1,0]\n\t"
    "v_pk_fma_f32 v[142:143], v[136:137], v[126:127], v[142:143]\n\t"
    "s_waitcnt lgkmcnt(7)\n\t"
    "v_pk_fma_f32 v[144:145], v[140:141], v[124:125], v[144:145]\n\t"
    "v_pk_fma_f32 v[146:147], v[142:143], v[124:125], v[146:147]\n\t"
    "s_add_u32 m0, s70, 33792\n\t"
    "v_pk_fma_f32 v[144:145], v[142:143], v[126:127], v[144:145] neg_lo:[0,1,0] neg_hi:[0,1,0]\n\t"
    "buffer_load_dwordx4 v4, s[44:47], s64 offen lds\n\t"
    "v_pk_fma_f32 v[146:147], v[140:141], v[126:127], v[146:147]\n\t"
    "v_pk_fma_f32 v[148:149], v[144:145], v[124:125], v[148:149]\n\t"
    "v_pk_fma_f32 v[150:151], v[146:147], v[124:125], v[150:151]\n\t"
    "v_pk_fma_f32 v[148:149], v[146:147], v[126:127], v[148:149] neg_lo:[0,1,0] neg_hi:[0,1,0]\n\t"
    "v_pk_fma_f32 v[150:151], v[144:145], v[126:127], v[150:151]\n\t"
    "v_pk_fma_f32 v[152:153], v[148:149], v[124:125], v[152:153]\n\t"
    "v_pk_fma_f32 v[154:155], v[150:151], v[124:125], v[154:155]\n\t"
    "v_pk_fma_f32 v[152:153], v[150:151], v[126:127], v[152:153] neg_lo:[0,1,0] neg_hi:[0,1,0]\n\t"
    "v_pk_fma_f32 v[154:155], v[148:149], v[126:127], v[154:155]\n\t"
    "v_pk_fma_f32 v[156:157], v[152:153], v[124:125], v[156:157]\n\t"
    "v_pk_fma_f32 v[158:159], v[154:155], v[124:125], v[158:159]\n\t"
    "v_pk_fma_f32 v[156:157], v[154:155], v[126:127], v[156:157] neg_lo:[0,1,0] neg_hi:[0,1,0]\n\t"
    "v_pk_fma_f32 v[158:159], v[152:153], v[126:127], v[158:159]\n\t"
    "s_waitcnt lgkmcnt(3)\n\t"
    "v_pk_fma_f32 v[160:161], v[156:157], v[124:125], v[160:161]\n\t"
    "s_add_u32 m0, s71, 33792\n\t"
    "v_pk_fma_f32 v[162:163], v[158:159], v[124:125], v[162:163]\n\t"
    "buffer_load_dwordx4 v5, s[44:47], s64 offen lds\n\t"
    "v_pk_fma_f32 v[160:161], v[158:159], v[126:127], v[160:161] neg_lo:[0,1,0] neg_hi:[0,1,0]\n\t"
    "v_pk_fma_f32 v[162:163], v[156:157], v[126:127], v[162:163]\n\t"
    "v_pk_fma_f32 v[164:165], v[160:161], v[124:125], v[164:165]\n\t"
    "v_pk_fma_f32 v[166:167], v[162:163], v[124:125], v[166:167]\n\t"
    "v_pk_fma_f32 v[164:165], v[162:163], v[126:127], v[164:165] neg_lo:[0,1,0] neg_hi:[0,1,0]\n\t"
    "v_pk_fma_f32 v[166:167], v[160:161], v[126:127], v[166:167]\n\t"
    "v_pk_fma_f32 v[168:169], v[164:165], v[124:125], v[168:169]\n\t"
    "v_pk_fma_f32 v[170:171], v[166:167], v[124:125], v[170:171]\n\t"
    "v_pk_fma_f32 v[168:169], v[166:167], v[126:127], v[168:169] neg_lo:[0,1,0] neg_hi:[0,1,0]\n\t"
    "v_pk_fma_f32 v[170:171], v[164:165], v[126:127], v[170:171]\n\t"
    "v_pk_fma_f32 v[172:173], v[168:169], v[124:125], v[172:173]\n\t"
    "v_pk_fma_f32 v[174:175], v[170:171], v[124:125], v[174:175]\n\t"
    "v_pk_fma_f32 v[172:173], v[170:171], v[126:127], v[172:173] neg_lo:[0,1,0] neg_hi:[0,1,0]\n\t"
    "v_pk_fma_f32 v[174:175], v[168:169], v[126:127], v[174:175]\n\t"
    "s_add_u32 m0, s68, 50688\n\t"
    "s_waitcnt lgkmcnt(0)\n\t"
    "buffer_load_dwordx4 v2, s[44:47], s65 offen lds\n\t"
    "v_pk_fma_f32 v[176:177], v[172:173], v[124:125], v[176:177]\n\t"
    "v_pk_fma_f32 v[178:179], v[174:175], v[124:125], v[178:179]\n\t"
    "v_pk_fma_f32 v[176:177], v[174:175], v[126:127], v[176:177] neg_lo:[0,1,0] neg_hi:[0,1,0]\n\t"
    "v_pk_fma_f32 v[178:179], v[172:173], v[126:127], v[178:179]\n\t"
    "v_pk_fma_f32 v[180:181], v[176:177], v[124:125], v[180:181]\n\t"
    "v_pk_fma_f32 v[182:183], v[178:179], v[124:125], v[182:183]\n\t"
    "v_pk_fma_f32 v[180:181], v[178:179], v[126:127], v[180:181] neg_lo:[0,1,0] neg_hi:[0,1,0]\n\t"
    "v_pk_fma_f32 v[182:183], v[176:177], v[126:127], v[182:183]\n\t"
    "v_pk_fma_f32 v[12:13], v[180:181], v[124:125], v[184:185]\n\t"
    "v_pk_fma_f32 v[14:15], v[182:183], v[124:125], v[186:187]\n\t"
    "v_pk_fma_f32 v[12:13], v[182:183], v[126:127], v[12:13] neg_lo:[0,1,0] neg_hi:[0,1,0]\n\t"
    "v_pk_fma_f32 v[14:15], v[180:181], v[126:127], v[14:15]\n\t"
    "s_cmp_lg_u32 s87, 3\n\t"
    "s_cbranch_scc1 L_nopub_%=\n\t"
    "s_cmp_eq_u32 s88, 7\n\t"
    "s_cbranch_scc1 L_nopub_%=\n\t"
    "v_pk_fma_f32 v[120:121], v[12:13], v[124:125], v[120:121]\n\t"
    "v_pk_fma_f32 v[122:123], v[14:15], v[124:125], v[122:123]\n\t"
    "v_pk_fma_f32 v[120:121], v[14:15], v[126:127], v[120:121] neg_lo:[0,1,0] neg_hi:[0,1,0]\n\t"
    "v_pk_fma_f32 v[122:123], v[12:13], v[126:127], v[122:123]\n\t"
    "v_mov_b32_e32 v8, v121\n\t"
    "v_mov_b32_e32 v10, v123\n\t"
    "v_mov_b32_e32 v121, s82\n\t"
    "v_mov_b32_e32 v123, s83\n\t"
    "v_mov_b32_e32 v9, s82\n\t"
    "v_mov_b32_e32 v11, s83\n\t"
    "s_lshl_b32 s90, s91, 3\n\t"
    "s_add_u32 s90, s90, 35913728\n\t"
    "s_lshl_b64 exec, 0xffff, 48\n\t"
    "buffer_store_dwordx4 v[120:123], v30, s[16:19], s90 offen sc1\n\t"
    "buffer_store_dwordx4 v[8:11], v30, s[16:19], s90 offen offset:16 sc1\n\t"
    "s_mov_b64 exec, -1\n\t"
    "s_nop 1\n\t"
    "L_nopub_%=:\n\t"
    "s_mov_b32 s77, 0xffff0000\n\t"
    "v_accvgpr_read_b32 v129, a64\n\t"
    "v_accvgpr_read_b32 v237, a80\n\t"
    "v_lshlrev_b32_e32 v128, 16, v129\n\t"
    "v_and_b32_e32 v129, s77, v129\n\t"
    "v_lshlrev_b32_e32 v236, 16, v237\n\t"
    "v_and_b32_e32 v237, s77, v237\n\t"
    "v_pk_fma_f32 v[128:129], v[236:237], v[228:229], v[128:129]\n\t"
    "v_accvgpr_read_b32 v131, a65\n\t"
    "v_accvgpr_read_b32 v237, a81\n\t"
    "v_lshlrev_b32_e32 v130, 16, v131\n\t"
    "v_and_b32_e32 v131, s77, v131\n\t"
    "v_lshlrev_b32_e32 v236, 16, v237\n\t"
    "v_and_b32_e32 v237, s77, v237\n\t"
    "v_pk_fma_f32 v[130:131], v[236:237], v[230:231], v[130:131]\n\t"
    "s_add_u32 m0, s69, 50688\n\t"
    "v_accvgpr_read_b32 v133, a66\n\t"
    "buffer_load_dwordx4 v3, s[44:47], s65 offen lds\n\t"
    "v_accvgpr_read_b32 v237, a82\n\t"
    "v_lshlrev_b32_e32 v132, 16, v133\n\t"
    "v_and_b32_e32 v133, s77, v133\n\t"
    "v_lshlrev_b32_e32 v236, 16, v237\n\t"
    "v_and_b32_e32 v237, s77, v237\n\t"
    "v_pk_fma_f32 v[132:133], v[236:237], v[228:229], v[132:133]\n\t"
    "v_accvgpr_read_b32 v135, a67\n\t"
    "v_accvgpr_read_b32 v237, a83\n\t"
    "v_lshlrev_b32_e32 v134, 16, v135\n\t"
    "v_and_b32_e32 v135, s77, v135\n\t"
    "v_lshlrev_b32_e32 v236, 16, v237\n\t"
    "v_and_b32_e32 v237, s77, v237\n\t"
    "v_pk_fma_f32 v[134:135], v[236:237], v[230:231], v[134:135]\n\t"
    "buffer_load_dwordx2 v[248:249], v250, s[16:19], s95 offen sc1\n\t"
    "v_accvgpr_read_b32 v137, a68\n\t"
    "v_accvgpr_read_b32 v237, a84\n\t"
    "v_lshlrev_b32_e32 v136, 16, v137\n\t"
    "v_and_b32_e32 v137, s77, v137\n\t"
    "v_lshlrev_b32_e32 v236, 16, v237\n\t"
    "v_and_b32_e32 v237, s77, v237\n\t"
    "v_pk_fma_f32 v[136:137], v[236:237], v[228:229], v[136:137]\n\t"
    "v_accvgpr_read_b32 v139, a69\n\t"
    "v_accvgpr_read_b32 v237, a85\n\t"
    "v_lshlrev_b32_e32 v138, 16, v139\n\t"
    "v_and_b32_e32 v139, s77, v139\n\t"
    "v_lshlrev_b32_e32 v236, 16, v237\n\t"
    "v_and_b32_e32 v237, s77, v237\n\t"
    "v_pk_fma_f32 v[138:139], v[236:237], v[230:231], v[138:139]\n\t"
    "s_add_u32 m0, s70, 50688\n\t"
    "v_accvgpr_read_b32 v141, a70\n\t"
    "buffer_load_dwordx4 v4, s[44:47], s65 offen lds\n\t"
    "v_accvgpr_read_b32 v237, a86\n\t"
    "v_lshlrev_b32_e32 v140, 16, v141\n\t"
    "v_and_b32_e32 v141, s77, v141\n\t"
    "v_lshlrev_b32_e32 v236, 16, v237\n\t"
    "v_and_b32_e32 v237, s77, v237\n\t"
    "v_pk_fma_f32 v[140:141], v[236:237], v[228:229], v[140:141]\n\t"
    "v_accvgpr_read_b32 v143, a71\n\t"
    "v_accvgpr_read_b32 v237, a87\n\t"
    "v_lshlrev_b32_e32 v142, 16, v143\n\t"
    "v_and_b32_e32 v143, s77, v143\n\t"
    "v_lshlrev_b32_e32 v236, 16, v237\n\t"
    "v_and_b32_e32 v237, s77, v237\n\t"
    "v_pk_fma_f32 v[142:143], v[236:237], v[230:231], v[142:143]\n\t"
    "s_add_u32 m0, s71, 50688\n\t"
    "v_accvgpr_read_b32 v145, a72\n\t"
    "buffer_load_dwordx4 v5, s[44:47], s65 offen lds\n\t"
    "v_accvgpr_read_b32 v237, a88\n\t"
    "v_lshlrev_b32_e32 v144, 16, v145\n\t"
    "v_and_b32_e32 v145, s77, v145\n\t"
    "v_lshlrev_b32_e32 v236, 16, v237\n\t"
    "v_and_b32_e32 v237, s77, v237\n\t"
    "v_pk_fma_f32 v[144:145], v[236:237], v[232:233], v[144:145]\n\t"
    "v_accvgpr_read_b32 v147, a73\n\t"
    "v_accvgpr_read_b32 v237, a89\n\t"
    "v_lshlrev_b32_e32 v146, 16, v147\n\t"
    "v_and_b32_e32 v147, s77, v147\n\t"
    "v_lshlrev_b32_e32 v236, 16, v237\n\t"
    "v_and_b32_e32 v237, s77, v237\n\t"
    "v_pk_fma_f32 v[146:147], v[236:237], v[234:235], v[146:147]\n\t"
    "s_add_u32 s64, s64, s72\n\t"
    "v_accvgpr_read_b32 v149, a74\n\t"
    "s_add_u32 s65, s65, s72\n\t"
    "v_accvgpr_read_b32 v237, a90\n\t"
    "s_add_u32 m0, s68, 67584\n\t"
    "v_lshlrev_b32_e32 v148, 16, v149\n\t"
    "buffer_load_dwordx4 v2, s[44:47], s64 offen lds\n\t"
    "v_and_b32_e32 v149, s77, v149\n\t"
    "v_lshlrev_b32_e32 v236, 16, v237\n\t"
    "v_and_b32_e32 v237, s77, v237\n\t"
    "v_pk_fma_f32 v[148:149], v[236:237], v[232:233], v[148:149]\n\t"
    "v_accvgpr_read_b32 v151, a75\n\t"
    "v_accvgpr_read_b32 v237, a91\n\t"
    "v_lshlrev_b32_e32 v150, 16, v151\n\t"
    "v_and_b32_e32 v151, s77, v151\n\t"
    "v_lshlrev_b32_e32 v236, 16, v237\n\t"
    "v_and_b32_e32 v237, s77, v237\n\t"
    "v_pk_fma_f32 v[150:151], v[236:237], v[234:235], v[150:151]\n\t"
    "v_accvgpr_read_b32 v153, a76\n\t"
    "v_accvgpr_read_b32 v237, a92\n\t"
    "s_add_u32 m0, s69, 67584\n\t"
    "v_lshlrev_b32_e32 v152, 16, v153\n\t"
    "buffer_load_dwordx4 v3, s[44:47], s64 offen lds\n\t"
    "v_and_b32_e32 v153, s77, v153\n\t"
    "v_lshlrev_b32_e32 v236, 16, v237\n\t"
    "v_and_b32_e32 v237, s77, v237\n\t"
    "v_pk_fma_f32 v[152:153], v[236:237], v[232:233], v[152:153]\n\t"
    "v_accvgpr_read_b32 v155, a77\n\t"
    "v_accvgpr_read_b32 v237, a93\n\t"
    "v_lshlrev_b32_e32 v154, 16, v155\n\t"
    "v_and_b32_e32 v155, s77, v155\n\t"
    "v_lshlrev_b32_e32 v236, 16, v237\n\t"
    "v_and_b32_e32 v237, s77, v237\n\t"
    "v_pk_fma_f32 v[154:155], v[236:237], v[234:235], v[154:155]\n\t"
    "v_accvgpr_read_b32 v157, a78\n\t"
    "v_accvgpr_read_b32 v237, a94\n\t"
    "s_add_u32 m0, s70, 67584\n\t"
    "v_lshlrev_b32_e32 v156, 16, v157\n\t"
    "buffer_load_dwordx4 v4, s[44:47], s64 offen lds\n\t"
    "v_and_b32_e32 v157, s77, v157\n\t"
    "v_lshlrev_b32_e32 v236, 16, v237\n\t"
    "v_and_b32_e32 v237, s77, v237\n\t"
    "v_pk_fma_f32 v[156:157], v[236:237], v[232:233], v[156:157]\n\t"
    "v_accvgpr_read_b32 v159, a79\n\t"
    "v_accvgpr_read_b32 v237, a95\n\t"
    "v_lshlrev_b32_e32 v158, 16, v159\n\t"
    "v_and_b32_e32 v159, s77, v159\n\t"
    "v_lshlrev_b32_e32 v236, 16, v237\n\t"
    "v_and_b32_e32 v237, s77, v237\n\t"
    "v_pk_fma_f32 v[158:159], v[236:237], v[234:235], v[158:159]\n\t"
    "v_mov_b64_e32 v[160:161], 0\n\t"
    "v_mov_b64_e32 v[162:163], 0\n\t"
    "s_add_u32 m0, s71, 67584\n\t"
    "v_mov_b64_e32 v[164:165], 0\n\t"
    "buffer_load_dwordx4 v5, s[44:47], s64 offen lds\n\t"
    "v_mov_b64_e32 v[166:167], 0\n\t"
    "v_mov_b64_e32 v[168:169], 0\n\t"
    "v_mov_b64_e32 v[170:171], 0\n\t"
    "v_mov_b64_e32 v[172:173], 0\n\t"
    "v_mov_b64_e32 v[174:175], 0\n\t"
    "v_mov_b64_e32 v[176:177], 0\n\t"
    "v_mov_b64_e32 v[178:179], 0\n\t"
    "v_mov_b64_e32 v[180:181], 0\n\t"
    "v_mov_b64_e32 v[182:183], 0\n\t"
    "v_mov_b64_e32 v[184:185], 0\n\t"
    "v_mov_b64_e32 v[186:187], 0\n\t"
    "v_mov_b64_e32 v[188:189], 0\n\t"
    "v_mov_b64_e32 v[190:191], 0\n\t"
    "s_add_u32 m0, s68, 84480\n\t"
    "s_nop 0\n\t"
    "buffer_load_dwordx4 v2, s[44:47], s65 offen lds\n\t"
    "s_add_u32 m0, s69, 84480\n\t"
    "s_nop 0\n\t"
    "buffer_load_dwordx4 v3, s[44:47], s65 offen lds\n\t"
    "s_add_u32 m0, s70, 84480\n\t"
    "s_nop 0\n\t"
    "buffer_load_dwordx4 v4, s[44:47], s65 offen lds\n\t"
    "s_add_u32 m0, s71, 84480\n\t"
    "s_nop 0\n\t"
    "buffer_load_dwordx4 v5, s[44:47], s65 offen lds\n\t"
    "s_add_u32 s64, s64, s72\n\t"
    "s_add_u32 s65, s65, s72\n\t"
    "s_waitcnt vmcnt(10)\n\t"
    "s_mov_b32 s77, 0\n\t"
    "L_spG_%=:\n\t"
    "v_cmp_eq_u64_e32 vcc, s[82:83], v[248:249]\n\t"
    "s_cmp_eq_u64 vcc, -1\n\t"
    "s_cbranch_scc1 L_spGok_%=\n\t"
    "buffer_load_dwordx2 v[248:249], v250, s[16:19], s95 offen sc1\n\t"
    "s_add_u32 s77, s77, 1\n\t"
    "s_waitcnt vmcnt(0)\n\t"
    "s_cmp_lt_u32 s77, 0x40000\n\t"
    "s_cbranch_scc1 L_spG_%=\n\t"
    "L_spGok_%=:\n\t"
    "s_lshl_b32 s90, s89, 9\n\t"
    "s_lshl_b32 s94, s88, 14\n\t"
    "s_add_u32 s90, s90, s94\n\t"
    "s_add_u32 s90, s90, 35799040\n\t"
    "s_cmp_ge_u32 s88, 7\n\t"
    "s_cselect_b32 s94, s90, 0x7ff00000\n\t"
    "buffer_load_dwordx4 v[192:195], v30, s[16:19], s94 offen sc1\n\t"
    "buffer_load_dwordx4 v[196:199], v30, s[16:19], s94 offen offset:16 sc1\n\t"
    "s_add_u32 s90, s90, 16384\n\t"
    "s_cmp_ge_u32 s88, 6\n\t"
    "s_cselect_b32 s94, s90, 0x7ff00000\n\t"
    "buffer_load_dwordx4 v[200:203], v30, s[16:19], s94 offen sc1\n\t"
    "buffer_load_dwordx4 v[204:207], v30, s[16:19], s94 offen offset:16 sc1\n\t"
    "s_add_u32 s90, s90, 16384\n\t"
    "s_cmp_ge_u32 s88, 5\n\t"
    "s_cselect_b32 s94, s90, 0x7ff00000\n\t"
    "buffer_load_dwordx4 v[208:211], v30, s[16:19], s94 offen sc1\n\t"
    "buffer_load_dwordx4 v[212:215], v30, s[16:19], s94 offen offset:16 sc1\n\t"
    "s_add_u32 s90, s90, 16384\n\t"
    "s_cmp_ge_u32 s88, 4\n\t"
    "s_cselect_b32 s94, s90, 0x7ff00000\n\t"
    "buffer_load_dwordx4 v[216:219], v30, s[16:19], s94 offen sc1\n\t"
    "buffer_load_dwordx4 v[220:223], v30, s[16:19], s94 offen offset:16 sc1\n\t"
    "s_add_u32 s90, s90, 16384\n\t"
    "s_cmp_ge_u32 s88, 3\n\t"
    "s_cselect_b32 s94, s90, 0x7ff00000\n\t"
    "buffer_load_dwordx4 v[224:227], v30, s[16:19], s94 offen sc1\n\t"
    "buffer_load_dwordx4 v[228:231], v30, s[16:19], s94 offen offset:16 sc1\n\t"
    "s_add_u32 s90, s90, 16384\n\t"
    "s_cmp_ge_u32 s88, 2\n\t"
    "s_cselect_b32 s94, s90, 0x7ff00000\n\t"
    "buffer_load_dwordx4 v[232:235], v30, s[16:19], s94 offen sc1\n\t"
    "buffer_load_dwordx4 v[236:239], v30, s[16:19], s94 offen offset:16 sc1\n\t"
    "s_add_u32 s90, s90, 16384\n\t"
    "s_cmp_ge_u32 s88, 1\n\t"
    "s_cselect_b32 s94, s90, 0x7ff00000\n\t"
    "buffer_load_dwordx4 v[240:243], v30, s[16:19], s94 offen sc1\n\t"
    "buffer_load_dwordx4 v[244:247], v30, s[16:19], s94 offen offset:16 sc1\n\t"
    "s_add_u32 s90, s90, 16384\n\t"
    "v_pk_mul_f32 v[252:253], v[32:33], v[100:101] op_sel:[0,1] op_sel_hi:[1,1]\n\t"
    "v_pk_mul_f32 v[32:33], v[32:33], v[100:101] op_sel:[0,0] op_sel_hi:[1,0]\n\t"
    "v_pk_fma_f32 v[32:33], v[64:65], v[100:101], v[32:33] op_sel:[0,1,0] op_sel_hi:[1,1,1] neg_lo:[0,1,0] neg_hi:[0,1,0]\n\t"
    "v_pk_fma_f32 v[64:65], v[64:65], v[100:101], v[252:253] op_sel:[0,0,0] op_sel_hi:[1,0,1]\n\t"
    "v_pk_mul_f32 v[252:253], v[34:35], v[100:101] op_sel:[0,1] op_sel_hi:[1,1]\n\t"
    "v_pk_mul_f32 v[34:35], v[34:35], v[100:101] op_sel:[0,0] op_sel_hi:[1,0]\n\t"
    "v_pk_fma_f32 v[34:35], v[66:67], v[100:101], v[34:35] op_sel:[0,1,0] op_sel_hi:[1,1,1] neg_lo:[0,1,0] neg_hi:[0,1,0]\n\t"
    "v_pk_fma_f32 v[66:67], v[66:67], v[100:101], v[252:253] op_sel:[0,0,0] op_sel_hi:[1,0,1]\n\t"
    "v_pk_mul_f32 v[252:253], v[36:37], v[100:101] op_sel:[0,1] op_sel_hi:[1,1]\n\t"
    "v_pk_mul_f32 v[36:37], v[36:37], v[100:101] op_sel:[0,0] op_sel_hi:[1,0]\n\t"
    "v_pk_fma_f32 v[36:37], v[68:69], v[100:101], v[36:37] op_sel:[0,1,0] op_sel_hi:[1,1,1] neg_lo:[0,1,0] neg_hi:[0,1,0]\n\t"
    "v_pk_fma_f32 v[68:69], v[68:69], v[100:101], v[252:253] op_sel:[0,0,0] op_sel_hi:[1,0,1]\n\t"
    "v_pk_mul_f32 v[252:253], v[38:39], v[100:101] op_sel:[0,1] op_sel_hi:[1,1]\n\t"
    "v_pk_mul_f32 v[38:39], v[38:39], v[100:101] op_sel:[0,0] op_sel_hi:[1,0]\n\t"
    "v_pk_fma_f32 v[38:39], v[70:71], v[100:101], v[38:39] op_sel:[0,1,0] op_sel_hi:[1,1,1] neg_lo:[0,1,0] neg_hi:[0,1,0]\n\t"
    "buffer_load_dwordx4 a[0:3], v18, s[40:43], s66 offen offset:0 sc1\n\t"
    "v_pk_fma_f32 v[70:71], v[70:71], v[100:101], v[252:253] op_sel:[0,0,0] op_sel_hi:[1,0,1]\n\t"
    "v_pk_mul_f32 v[252:253], v[40:41], v[100:101] op_sel:[0,1] op_sel_hi:[1,1]\n\t"
    "v_pk_mul_f32 v[40:41], v[40:41], v[100:101] op_sel:[0,0] op_sel_hi:[1,0]\n\t"
    "v_pk_fma_f32 v[40:41], v[72:73], v[100:101], v[40:41] op_sel:[0,1,0] op_sel_hi:[1,1,1] neg_lo:[0,1,0] neg_hi:[0,1,0]\n\t"
    "v_pk_fma_f32 v[72:73], v[72:73], v[100:101], v[252:253] op_sel:[0,0,0] op_sel_hi:[1,0,1]\n\t"
    "v_pk_mul_f32 v[252:253], v[42:43], v[100:101] op_sel:[0,1] op_sel_hi:[1,1]\n\t"
    "v_pk_mul_f32 v[42:43], v[42:43], v[100:101] op_sel:[0,0] op_sel_hi:[1,0]\n\t"
    "v_pk_fma_f32 v[42:43], v[74:75], v[100:101], v[42:43] op_sel:[0,1,0] op_sel_hi:[1,1,1] neg_lo:[0,1,0] neg_hi:[0,1,0]\n\t"
    "v_pk_fma_f32 v[74:75], v[74:75], v[100:101], v[252:253] op_sel:[0,0,0] op_sel_hi:[1,0,1]\n\t"
    "v_pk_mul_f32 v[252:253], v[44:45], v[100:101] op_sel:[0,1] op_sel_hi:[1,1]\n\t"
    "v_pk_mul_f32 v[44:45], v[44:45], v[100:101] op_sel:[0,0] op_sel_hi:[1,0]\n\t"
    "v_pk_fma_f32 v[44:45], v[76:77], v[100:101], v[44:45] op_sel:[0,1,0] op_sel_hi:[1,1,1] neg_lo:[0,1,0] neg_hi:[0,1,0]\n\t"
    "v_pk_fma_f32 v[76:77], v[76:77], v[100:101], v[252:253] op_sel:[0,0,0] op_sel_hi:[1,0,1]\n\t"
    "v_pk_mul_f32 v[252:253], v[46:47], v[100:101] op_sel:[0,1] op_sel_hi:[1,1]\n\t"
    "v_pk_mul_f32 v[46:47], v[46:47], v[100:101] op_sel:[0,0] op_sel_hi:[1,0]\n\t"
    "buffer_load_dwordx4 a[4:7], v6, s[40:43], s66 offen offset:0 sc1\n\t"
    "v_pk_fma_f32 v[46:47], v[78:79], v[100:101], v[46:47] op_sel:[0,1,0] op_sel_hi:[1,1,1] neg_lo:[0,1,0] neg_hi:[0,1,0]\n\t"
    "v_pk_fma_f32 v[78:79], v[78:79], v[100:101], v[252:253] op_sel:[0,0,0] op_sel_hi:[1,0,1]\n\t"
    "v_pk_mul_f32 v[252:253], v[48:49], v[102:103] op_sel:[0,1] op_sel_hi:[1,1]\n\t"
    "v_pk_mul_f32 v[48:49], v[48:49], v[102:103] op_sel:[0,0] op_sel_hi:[1,0]\n\t"
    "v_pk_fma_f32 v[48:49], v[80:81], v[102:103], v[48:49] op_sel:[0,1,0] op_sel_hi:[1,1,1] neg_lo:[0,1,0] neg_hi:[0,1,0]\n\t"
    "v_pk_fma_f32 v[80:81], v[80:81], v[102:103], v[252:253] op_sel:[0,0,0] op_sel_hi:[1,0,1]\n\t"
    "v_pk_mul_f32 v[252:253], v[50:51], v[102:103] op_sel:[0,1] op_sel_hi:[1,1]\n\t"
    "v_pk_mul_f32 v[50:51], v[50:51], v[102:103] op_sel:[0,0] op_sel_hi:[1,0]\n\t"
    "v_pk_fma_f32 v[50:51], v[82:83], v[102:103], v[50:51] op_sel:[0,1,0] op_sel_hi:[1,1,1] neg_lo:[0,1,0] neg_hi:[0,1,0]\n\t"
    "v_pk_fma_f32 v[82:83], v[82:83], v[102:103], v[252:253] op_sel:[0,0,0] op_sel_hi:[1,0,1]\n\t"
    "v_pk_mul_f32 v[252:253], v[52:53], v[102:103] op_sel:[0,1] op_sel_hi:[1,1]\n\t"
    "v_pk_mul_f32 v[52:53], v[52:53], v[102:103] op_sel:[0,0] op_sel_hi:[1,0]\n\t"
    "v_pk_fma_f32 v[52:53], v[84:85], v[102:103], v[52:53] op_sel:[0,1,0] op_sel_hi:[1,1,1] neg_lo:[0,1,0] neg_hi:[0,1,0]\n\t"
    "v_pk_fma_f32 v[84:85], v[84:85], v[102:103], v[252:253] op_sel:[0,0,0] op_sel_hi:[1,0,1]\n\t"
    "v_pk_mul_f32 v[252:253], v[54:55], v[102:103] op_sel:[0,1] op_sel_hi:[1,1]\n\t"
    "buffer_load_dwordx4 a[8:11], v18, s[40:43], s66 offen offset:1024 sc1\n\t"
    "v_pk_mul_f32 v[54:55], v[54:55], v[102:103] op_sel:[0,0] op_sel_hi:[1,0]\n\t"
    "v_pk_fma_f32 v[54:55], v[86:87], v[102:103], v[54:55] op_sel:[0,1,0] op_sel_hi:[1,1,1] neg_lo:[0,1,0] neg_hi:[0,1,0]\n\t"
    "v_pk_fma_f32 v[86:87], v[86:87], v[102:103], v[252:253] op_sel:[0,0,0] op_sel_hi:[1,0,1]\n\t"
    "v_pk_mul_f32 v[252:253], v[56:57], v[102:103] op_sel:[0,1] op_sel_hi:[1,1]\n\t"
    "v_pk_mul_f32 v[56:57], v[56:57], v[102:103] op_sel:[0,0] op_sel_hi:[1,0]\n\t"
    "v_pk_fma_f32 v[56:57], v[88:89], v[102:103], v[56:57] op_sel:[0,1,0] op_sel_hi:[1,1,1] neg_lo:[0,1,0] neg_hi:[0,1,0]\n\t"
    "v_pk_fma_f32 v[88:89], v[88:89], v[102:103], v[252:253] op_sel:[0,0,0] op_sel_hi:[1,0,1]\n\t"
    "v_pk_mul_f32 v[252:253], v[58:59], v[102:103] op_sel:[0,1] op_sel_hi:[1,1]\n\t"
    "v_pk_mul_f32 v[58:59], v[58:59], v[102:103] op_sel:[0,0] op_sel_hi:[1,0]\n\t"
    "v_pk_fma_f32 v[58:59], v[90:91], v[102:103], v[58:59] op_sel:[0,1,0] op_sel_hi:[1,1,1] neg_lo:[0,1,0] neg_hi:[0,1,0]\n\t"
    "v_pk_fma_f32 v[90:91], v[90:91], v[102:103], v[252:253] op_sel:[0,0,0] op_sel_hi:[1,0,1]\n\t"
    "v_pk_mul_f32 v[252:253], v[60:61], v[102:103] op_sel:[0,1] op_sel_hi:[1,1]\n\t"
    "v_pk_mul_f32 v[60:61], v[60:61], v[102:103] op_sel:[0,0] op_sel_hi:[1,0]\n\t"
    "v_pk_fma_f32 v[60:61], v[92:93], v[102:103], v[60:61] op_sel:[0,1,0] op_sel_hi:[1,1,1] neg_lo:[0,1,0] neg_hi:[0,1,0]\n\t"
    "v_pk_fma_f32 v[92:93], v[92:93], v[102:103], v[252:253] op_sel:[0,0,0] op_sel_hi:[1,0,1]\n\t"
    "buffer_load_dwordx4 a[12:15], v6, s[40:43], s66 offen offset:1024 sc1\n\t"
    "v_pk_mul_f32 v[252:253], v[62:63], v[102:103] op_sel:[0,1] op_sel_hi:[1,1]\n\t"
    "v_pk_mul_f32 v[62:63], v[62:63], v[102:103] op_sel:[0,0] op_sel_hi:[1,0]\n\t"
    "v_pk_fma_f32 v[62:63], v[94:95], v[102:103], v[62:63] op_sel:[0,1,0] op_sel_hi:[1,1,1] neg_lo:[0,1,0] neg_hi:[0,1,0]\n\t"
    "v_pk_fma_f32 v[94:95], v[94:95], v[102:103], v[252:253] op_sel:[0,0,0] op_sel_hi:[1,0,1]\n\t"
    "v_pk_mul_f32 v[120:121], v[124:125], v[124:125]\n\t"
    "v_pk_mul_f32 v[122:123], v[126:127], v[124:125]\n\t"
    "v_pk_fma_f32 v[120:121], v[126:127], v[126:127], v[120:121] neg_lo:[0,1,0] neg_hi:[0,1,0]\n\t"
    "v_pk_fma_f32 v[122:123], v[124:125], v[126:127], v[122:123]\n\t"
    "v_pk_mul_f32 v[124:125], v[120:121], v[120:121]\n\t"
    "v_pk_mul_f32 v[126:127], v[122:123], v[120:121]\n\t"
    "v_pk_fma_f32 v[124:125], v[122:123], v[122:123], v[124:125] neg_lo:[0,1,0] neg_hi:[0,1,0]\n\t"
    "v_pk_fma_f32 v[126:127], v[120:121], v[122:123], v[126:127]\n\t"
    "v_pk_mul_f32 v[120:121], v[124:125], v[124:125]\n\t"
    "v_pk_mul_f32 v[122:123], v[126:127], v[124:125]\n\t"
    "v_pk_fma_f32 v[120:121], v[126:127], v[126:127], v[120:121] neg_lo:[0,1,0] neg_hi:[0,1,0]\n\t"
    "buffer_load_dwordx4 a[16:19], v18, s[40:43], s66 offen offset:2048 sc1\n\t"
    "v_pk_fma_f32 v[122:123], v[124:125], v[126:127], v[122:123]\n\t"
    "v_pk_mul_f32 v[124:125], v[120:121], v[120:121]\n\t"
    "v_pk_mul_f32 v[126:127], v[122:123], v[120:121]\n\t"
    "v_pk_fma_f32 v[124:125], v[122:123], v[122:123], v[124:125] neg_lo:[0,1,0] neg_hi:[0,1,0]\n\t"
    "v_pk_fma_f32 v[126:127], v[120:121], v[122:123], v[126:127]\n\t"
    "buffer_load_dwordx4 a[20:23], v6, s[40:43], s66 offen offset:2048 sc1\n\t"
    "buffer_load_dwordx4 a[24:27], v18, s[40:43], s66 offen offset:3072 sc1\n\t"
    "buffer_load_dwordx4 a[28:31], v6, s[40:43], s66 offen offset:3072 sc1\n\t"
    "s_add_u32 s66, s66, 524288\n\t"
    "buffer_load_dwordx4 a[32:35], v18, s[40:43], s66 offen offset:0 sc1\n\t"
    "buffer_load_dwordx4 a[36:39], v6, s[40:43], s66 offen offset:0 sc1\n\t"
    "buffer_load_dwordx4 a[40:43], v18, s[40:43], s66 offen offset:1024 sc1\n\t"
    "buffer_load_dwordx4 a[44:47], v6, s[40:43], s66 offen offset:1024 sc1\n\t"
    "buffer_load_dwordx4 a[48:51], v18, s[40:43], s66 offen offset:2048 sc1\n\t"
    "buffer_load_dwordx4 a[52:55], v6, s[40:43], s66 offen offset:2048 sc1\n\t"
    "buffer_load_dwordx4 a[56:59], v18, s[40:43], s66 offen offset:3072 sc1\n\t"
    "buffer_load_dwordx4 a[60:63], v6, s[40:43], s66 offen offset:3072 sc1\n\t"
    "s_add_u32 s66, s66, 524288\n\t"
    "buffer_load_dwordx4 a[64:67], v18, s[40:43], s66 offen offset:0 sc1\n\t"
    "s_waitcnt vmcnt(17)\n\t"
    "s_mov_b32 s77, 0\n\t"
    "L_spC_%=:\n\t"
    "v_mov_b32_e32 v248, 0\n\t"
    "s_cmp_ge_u32 s88, 7\n\t"
    "s_cselect_b64 s[36:37], s[82:83], 0\n\t"
    "v_bitop3_b32 v248, v193, s36, v248 bitop3:0xbe\n\t"
    "v_bitop3_b32 v248, v195, s37, v248 bitop3:0xbe\n\t"
    "v_bitop3_b32 v248, v197, s36, v248 bitop3:0xbe\n\t"
    "v_bitop3_b32 v248, v199, s37, v248 bitop3:0xbe\n\t"
    "s_cmp_ge_u32 s88, 6\n\t"
    "s_cselect_b64 s[36:37], s[82:83], 0\n\t"
    "v_bitop3_b32 v248, v201, s36, v248 bitop3:0xbe\n\t"
    "v_bitop3_b32 v248, v203, s37, v248 bitop3:0xbe\n\t"
    "v_bitop3_b32 v248, v205, s36, v248 bitop3:0xbe\n\t"
    "v_bitop3_b32 v248, v207, s37, v248 bitop3:0xbe\n\t"
    "s_cmp_ge_u32 s88, 5\n\t"
    "s_cselect_b64 s[36:37], s[82:83], 0\n\t"
    "v_bitop3_b32 v248, v209, s36, v248 bitop3:0xbe\n\t"
    "v_bitop3_b32 v248, v211, s37, v248 bitop3:0xbe\n\t"
    "v_bitop3_b32 v248, v213, s36, v248 bitop3:0xbe\n\t"
    "v_bitop3_b32 v248, v215, s37, v248 bitop3:0xbe\n\t"
    "s_cmp_ge_u32 s88, 4\n\t"
    "s_cselect_b64 s[36:37], s[82:83], 0\n\t"
    "v_bitop3_b32 v248, v217, s36, v248 bitop3:0xbe\n\t"
    "v_bitop3_b32 v248, v219, s37, v248 bitop3:0xbe\n\t"
    "v_bitop3_b32 v248, v221, s36, v248 bitop3:0xbe\n\t"
    "v_bitop3_b32 v248, v223, s37, v248 bitop3:0xbe\n\t"
    "s_cmp_ge_u32 s88, 3\n\t"
    "s_cselect_b64 s[36:37], s[82:83], 0\n\t"
    "v_bitop3_b32 v248, v225, s36, v248 bitop3:0xbe\n\t"
    "v_bitop3_b32 v248, v227, s37, v248 bitop3:0xbe\n\t"
    "v_bitop3_b32 v248, v229, s36, v248 bitop3:0xbe\n\t"
    "v_bitop3_b32 v248, v231, s37, v248 bitop3:0xbe\n\t"
    "s_cmp_ge_u32 s88, 2\n\t"
    "s_cselect_b64 s[36:37], s[82:83], 0\n\t"
    "v_bitop3_b32 v248, v233, s36, v248 bitop3:0xbe\n\t"
    "v_bitop3_b32 v248, v235, s37, v248 bitop3:0xbe\n\t"
    "v_bitop3_b32 v248, v237, s36, v248 bitop3:0xbe\n\t"
    "v_bitop3_b32 v248, v239, s37, v248 bitop3:0xbe\n\t"
    "s_cmp_ge_u32 s88, 1\n\t"
    "s_cselect_b64 s[36:37], s[82:83], 0\n\t"
    "v_bitop3_b32 v248, v241, s36, v248 bitop3:0xbe\n\t"
    "v_bitop3_b32 v248, v243, s37, v248 bitop3:0xbe\n\t"
    "v_bitop3_b32 v248, v245, s36, v248 bitop3:0xbe\n\t"
    "v_bitop3_b32 v248, v247, s37, v248 bitop3:0xbe\n\t"
    "v_cmp_ne_u32_e32 vcc, 0, v248\n\t"
    "s_cbranch_vccz L_spCok_%=\n\t"
    "s_lshl_b32 s90, s89, 9\n\t"
    "s_lshl_b32 s94, s88, 14\n\t"
    "s_add_u32 s90, s90, s94\n\t"
    "s_add_u32 s90, s90, 35799040\n\t"
    "s_cmp_ge_u32 s88, 7\n\t"
    "s_cselect_b32 s94, s90, 0x7ff00000\n\t"
    "buffer_load_dwordx4 v[192:195], v30, s[16:19], s94 offen sc1\n\t"
    "buffer_load_dwordx4 v[196:199], v30, s[16:19], s94 offen offset:16 sc1\n\t"
    "s_add_u32 s90, s90, 16384\n\t"
    "s_cmp_ge_u32 s88, 6\n\t"
    "s_cselect_b32 s94, s90, 0x7ff00000\n\t"
    "buffer_load_dwordx4 v[200:203], v30, s[16:19], s94 offen sc1\n\t"
    "buffer_load_dwordx4 v[204:207], v30, s[16:19], s94 offen offset:16 sc1\n\t"
    "s_add_u32 s90, s90, 16384\n\t"
    "s_cmp_ge_u32 s88, 5\n\t"
    "s_cselect_b32 s94, s90, 0x7ff00000\n\t"
    "buffer_load_dwordx4 v[208:211], v30, s[16:19], s94 offen sc1\n\t"
    "buffer_load_dwordx4 v[212:215], v30, s[16:19], s94 offen offset:16 sc1\n\t"
    "s_add_u32 s90, s90, 16384\n\t"
    "s_cmp_ge_u32 s88, 4\n\t"
    "s_cselect_b32 s94, s90, 0x7ff00000\n\t"
    "buffer_load_dwordx4 v[216:219], v30, s[16:19], s94 offen sc1\n\t"
    "buffer_load_dwordx4 v[220:223], v30, s[16:19], s94 offen offset:16 sc1\n\t"
    "s_add_u32 s90, s90, 16384\n\t"
    "s_cmp_ge_u32 s88, 3\n\t"
    "s_cselect_b32 s94, s90, 0x7ff00000\n\t"
    "buffer_load_dwordx4 v[224:227], v30, s[16:19], s94 offen sc1\n\t"
    "buffer_load_dwordx4 v[228:231], v30, s[16:19], s94 offen offset:16 sc1\n\t"
    "s_add_u32 s90, s90, 16384\n\t"
    "s_cmp_ge_u32 s88, 2\n\t"
    "s_cselect_b32 s94, s90, 0x7ff00000\n\t"
    "buffer_load_dwordx4 v[232:235], v30, s[16:19], s94 offen sc1\n\t"
    "buffer_load_dwordx4 v[236:239], v30, s[16:19], s94 offen offset:16 sc1\n\t"
    "s_add_u32 s90, s90, 16384\n\t"
    "s_cmp_ge_u32 s88, 1\n\t"
    "s_cselect_b32 s94, s90, 0x7ff00000\n\t"
    "buffer_load_dwordx4 v[240:243], v30, s[16:19], s94 offen sc1\n\t"
    "buffer_load_dwordx4 v[244:247], v30, s[16:19], s94 offen offset:16 sc1\n\t"
    "s_add_u32 s90, s90, 16384\n\t"
    "s_add_u32 s77, s77, 1\n\t"
    "s_waitcnt vmcnt(0)\n\t"
    "s_cmp_lt_u32 s77, 0x40000\n\t"
    "s_cbranch_scc1 L_spC_%=\n\t"
    "L_spCok_%=:\n\t"
    "s_cmp_lg_u32 s91, 14336\n\t"
    "s_cbranch_scc1 L_noep_%=\n\t"
    "v_mov_b32_e32 v252, s82\n\t"
    "s_mov_b32 s90, 35651584\n\t"
    "buffer_store_dword v252, v0, s[16:19], s90 offen sc1\n\t"
    "L_noep_%=:\n\t"
    "v_fma_f32 v200, v192, v124, v200\n\t"
    "v_fma_f32 v204, v196, v125, v204\n\t"
    "v_fma_f32 v202, v194, v124, v202\n\t"
    "v_fma_f32 v206, v198, v125, v206\n\t"
    "v_fma_f32 v200, -v194, v126, v200\n\t"
    "v_fma_f32 v204, -v198, v127, v204\n\t"
    "v_fmac_f32_e32 v202, v192, v126\n\t"
    "v_fmac_f32_e32 v206, v196, v127\n\t"
    "v_fma_f32 v208, v200, v124, v208\n\t"
    "v_fma_f32 v212, v204, v125, v212\n\t"
    "v_fma_f32 v210, v202, v124, v210\n\t"
    "v_fma_f32 v214, v206, v125, v214\n\t"
    "v_fma_f32 v208, -v202, v126, v208\n\t"
    "buffer_load_dwordx4 a[68:71], v6, s[40:43], s66 offen offset:0 sc1\n\t"
    "v_fma_f32 v212, -v206, v127, v212\n\t"
    "v_fmac_f32_e32 v210, v200, v126\n\t"
    "v_fmac_f32_e32 v214, v204, v127\n\t"
    "v_fma_f32 v216, v208, v124, v216\n\t"
    "v_fma_f32 v220, v212, v125, v220\n\t"
    "v_fma_f32 v218, v210, v124, v218\n\t"
    "v_fma_f32 v222, v214, v125, v222\n\t"
    "v_fma_f32 v216, -v210, v126, v216\n\t"
    "v_fma_f32 v220, -v214, v127, v220\n\t"
    "v_fmac_f32_e32 v218, v208, v126\n\t"
    "v_fmac_f32_e32 v222, v212, v127\n\t"
    "v_fma_f32 v224, v216, v124, v224\n\t"
    "buffer_load_dwordx4 a[72:75], v18, s[40:43], s66 offen offset:1024 sc1\n\t"
    "v_fma_f32 v228, v220, v125, v228\n\t"
    "v_fma_f32 v226, v218, v124, v226\n\t"
    "v_fma_f32 v230, v222, v125, v230\n\t"
    "v_fma_f32 v224, -v218, v126, v224\n\t"
    "v_fma_f32 v228, -v222, v127, v228\n\t"
    "v_fmac_f32_e32 v226, v216, v126\n\t"
    "v_fmac_f32_e32 v230, v220, v127\n\t"
    "v_fma_f32 v232, v224, v124, v232\n\t"
    "v_fma_f32 v236, v228, v125, v236\n\t"
    "v_fma_f32 v234, v226, v124, v234\n\t"
    "v_fma_f32 v238, v230, v125, v238\n\t"
    "buffer_load_dwordx4 a[76:79], v6, s[40:43], s66 offen offset:1024 sc1\n\t"
    "v_fma_f32 v232, -v226, v126, v232\n\t"
    "v_fma_f32 v236, -v230, v127, v236\n\t"
    "v_fmac_f32_e32 v234, v224, v126\n\t"
    "v_fmac_f32_e32 v238, v228, v127\n\t"
    "v_fma_f32 v240, v232, v124, v240\n\t"
    "v_fma_f32 v244, v236, v125, v244\n\t"
    "v_fma_f32 v242, v234, v124, v242\n\t"
    "v_fma_f32 v246, v238, v125, v246\n\t"
    "v_fma_f32 v240, -v234, v126, v240\n\t"
    "v_fma_f32 v244, -v238, v127, v244\n\t"
    "v_fmac_f32_e32 v242, v232, v126\n\t"
    "buffer_load_dwordx4 a[80:83], v18, s[40:43], s66 offen offset:2048 sc1\n\t"
    "v_fmac_f32_e32 v246, v236, v127\n\t"
    "v_fma_f32 v12, v240, v108, v12\n\t"
    "v_fma_f32 v13, v244, v109, v13\n\t"
    "v_fma_f32 v14, v242, v108, v14\n\t"
    "v_fma_f32 v15, v246, v109, v15\n\t"
    "v_fma_f32 v12, -v242, v110, v12\n\t"
    "v_fma_f32 v13, -v246, v111, v13\n\t"
    "v_fmac_f32_e32 v14, v240, v110\n\t"
    "v_fmac_f32_e32 v15, v244, v111\n\t"
    "v_fma_f32 v113, v12, v104, v112\n\t"
    "v_fma_f32 v115, v14, v104, v114\n\t"
    "buffer_load_dwordx4 a[84:87], v6, s[40:43], s66 offen offset:2048 sc1\n\t"
    "v_fma_f32 v113, -v14, v106, v113\n\t"
    "v_fmac_f32_e32 v115, v12, v106\n\t"
    "v_fma_f32 v117, v13, v105, v116\n\t"
    "v_fma_f32 v119, v15, v105, v118\n\t"
    "v_fma_f32 v117, -v15, v107, v117\n\t"
    "v_fmac_f32_e32 v119, v13, v107\n\t"
    "v_mov_b32_e32 v112, v12\n\t"
    "v_mov_b32_e32 v114, v14\n\t"
    "v_mov_b32_e32 v116, v13\n\t"
    "v_mov_b32_e32 v118, v15\n\t"
    "v_pk_mul_f32 v[120:121], v[112:113], v[100:101] op_sel:[0,0] op_sel_hi:[1,0]\n\t"
    "v_pk_mul_f32 v[122:123], v[114:115], v[100:101] op_sel:[0,0] op_sel_hi:[1,0]\n\t"
    "buffer_load_dwordx4 a[88:91], v18, s[40:43], s66 offen offset:3072 sc1\n\t"
    "v_pk_fma_f32 v[120:121], v[114:115], v[100:101], v[120:121] op_sel:[0,1,0] op_sel_hi:[1,1,1] neg_lo:[0,1,0] neg_hi:[0,1,0]\n\t"
    "v_pk_fma_f32 v[122:123], v[112:113], v[100:101], v[122:123] op_sel:[0,1,0] op_sel_hi:[1,1,1]\n\t"
    "v_pk_mul_f32 v[124:125], v[116:117], v[102:103] op_sel:[0,0] op_sel_hi:[1,0]\n\t"
    "v_pk_mul_f32 v[126:127], v[118:119], v[102:103] op_sel:[0,0] op_sel_hi:[1,0]\n\t"
    "v_pk_fma_f32 v[124:125], v[118:119], v[102:103], v[124:125] op_sel:[0,1,0] op_sel_hi:[1,1,1] neg_lo:[0,1,0] neg_hi:[0,1,0]\n\t"
    "v_pk_fma_f32 v[126:127], v[116:117], v[102:103], v[126:127] op_sel:[0,1,0] op_sel_hi:[1,1,1]\n\t"
    "v_pk_fma_f32 v[32:33], v[120:121], v[96:97], v[32:33] op_sel:[0,0,0] op_sel_hi:[1,0,1]\n\t"
    "v_pk_fma_f32 v[48:49], v[124:125], v[98:99], v[48:49] op_sel:[0,0,0] op_sel_hi:[1,0,1]\n\t"
    "v_pk_fma_f32 v[64:65], v[122:123], v[96:97], v[64:65] op_sel:[0,0,0] op_sel_hi:[1,0,1]\n\t"
    "v_pk_fma_f32 v[80:81], v[126:127], v[98:99], v[80:81] op_sel:[0,0,0] op_sel_hi:[1,0,1]\n\t"
    "v_pk_fma_f32 v[32:33], v[122:123], v[96:97], v[32:33] op_sel:[0,1,0] op_sel_hi:[1,1,1] neg_lo:[0,1,0] neg_hi:[0,1,0]\n\t"
    "buffer_load_dwordx4 a[92:95], v6, s[40:43], s66 offen offset:3072 sc1\n\t"
    "v_pk_fma_f32 v[48:49], v[126:127], v[98:99], v[48:49] op_sel:[0,1,0] op_sel_hi:[1,1,1] neg_lo:[0,1,0] neg_hi:[0,1,0]\n\t"
    "v_pk_fma_f32 v[64:65], v[120:121], v[96:97], v[64:65] op_sel:[0,1,0] op_sel_hi:[1,1,1]\n\t"
    "v_pk_fma_f32 v[80:81], v[124:125], v[98:99], v[80:81] op_sel:[0,1,0] op_sel_hi:[1,1,1]\n\t"
    "v_pk_fma_f32 v[34:35], v[32:33], v[96:97], v[34:35] op_sel:[0,0,0] op_sel_hi:[1,0,1]\n\t"
    "v_pk_fma_f32 v[50:51], v[48:49], v[98:99], v[50:51] op_sel:[0,0,0] op_sel_hi:[1,0,1]\n\t"
    "v_pk_fma_f32 v[66:67], v[64:65], v[96:97], v[66:67] op_sel:[0,0,0] op_sel_hi:[1,0,1]\n\t"
    "v_pk_fma_f32 v[82:83], v[80:81], v[98:99], v[82:83] op_sel:[0,0,0] op_sel_hi:[1,0,1]\n\t"
    "v_pk_fma_f32 v[34:35], v[64:65], v[96:97], v[34:35] op_sel:[0,1,0] op_sel_hi:[1,1,1] neg_lo:[0,1,0] neg_hi:[0,1,0]\n\t"
    "v_pk_fma_f32 v[50:51], v[80:81], v[98:99], v[50:51] op_sel:[0,1,0] op_sel_hi:[1,1,1] neg_lo:[0,1,0] neg_hi:[0,1,0]\n\t"
    "v_pk_fma_f32 v[66:67], v[32:33], v[96:97], v[66:67] op_sel:[0,1,0] op_sel_hi:[1,1,1]\n\t"
    "v_pk_fma_f32 v[82:83], v[48:49], v[98:99], v[82:83] op_sel:[0,1,0] op_sel_hi:[1,1,1]\n\t"
    "s_add_u32 s66, s66, 524288\n\t"
    "v_pk_fma_f32 v[36:37], v[34:35], v[96:97], v[36:37] op_sel:[0,0,0] op_sel_hi:[1,0,1]\n\t"
    "s_add_u32 m0, s68, 101376\n\t"
    "v_pk_fma_f32 v[52:53], v[50:51], v[98:99], v[52:53] op_sel:[0,0,0] op_sel_hi:[1,0,1]\n\t"
    "buffer_load_dwordx4 v2, s[44:47], s64 offen lds\n\t"
    "v_pk_fma_f32 v[68:69], v[66:67], v[96:97], v[68:69] op_sel:[0,0,0] op_sel_hi:[1,0,1]\n\t"
    "v_pk_fma_f32 v[84:85], v[82:83], v[98:99], v[84:85] op_sel:[0,0,0] op_sel_hi:[1,0,1]\n\t"
    "v_pk_fma_f32 v[36:37], v[66:67], v[96:97], v[36:37] op_sel:[0,1,0] op_sel_hi:[1,1,1] neg_lo:[0,1,0] neg_hi:[0,1,0]\n\t"
    "v_pk_fma_f32 v[52:53], v[82:83], v[98:99], v[52:53] op_sel:[0,1,0] op_sel_hi:[1,1,1] neg_lo:[0,1,0] neg_hi:[0,1,0]\n\t"
    "v_pk_fma_f32 v[68:69], v[34:35], v[96:97], v[68:69] op_sel:[0,1,0] op_sel_hi:[1,1,1]\n\t"
    "v_pk_fma_f32 v[84:85], v[50:51], v[98:99], v[84:85] op_sel:[0,1,0] op_sel_hi:[1,1,1]\n\t"
    "v_pk_fma_f32 v[38:39], v[36:37], v[96:97], v[38:39] op_sel:[0,0,0] op_sel_hi:[1,0,1]\n\t"
    "v_pk_fma_f32 v[54:55], v[52:53], v[98:99], v[54:55] op_sel:[0,0,0] op_sel_hi:[1,0,1]\n\t"
    "v_pk_fma_f32 v[70:71], v[68:69], v[96:97], v[70:71] op_sel:[0,0,0] op_sel_hi:[1,0,1]\n\t"
    "v_pk_fma_f32 v[86:87], v[84:85], v[98:99], v[86:87] op_sel:[0,0,0] op_sel_hi:[1,0,1]\n\t"
    "buffer_load_dwordx4 a[96:99], v18, s[40:43], s66 offen offset:0 sc1\n\t"
    "v_pk_fma_f32 v[38:39], v[68:69], v[96:97], v[38:39] op_sel:[0,1,0] op_sel_hi:[1,1,1] neg_lo:[0,1,0] neg_hi:[0,1,0]\n\t"
    "v_pk_fma_f32 v[54:55], v[84:85], v[98:99], v[54:55] op_sel:[0,1,0] op_sel_hi:[1,1,1] neg_lo:[0,1,0] neg_hi:[0,1,0]\n\t"
    "v_pk_fma_f32 v[70:71], v[36:37], v[96:97], v[70:71] op_sel:[0,1,0] op_sel_hi:[1,1,1]\n\t"
    "v_pk_fma_f32 v[86:87], v[52:53], v[98:99], v[86:87] op_sel:[0,1,0] op_sel_hi:[1,1,1]\n\t"
    "v_pk_fma_f32 v[40:41], v[38:39], v[96:97], v[40:41] op_sel:[0,0,0] op_sel_hi:[1,0,1]\n\t"
    "v_pk_fma_f32 v[56:57], v[54:55], v[98:99], v[56:57] op_sel:[0,0,0] op_sel_hi:[1,0,1]\n\t"
    "v_pk_fma_f32 v[72:73], v[70:71], v[96:97], v[72:73] op_sel:[0,0,0] op_sel_hi:[1,0,1]\n\t"
    "v_pk_fma_f32 v[88:89], v[86:87], v[98:99], v[88:89] op_sel:[0,0,0] op_sel_hi:[1,0,1]\n\t"
    "v_pk_fma_f32 v[40:41], v[70:71], v[96:97], v[40:41] op_sel:[0,1,0] op_sel_hi:[1,1,1] neg_lo:[0,1,0] neg_hi:[0,1,0]\n\t"
    "v_pk_fma_f32 v[56:57], v[86:87], v[98:99], v[56:57] op_sel:[0,1,0] op_sel_hi:[1,1,1] neg_lo:[0,1,0] neg_hi:[0,1,0]\n\t"
    "v_pk_fma_f32 v[72:73], v[38:39], v[96:97], v[72:73] op_sel:[0,1,0] op_sel_hi:[1,1,1]\n\t"
    "s_add_u32 m0, s69, 101376\n\t"
    "v_pk_fma_f32 v[88:89], v[54:55], v[98:99], v[88:89] op_sel:[0,1,0] op_sel_hi:[1,1,1]\n\t"
    "buffer_load_dwordx4 v3, s[44:47], s64 offen lds\n\t"
    "v_pk_fma_f32 v[42:43], v[40:41], v[96:97], v[42:43] op_sel:[0,0,0] op_sel_hi:[1,0,1]\n\t"
    "v_pk_fma_f32 v[58:59], v[56:57], v[98:99], v[58:59] op_sel:[0,0,0] op_sel_hi:[1,0,1]\n\t"
    "v_pk_fma_f32 v[74:75], v[72:73], v[96:97], v[74:75] op_sel:[0,0,0] op_sel_hi:[1,0,1]\n\t"
    "v_pk_fma_f32 v[90:91], v[88:89], v[98:99], v[90:91] op_sel:[0,0,0] op_sel_hi:[1,0,1]\n\t"
    "v_pk_fma_f32 v[42:43], v[72:73], v[96:97], v[42:43] op_sel:[0,1,0] op_sel_hi:[1,1,1] neg_lo:[0,1,0] neg_hi:[0,1,0]\n\t"
    "v_pk_fma_f32 v[58:59], v[88:89], v[98:99], v[58:59] op_sel:[0,1,0] op_sel_hi:[1,1,1] neg_lo:[0,1,0] neg_hi:[0,1,0]\n\t"
    "v_pk_fma_f32 v[74:75], v[40:41], v[96:97], v[74:75] op_sel:[0,1,0] op_sel_hi:[1,1,1]\n\t"
    "v_pk_fma_f32 v[90:91], v[56:57], v[98:99], v[90:91] op_sel:[0,1,0] op_sel_hi:[1,1,1]\n\t"
    "v_pk_fma_f32 v[44:45], v[42:43], v[96:97], v[44:45] op_sel:[0,0,0] op_sel_hi:[1,0,1]\n\t"
    "v_pk_fma_f32 v[60:61], v[58:59], v[98:99], v[60:61] op_sel:[0,0,0] op_sel_hi:[1,0,1]\n\t"
    "buffer_load_dwordx4 a[100:103], v6, s[40:43], s66 offen offset:0 sc1\n\t"
    "v_pk_fma_f32 v[76:77], v[74:75], v[96:97], v[76:77] op_sel:[0,0,0] op_sel_hi:[1,0,1]\n\t"
    "v_pk_fma_f32 v[92:93], v[90:91], v[98:99], v[92:93] op_sel:[0,0,0] op_sel_hi:[1,0,1]\n\t"
    "v_pk_fma_f32 v[44:45], v[74:75], v[96:97], v[44:45] op_sel:[0,1,0] op_sel_hi:[1,1,1] neg_lo:[0,1,0] neg_hi:[0,1,0]\n\t"
    "v_pk_fma_f32 v[60:61], v[90:91], v[98:99], v[60:61] op_sel:[0,1,0] op_sel_hi:[1,1,1] neg_lo:[0,1,0] neg_hi:[0,1,0]\n\t"
    "v_pk_fma_f32 v[76:77], v[42:43], v[96:97], v[76:77] op_sel:[0,1,0] op_sel_hi:[1,1,1]\n\t"
    "v_pk_fma_f32 v[92:93], v[58:59], v[98:99], v[92:93] op_sel:[0,1,0] op_sel_hi:[1,1,1]\n\t"
    "v_pk_fma_f32 v[46:47], v[44:45], v[96:97], v[46:47] op_sel:[0,0,0] op_sel_hi:[1,0,1]\n\t"
    "v_pk_fma_f32 v[62:63], v[60:61], v[98:99], v[62:63] op_sel:[0,0,0] op_sel_hi:[1,0,1]\n\t"
    "v_pk_fma_f32 v[78:79], v[76:77], v[96:97], v[78:79] op_sel:[0,0,0] op_sel_hi:[1,0,1]\n\t"
    "v_pk_fma_f32 v[94:95], v[92:93], v[98:99], v[94:95] op_sel:[0,0,0] op_sel_hi:[1,0,1]\n\t"
    "v_pk_fma_f32 v[46:47], v[76:77], v[96:97], v[46:47] op_sel:[0,1,0] op_sel_hi:[1,1,1] neg_lo:[0,1,0] neg_hi:[0,1,0]\n\t"
    "s_add_u32 m0, s70, 101376\n\t"
    "v_pk_fma_f32 v[62:63], v[92:93], v[98:99], v[62:63] op_sel:[0,1,0] op_sel_hi:[1,1,1] neg_lo:[0,1,0] neg_hi:[0,1,0]\n\t"
    "buffer_load_dwordx4 v4, s[44:47], s64 offen lds\n\t"
    "v_pk_fma_f32 v[78:79], v[44:45], v[96:97], v[78:79] op_sel:[0,1,0] op_sel_hi:[1,1,1]\n\t"
    "v_pk_fma_f32 v[94:95], v[60:61], v[98:99], v[94:95] op_sel:[0,1,0] op_sel_hi:[1,1,1]\n\t"
    "v_cvt_pk_bf16_f32 v32, v32, v33\n\t"
    "v_cvt_pk_bf16_f32 v33, v34, v35\n\t"
    "v_cvt_pk_bf16_f32 v34, v36, v37\n\t"
    "v_cvt_pk_bf16_f32 v35, v38, v39\n\t"
    "v_cvt_pk_bf16_f32 v36, v40, v41\n\t"
    "v_cvt_pk_bf16_f32 v37, v42, v43\n\t"
    "v_cvt_pk_bf16_f32 v38, v44, v45\n\t"
    "v_cvt_pk_bf16_f32 v39, v46, v47\n\t"
    "buffer_load_dwordx4 a[104:107], v18, s[40:43], s66 offen offset:1024 sc1\n\t"
    "v_cvt_pk_bf16_f32 v48, v48, v49\n\t"
    "v_cvt_pk_bf16_f32 v49, v50, v51\n\t"
    "v_cvt_pk_bf16_f32 v50, v52, v53\n\t"
    "v_cvt_pk_bf16_f32 v51, v54, v55\n\t"
    "v_cvt_pk_bf16_f32 v52, v56, v57\n\t"
    "v_cvt_pk_bf16_f32 v53, v58, v59\n\t"
    "v_cvt_pk_bf16_f32 v54, v60, v61\n\t"
    "v_cvt_pk_bf16_f32 v55, v62, v63\n\t"
    "ds_write_b128 v24, v[32:35] offset:0\n\t"
    "ds_write_b128 v24, v[36:39] offset:16\n\t"
    "ds_write_b128 v24, v[48:51] offset:2304\n\t"
    "s_add_u32 m0, s71, 101376\n\t"
    "ds_write_b128 v24, v[52:55] offset:2320\n\t"
    "buffer_load_dwordx4 v5, s[44:47], s64 offen lds\n\t"
    "v_mov_b64_e32 v[192:193], 0\n\t"
    "v_mov_b64_e32 v[194:195], 0\n\t"
    "v_mov_b64_e32 v[196:197], 0\n\t"
    "v_mov_b64_e32 v[198:199], 0\n\t"
    "v_mov_b64_e32 v[200:201], 0\n\t"
    "v_mov_b64_e32 v[202:203], 0\n\t"
    "v_mov_b64_e32 v[204:205], 0\n\t"
    "v_mov_b64_e32 v[206:207], 0\n\t"
    "v_mov_b64_e32 v[208:209], 0\n\t"
    "v_mov_b64_e32 v[210:211], 0\n\t"
    "v_mov_b64_e32 v[212:213], 0\n\t"
    "v_mov_b64_e32 v[214:215], 0\n\t"
    "buffer_load_dwordx4 a[108:111], v6, s[40:43], s66 offen offset:1024 sc1\n\t"
    "v_mov_b64_e32 v[216:217], 0\n\t"
    "v_mov_b64_e32 v[218:219], 0\n\t"
    "v_mov_b64_e32 v[220:221], 0\n\t"
    "v_mov_b64_e32 v[222:223], 0\n\t"
    "s_waitcnt lgkmcnt(0)\n\t"
    "ds_read_b64_tr_b16 v[40:41], v25 offset:0\n\t"
    "ds_read_b64_tr_b16 v[42:43], v25 offset:576\n\t"
    "ds_read_b64_tr_b16 v[44:45], v25 offset:8\n\t"
    "ds_read_b64_tr_b16 v[46:47], v25 offset:584\n\t"
    "ds_read_b64_tr_b16 v[56:57], v25 offset:16\n\t"
    "ds_read_b64_tr_b16 v[58:59], v25 offset:592\n\t"
    "ds_read_b64_tr_b16 v[60:61], v25 offset:24\n\t"
    "s_add_u32 m0, s68, 118272\n\t"
    "ds_read_b64_tr_b16 v[62:63], v25 offset:600\n\t"
    "buffer_load_dwordx4 v2, s[44:47], s65 offen lds\n\t"
    "v_cvt_pk_bf16_f32 v64, -v64, -v65\n\t"
    "v_cvt_pk_bf16_f32 v65, -v66, -v67\n\t"
    "v_cvt_pk_bf16_f32 v66, -v68, -v69\n\t"
    "v_cvt_pk_bf16_f32 v67, -v70, -v71\n\t"
    "v_cvt_pk_bf16_f32 v68, -v72, -v73\n\t"
    "v_cvt_pk_bf16_f32 v69, -v74, -v75\n\t"
    "v_cvt_pk_bf16_f32 v70, -v76, -v77\n\t"
    "v_cvt_pk_bf16_f32 v71, -v78, -v79\n\t"
    "v_cvt_pk_bf16_f32 v80, -v80, -v81\n\t"
    "v_cvt_pk_bf16_f32 v81, -v82, -v83\n\t"
    "buffer_load_dwordx4 a[112:115], v18, s[40:43], s66 offen offset:2048 sc1\n\t"
    "v_cvt_pk_bf16_f32 v82, -v84, -v85\n\t"
    "v_cvt_pk_bf16_f32 v83, -v86, -v87\n\t"
    "v_cvt_pk_bf16_f32 v84, -v88, -v89\n\t"
    "v_cvt_pk_bf16_f32 v85, -v90, -v91\n\t"
    "v_cvt_pk_bf16_f32 v86, -v92, -v93\n\t"
    "v_cvt_pk_bf16_f32 v87, -v94, -v95\n\t"
    "s_waitcnt lgkmcnt(0)\n\t"
    "ds_write_b128 v24, v[64:67] offset:0\n\t"
    "ds_write_b128 v24, v[68:71] offset:16\n\t"
    "ds_write_b128 v24, v[80:83] offset:2304\n\t"
    "ds_write_b128 v24, v[84:87] offset:2320\n\t"
    "v_mov_b64_e32 v[224:225], 0\n\t"
    "s_add_u32 m0, s69, 118272\n\t"
    "v_mov_b64_e32 v[226:227], 0\n\t"
    "buffer_load_dwordx4 v3, s[44:47], s65 offen lds\n\t"
    "v_mov_b64_e32 v[228:229], 0\n\t"
    "v_mov_b64_e32 v[230:231], 0\n\t"
    "v_mov_b64_e32 v[232:233], 0\n\t"
    "v_mov_b64_e32 v[234:235], 0\n\t"
    "v_mov_b64_e32 v[236:237], 0\n\t"
    "v_mov_b64_e32 v[238:239], 0\n\t"
    "v_mov_b64_e32 v[240:241], 0\n\t"
    "v_mov_b64_e32 v[242:243], 0\n\t"
    "v_mov_b64_e32 v[244:245], 0\n\t"
    "v_mov_b64_e32 v[246:247], 0\n\t"
    "v_mov_b64_e32 v[248:249], 0\n\t"
    "v_mov_b64_e32 v[250:251], 0\n\t"
    "buffer_load_dwordx4 a[116:119], v6, s[40:43], s66 offen offset:2048 sc1\n\t"
    "v_mov_b64_e32 v[252:253], 0\n\t"
    "v_mov_b64_e32 v[254:255], 0\n\t"
    "s_waitcnt lgkmcnt(0)\n\t"
    "ds_read_b64_tr_b16 v[72:73], v25 offset:0\n\t"
    "ds_read_b64_tr_b16 v[74:75], v25 offset:576\n\t"
    "ds_read_b64_tr_b16 v[76:77], v25 offset:8\n\t"
    "ds_read_b64_tr_b16 v[78:79], v25 offset:584\n\t"
    "ds_read_b64_tr_b16 v[88:89], v25 offset:16\n\t"
    "ds_read_b64_tr_b16 v[90:91], v25 offset:592\n\t"
    "ds_read_b64_tr_b16 v[92:93], v25 offset:24\n\t"
    "ds_read_b64_tr_b16 v[94:95], v25 offset:600\n\t"
    "s_add_u32 m0, s70, 118272\n\t"
    "s_nop 0\n\t"
    "buffer_load_dwordx4 v4, s[44:47], s65 offen lds\n\t"
    "buffer_load_dwordx4 a[120:123], v18, s[40:43], s66 offen offset:3072 sc1\n\t"
    "s_add_u32 m0, s71, 118272\n\t"
    "s_nop 0\n\t"
    "buffer_load_dwordx4 v5, s[44:47], s65 offen lds\n\t"
    "buffer_load_dwordx4 a[124:127], v6, s[40:43], s66 offen offset:3072 sc1\n\t"
    "s_add_u32 s66, s66, 524288\n\t"
    "s_add_u32 s64, s64, s72\n\t"
    "s_add_u32 s65, s65, s72\n\t"
    "s_add_u32 s90, s89, 112\n\t"
    "s_lshl_b32 s90, s90, 17\n\t"
    "s_add_u32 s90, s90, 4194304\n\t"
    "s_add_u32 s94, s90, 4194304\n\t"
    "v_lshlrev_b32_e32 v14, 2, v31\n\t"
    "v_and_b32_e32 v14, 0x7c0, v14\n\t"
    "s_add_u32 s95, s99, 35688448\n\t"
    "v_mov_b32_e32 v104, s82\n\t"
    "v_mov_b32_e32 v105, s83\n\t"
    "v_mov_b32_e32 v106, v0\n\t"
    "v_readfirstlane_b32 s77, v27\n\t"
    "s_nop 1\n\t"
    "v_add_u32_e32 v107, s77, v31\n\t"
    "s_waitcnt lgkmcnt(0)\n\t"
    "buffer_store_dwordx4 v[40:43], v7, s[16:19], s90 offen offset:0 sc1\n\t"
    "buffer_store_dwordx4 v[44:47], v7, s[16:19], s90 offen offset:1024 sc1\n\t"
    "buffer_store_dwordx4 v[56:59], v7, s[16:19], s90 offen offset:2048 sc1\n\t"
    "buffer_store_dwordx4 v[60:63], v7, s[16:19], s90 offen offset:3072 sc1\n\t"
    "buffer_store_dwordx4 v[72:75], v7, s[16:19], s94 offen offset:0 sc1\n\t"
    "buffer_store_dwordx4 v[76:79], v7, s[16:19], s94 offen offset:1024 sc1\n\t"
    "buffer_store_dwordx4 v[88:91], v7, s[16:19], s94 offen offset:2048 sc1\n\t"
    "buffer_store_dwordx4 v[92:95], v7, s[16:19], s94 offen offset:3072 sc1\n\t"
    "s_add_u32 s94, s91, 35688448\n\t"
    "s_lshr_b32 s77, s87, 1\n\t"
    "s_lshl_b32 s90, s77, 1\n\t"
    "s_xor_b32 s33, s73, 0\n\t"
    "s_add_u32 s90, s90, s33\n\t"
    "s_lshl_b32 s33, s90, 6\n\t"
    "v_add_u32_e32 v112, s33, v19\n\t"
    "v_add_u32_e32 v116, 67584, v112\n\t"
    "s_lshl_b32 s33, s90, 12\n\t"
    "v_add_u32_e32 v64, s33, v20\n\t"
    "v_add_u32_e32 v72, 67584, v64\n\t"
    "v_add_u32_e32 v65, s33, v21\n\t"
    "v_add_u32_e32 v73, 67584, v65\n\t"
    "s_lshl_b32 s90, s77, 1\n\t"
    "s_xor_b32 s33, s73, 1\n\t"
    "s_add_u32 s90, s90, s33\n\t"
    "s_lshl_b32 s33, s90, 6\n\t"
    "v_add_u32_e32 v113, s33, v19\n\t"
    "v_add_u32_e32 v117, 67584, v113\n\t"
    "s_lshl_b32 s33, s90, 12\n\t"
    "v_add_u32_e32 v66, s33, v20\n\t"
    "v_add_u32_e32 v74, 67584, v66\n\t"
    "v_add_u32_e32 v67, s33, v21\n\t"
    "v_add_u32_e32 v75, 67584, v67\n\t"
    "s_xor_b32 s90, s77, 1\n\t"
    "s_lshl_b32 s90, s90, 1\n\t"
    "s_xor_b32 s33, s73, 0\n\t"
    "s_add_u32 s90, s90, s33\n\t"
    "s_lshl_b32 s33, s90, 6\n\t"
    "v_add_u32_e32 v114, s33, v19\n\t"
    "v_add_u32_e32 v118, 67584, v114\n\t"
    "s_lshl_b32 s33, s90, 12\n\t"
    "v_add_u32_e32 v68, s33, v20\n\t"
    "v_add_u32_e32 v76, 67584, v68\n\t"
    "v_add_u32_e32 v69, s33, v21\n\t"
    "v_add_u32_e32 v77, 67584, v69\n\t"
    "s_xor_b32 s90, s77, 1\n\t"
    "s_lshl_b32 s90, s90, 1\n\t"
    "s_xor_b32 s33, s73, 1\n\t"
    "s_add_u32 s90, s90, s33\n\t"
    "s_lshl_b32 s33, s90, 6\n\t"
    "v_add_u32_e32 v115, s33, v19\n\t"
    "v_add_u32_e32 v119, 67584, v115\n\t"
    "s_lshl_b32 s33, s90, 12\n\t"
    "v_add_u32_e32 v70, s33, v20\n\t"
    "v_add_u32_e32 v78, 67584, v70\n\t"
    "v_add_u32_e32 v71, s33, v21\n\t"
    "v_add_u32_e32 v79, 67584, v71\n\t"
    "s_waitcnt vmcnt(32)\n\t"
    "s_barrier\n\t"
    "ds_read_b64_tr_b16 a[128:129], v112 offset:0\n\t"
    "ds_read_b64_tr_b16 a[130:131], v112 offset:256\n\t"
    "ds_read_b64_tr_b16 a[132:133], v112 offset:512\n\t"
    "ds_read_b64_tr_b16 a[134:135], v112 offset:768\n\t"
    "ds_read_b64_tr_b16 a[136:137], v112 offset:32\n\t"
    "ds_read_b64_tr_b16 a[138:139], v112 offset:288\n\t"
    "ds_read_b64_tr_b16 a[140:141], v112 offset:544\n\t"
    "ds_read_b64_tr_b16 a[142:143], v112 offset:800\n\t"
    "ds_read_b64_tr_b16 a[144:145], v113 offset:0\n\t"
    "ds_read_b64_tr_b16 a[146:147], v113 offset:256\n\t"
    "ds_read_b64_tr_b16 a[148:149], v113 offset:512\n\t"
    "ds_read_b64_tr_b16 a[150:151], v113 offset:768\n\t"
    "s_waitcnt lgkmcnt(8)\n\t"
    "v_mfma_f32_16x16x32_bf16 v[128:131], a[128:131], a[0:3], v[128:131]\n\t"
    "ds_read_b64_tr_b16 a[152:153], v113 offset:32\n\t"
    "v_mfma_f32_16x16x32_bf16 v[128:131], a[132:135], a[4:7], v[128:131]\n\t"
    "v_mfma_f32_16x16x32_bf16 v[132:135], a[128:131], a[8:11], v[132:135]\n\t"
    "ds_read_b64_tr_b16 a[154:155], v113 offset:288\n\t"
    "v_mfma_f32_16x16x32_bf16 v[132:135], a[132:135], a[12:15], v[132:135]\n\t"
    "v_mfma_f32_16x16x32_bf16 v[136:139], a[128:131], a[16:19], v[136:139]\n\t"
    "ds_read_b64_tr_b16 a[156:157], v113 offset:544\n\t"
    "v_mfma_f32_16x16x32_bf16 v[136:139], a[132:135], a[20:23], v[136:139]\n\t"
    "v_mfma_f32_16x16x32_bf16 v[140:143], a[128:131], a[24:27], v[140:143]\n\t"
    "ds_read_b64_tr_b16 a[158:159], v113 offset:800\n\t"
    "v_mfma_f32_16x16x32_bf16 v[140:143], a[132:135], a[28:31], v[140:143]\n\t"
    "s_waitcnt lgkmcnt(8)\n\t"
    "v_mfma_f32_16x16x32_bf16 v[156:159], a[136:139], a[24:27], v[156:159]\n\t"
    "ds_read_b64_tr_b16 a[160:161], v114 offset:0\n\t"
    "v_mfma_f32_16x16x32_bf16 v[156:159], a[140:143], a[28:31], v[156:159]\n\t"
    "v_mfma_f32_16x16x32_bf16 v[152:155], a[136:139], a[16:19], v[152:155]\n\t"
    "ds_read_b64_tr_b16 a[162:163], v114 offset:256\n\t"
    "v_mfma_f32_16x16x32_bf16 v[152:155], a[140:143], a[20:23], v[152:155]\n\t"
    "v_mfma_f32_16x16x32_bf16 v[148:151], a[136:139], a[8:11], v[148:151]\n\t"
    "ds_read_b64_tr_b16 a[164:165], v114 offset:512\n\t"
    "v_mfma_f32_16x16x32_bf16 v[148:151], a[140:143], a[12:15], v[148:151]\n\t"
    "v_mfma_f32_16x16x32_bf16 v[144:147], a[136:139], a[0:3], v[144:147]\n\t"
    "ds_read_b64_tr_b16 a[166:167], v114 offset:768\n\t"
    "v_mfma_f32_16x16x32_bf16 v[144:147], a[140:143], a[4:7], v[144:147]\n\t"
    "s_waitcnt lgkmcnt(8)\n\t"
    "v_mfma_f32_16x16x32_bf16 v[160:163], a[144:147], a[0:3], v[160:163]\n\t"
    "ds_read_b64_tr_b16 a[168:169], v114 offset:32\n\t"
    "v_mfma_f32_16x16x32_bf16 v[160:163], a[148:151], a[4:7], v[160:163]\n\t"
    "v_mfma_f32_16x16x32_bf16 v[164:167], a[144:147], a[8:11], v[164:167]\n\t"
    "ds_read_b64_tr_b16 a[170:171], v114 offset:288\n\t"
    "v_mfma_f32_16x16x32_bf16 v[164:167], a[148:151], a[12:15], v[164:167]\n\t"
    "v_mfma_f32_16x16x32_bf16 v[168:171], a[144:147], a[16:19], v[168:171]\n\t"
    "ds_read_b64_tr_b16 a[172:173], v114 offset:544\n\t"
    "v_mfma_f32_16x16x32_bf16 v[168:171], a[148:151], a[20:23], v[168:171]\n\t"
    "v_mfma_f32_16x16x32_bf16 v[172:175], a[144:147], a[24:27], v[172:175]\n\t"
    "ds_read_b64_tr_b16 a[174:175], v114 offset:800\n\t"
    "v_mfma_f32_16x16x32_bf16 v[172:175], a[148:151], a[28:31], v[172:175]\n\t"
    "s_waitcnt lgkmcnt(8)\n\t"
    "v_mfma_f32_16x16x32_bf16 v[188:191], a[152:155], a[24:27], v[188:191]\n\t"
    "ds_read_b64_tr_b16 a[176:177], v115 offset:0\n\t"
    "v_mfma_f32_16x16x32_bf16 v[188:191], a[156:159], a[28:31], v[188:191]\n\t"
    "v_mfma_f32_16x16x32_bf16 v[184:187], a[152:155], a[16:19], v[184:187]\n\t"
    "ds_read_b64_tr_b16 a[178:179], v115 offset:256\n\t"
    "v_mfma_f32_16x16x32_bf16 v[184:187], a[156:159], a[20:23], v[184:187]\n\t"
    "v_mfma_f32_16x16x32_bf16 v[180:183], a[152:155], a[8:11], v[180:183]\n\t"
    "ds_read_b64_tr_b16 a[180:181], v115 offset:512\n\t"
    "v_mfma_f32_16x16x32_bf16 v[180:183], a[156:159], a[12:15], v[180:183]\n\t"
    "v_mfma_f32_16x16x32_bf16 v[176:179], a[152:155], a[0:3], v[176:179]\n\t"
    "ds_read_b64_tr_b16 a[182:183], v115 offset:768\n\t"
    "v_mfma_f32_16x16x32_bf16 v[176:179], a[156:159], a[4:7], v[176:179]\n\t"
    "s_waitcnt lgkmcnt(8)\n\t"
    "v_mfma_f32_16x16x32_bf16 v[192:195], a[160:163], a[0:3], v[192:195]\n\t"
    "ds_read_b64_tr_b16 a[184:185], v115 offset:32\n\t"
    "v_mfma_f32_16x16x32_bf16 v[192:195], a[164:167], a[4:7], v[192:195]\n\t"
    "v_mfma_f32_16x16x32_bf16 v[196:199], a[160:163], a[8:11], v[196:199]\n\t"
    "ds_read_b64_tr_b16 a[186:187], v115 offset:288\n\t"
    "v_mfma_f32_16x16x32_bf16 v[196:199], a[164:167], a[12:15], v[196:199]\n\t"
    "v_mfma_f32_16x16x32_bf16 v[200:203], a[160:163], a[16:19], v[200:203]\n\t"
    "ds_read_b64_tr_b16 a[188:189], v115 offset:544\n\t"
    "v_mfma_f32_16x16x32_bf16 v[200:203], a[164:167], a[20:23], v[200:203]\n\t"
    "v_mfma_f32_16x16x32_bf16 v[204:207], a[160:163], a[24:27], v[204:207]\n\t"
    "ds_read_b64_tr_b16 a[190:191], v115 offset:800\n\t"
    "v_mfma_f32_16x16x32_bf16 v[204:207], a[164:167], a[28:31], v[204:207]\n\t"
    "s_waitcnt lgkmcnt(8)\n\t"
    "v_mfma_f32_16x16x32_bf16 v[220:223], a[168:171], a[24:27], v[220:223]\n\t"
    "v_mfma_f32_16x16x32_bf16 v[220:223], a[172:175], a[28:31], v[220:223]\n\t"
    "v_mfma_f32_16x16x32_bf16 v[216:219], a[168:171], a[16:19], v[216:219]\n\t"
    "v_mfma_f32_16x16x32_bf16 v[216:219], a[172:175], a[20:23], v[216:219]\n\t"
    "v_mfma_f32_16x16x32_bf16 v[212:215], a[168:171], a[8:11], v[212:215]\n\t"
    "v_mfma_f32_16x16x32_bf16 v[212:215], a[172:175], a[12:15], v[212:215]\n\t"
    "v_mfma_f32_16x16x32_bf16 v[208:211], a[168:171], a[0:3], v[208:211]\n\t"
    "v_mfma_f32_16x16x32_bf16 v[208:211], a[172:175], a[4:7], v[208:211]\n\t"
    "s_waitcnt lgkmcnt(0)\n\t"
    "s_barrier\n\t"
    "v_mfma_f32_16x16x32_bf16 v[224:227], a[176:179], a[0:3], v[224:227]\n\t"
    "ds_read_b64_tr_b16 a[128:129], v112 offset:33792\n\t"
    "v_mfma_f32_16x16x32_bf16 v[224:227], a[180:183], a[4:7], v[224:227]\n\t"
    "v_mfma_f32_16x16x32_bf16 v[228:231], a[176:179], a[8:11], v[228:231]\n\t"
    "ds_read_b64_tr_b16 a[130:131], v112 offset:34048\n\t"
    "v_mfma_f32_16x16x32_bf16 v[228:231], a[180:183], a[12:15], v[228:231]\n\t"
    "ds_read_b64_tr_b16 a[132:133], v112 offset:34304\n\t"
    "v_mfma_f32_16x16x32_bf16 v[232:235], a[176:179], a[16:19], v[232:235]\n\t"
    "ds_read_b64_tr_b16 a[134:135], v112 offset:34560\n\t"
    "v_mfma_f32_16x16x32_bf16 v[232:235], a[180:183], a[20:23], v[232:235]\n\t"
    "v_mfma_f32_16x16x32_bf16 v[236:239], a[176:179], a[24:27], v[236:239]\n\t"
    "ds_read_b64_tr_b16 a[136:137], v112 offset:33824\n\t"
    "v_mfma_f32_16x16x32_bf16 v[236:239], a[180:183], a[28:31], v[236:239]\n\t"
    "ds_read_b64_tr_b16 a[138:139], v112 offset:34080\n\t"
    "v_mfma_f32_16x16x32_bf16 v[252:255], a[184:187], a[24:27], v[252:255]\n\t"
    "ds_read_b64_tr_b16 a[140:141], v112 offset:34336\n\t"
    "v_mfma_f32_16x16x32_bf16 v[252:255], a[188:191], a[28:31], v[252:255]\n\t"
    "v_mfma_f32_16x16x32_bf16 v[248:251], a[184:187], a[16:19], v[248:251]\n\t"
    "ds_read_b64_tr_b16 a[142:143], v112 offset:34592\n\t"
    "v_mfma_f32_16x16x32_bf16 v[248:251], a[188:191], a[20:23], v[248:251]\n\t"
    "ds_read_b64_tr_b16 a[144:145], v113 offset:33792\n\t"
    "v_mfma_f32_16x16x32_bf16 v[244:247], a[184:187], a[8:11], v[244:247]\n\t"
    "ds_read_b64_tr_b16 a[146:147], v113 offset:34048\n\t"
    "v_mfma_f32_16x16x32_bf16 v[244:247], a[188:191], a[12:15], v[244:247]\n\t"
    "v_mfma_f32_16x16x32_bf16 v[240:243], a[184:187], a[0:3], v[240:243]\n\t"
    "ds_read_b64_tr_b16 a[148:149], v113 offset:34304\n\t"
    "v_mfma_f32_16x16x32_bf16 v[240:243], a[188:191], a[4:7], v[240:243]\n\t"
    "ds_read_b64_tr_b16 a[150:151], v113 offset:34560\n\t"
    "s_branch L_loopW1_%=\n\t"
    "L_loopW_%=:\n\t"
    "s_waitcnt lgkmcnt(8)\n\t"
    "v_mfma_f32_16x16x32_bf16 v[128:131], a[128:131], a[0:3], v[128:131]\n\t"
    "s_add_u32 m0, s68, 101376\n\t"
    "ds_read_b64_tr_b16 a[152:153], v113 offset:32\n\t"
    "v_mfma_f32_16x16x32_bf16 v[128:131], a[132:135], a[4:7], v[128:131]\n\t"
    "buffer_load_dwordx4 v2, s[44:47], s64 offen lds\n\t"
    "v_mfma_f32_16x16x32_bf16 v[132:135], a[128:131], a[8:11], v[132:135]\n\t"
    "ds_read_b64_tr_b16 a[154:155], v113 offset:288\n\t"
    "v_mfma_f32_16x16x32_bf16 v[132:135], a[132:135], a[12:15], v[132:135]\n\t"
    "v_mfma_f32_16x16x32_bf16 v[136:139], a[128:131], a[16:19], v[136:139]\n\t"
    "ds_read_b64_tr_b16 a[156:157], v113 offset:544\n\t"
    "v_mfma_f32_16x16x32_bf16 v[136:139], a[132:135], a[20:23], v[136:139]\n\t"
    "buffer_load_dwordx4 a[96:99], v18, s[40:43], s66 offen offset:0 sc1\n\t"
    "v_mfma_f32_16x16x32_bf16 v[140:143], a[128:131], a[24:27], v[140:143]\n\t"
    "ds_read_b64_tr_b16 a[158:159], v113 offset:800\n\t"
    "v_mfma_f32_16x16x32_bf16 v[140:143], a[132:135], a[28:31], v[140:143]\n\t"
    "s_waitcnt lgkmcnt(8)\n\t"
    "v_mfma_f32_16x16x32_bf16 v[156:159], a[136:139], a[24:27], v[156:159]\n\t"
    "s_add_u32 m0, s69, 101376\n\t"
    "ds_read_b64_tr_b16 a[160:161], v114 offset:0\n\t"
    "v_mfma_f32_16x16x32_bf16 v[156:159], a[140:143], a[28:31], v[156:159]\n\t"
    "buffer_load_dwordx4 v3, s[44:47], s64 offen lds\n\t"
    "v_mfma_f32_16x16x32_bf16 v[152:155], a[136:139], a[16:19], v[152:155]\n\t"
    "ds_read_b64_tr_b16 a[162:163], v114 offset:256\n\t"
    "v_mfma_f32_16x16x32_bf16 v[152:155], a[140:143], a[20:23], v[152:155]\n\t"
    "v_mfma_f32_16x16x32_bf16 v[148:151], a[136:139], a[8:11], v[148:151]\n\t"
    "ds_read_b64_tr_b16 a[164:165], v114 offset:512\n\t"
    "v_mfma_f32_16x16x32_bf16 v[148:151], a[140:143], a[12:15], v[148:151]\n\t"
    "buffer_load_dwordx4 a[100:103], v6, s[40:43], s66 offen offset:0 sc1\n\t"
    "v_mfma_f32_16x16x32_bf16 v[144:147], a[136:139], a[0:3], v[144:147]\n\t"
    "ds_read_b64_tr_b16 a[166:167], v114 offset:768\n\t"
    "v_mfma_f32_16x16x32_bf16 v[144:147], a[140:143], a[4:7], v[144:147]\n\t"
    "s_waitcnt lgkmcnt(8)\n\t"
    "v_mfma_f32_16x16x32_bf16 v[160:163], a[144:147], a[0:3], v[160:163]\n\t"
    "s_add_u32 m0, s70, 101376\n\t"
    "ds_read_b64_tr_b16 a[168:169], v114 offset:32\n\t"
    "v_mfma_f32_16x16x32_bf16 v[160:163], a[148:151], a[4:7], v[160:163]\n\t"
    "buffer_load_dwordx4 v4, s[44:47], s64 offen lds\n\t"
    "v_mfma_f32_16x16x32_bf16 v[164:167], a[144:147], a[8:11], v[164:167]\n\t"
    "ds_read_b64_tr_b16 a[170:171], v114 offset:288\n\t"
    "v_mfma_f32_16x16x32_bf16 v[164:167], a[148:151], a[12:15], v[164:167]\n\t"
    "v_mfma_f32_16x16x32_bf16 v[168:171], a[144:147], a[16:19], v[168:171]\n\t"
    "ds_read_b64_tr_b16 a[172:173], v114 offset:544\n\t"
    "v_mfma_f32_16x16x32_bf16 v[168:171], a[148:151], a[20:23], v[168:171]\n\t"
    "buffer_load_dwordx4 a[104:107], v18, s[40:43], s66 offen offset:1024 sc1\n\t"
    "v_mfma_f32_16x16x32_bf16 v[172:175], a[144:147], a[24:27], v[172:175]\n\t"
    "ds_read_b64_tr_b16 a[174:175], v114 offset:800\n\t"
    "v_mfma_f32_16x16x32_bf16 v[172:175], a[148:151], a[28:31], v[172:175]\n\t"
    "s_waitcnt lgkmcnt(8)\n\t"
    "v_mfma_f32_16x16x32_bf16 v[188:191], a[152:155], a[24:27], v[188:191]\n\t"
    "s_add_u32 m0, s71, 101376\n\t"
    "ds_read_b64_tr_b16 a[176:177], v115 offset:0\n\t"
    "v_mfma_f32_16x16x32_bf16 v[188:191], a[156:159], a[28:31], v[188:191]\n\t"
    "buffer_load_dwordx4 v5, s[44:47], s64 offen lds\n\t"
    "v_mfma_f32_16x16x32_bf16 v[184:187], a[152:155], a[16:19], v[184:187]\n\t"
    "ds_read_b64_tr_b16 a[178:179], v115 offset:256\n\t"
    "v_mfma_f32_16x16x32_bf16 v[184:187], a[156:159], a[20:23], v[184:187]\n\t"
    "v_mfma_f32_16x16x32_bf16 v[180:183], a[152:155], a[8:11], v[180:183]\n\t"
    "ds_read_b64_tr_b16 a[180:181], v115 offset:512\n\t"
    "v_mfma_f32_16x16x32_bf16 v[180:183], a[156:159], a[12:15], v[180:183]\n\t"
    "buffer_load_dwordx4 a[108:111], v6, s[40:43], s66 offen offset:1024 sc1\n\t"
    "v_mfma_f32_16x16x32_bf16 v[176:179], a[152:155], a[0:3], v[176:179]\n\t"
    "ds_read_b64_tr_b16 a[182:183], v115 offset:768\n\t"
    "v_mfma_f32_16x16x32_bf16 v[176:179], a[156:159], a[4:7], v[176:179]\n\t"
    "s_waitcnt lgkmcnt(8)\n\t"
    "v_mfma_f32_16x16x32_bf16 v[192:195], a[160:163], a[0:3], v[192:195]\n\t"
    "s_add_u32 m0, s68, 118272\n\t"
    "ds_read_b64_tr_b16 a[184:185], v115 offset:32\n\t"
    "v_mfma_f32_16x16x32_bf16 v[192:195], a[164:167], a[4:7], v[192:195]\n\t"
    "buffer_load_dwordx4 v2, s[44:47], s65 offen lds\n\t"
    "v_mfma_f32_16x16x32_bf16 v[196:199], a[160:163], a[8:11], v[196:199]\n\t"
    "ds_read_b64_tr_b16 a[186:187], v115 offset:288\n\t"
    "v_mfma_f32_16x16x32_bf16 v[196:199], a[164:167], a[12:15], v[196:199]\n\t"
    "v_mfma_f32_16x16x32_bf16 v[200:203], a[160:163], a[16:19], v[200:203]\n\t"
    "ds_read_b64_tr_b16 a[188:189], v115 offset:544\n\t"
    "v_mfma_f32_16x16x32_bf16 v[200:203], a[164:167], a[20:23], v[200:203]\n\t"
    "buffer_load_dwordx4 a[112:115], v18, s[40:43], s66 offen offset:2048 sc1\n\t"
    "v_mfma_f32_16x16x32_bf16 v[204:207], a[160:163], a[24:27], v[204:207]\n\t"
    "ds_read_b64_tr_b16 a[190:191], v115 offset:800\n\t"
    "v_mfma_f32_16x16x32_bf16 v[204:207], a[164:167], a[28:31], v[204:207]\n\t"
    "s_waitcnt lgkmcnt(8)\n\t"
    "v_mfma_f32_16x16x32_bf16 v[220:223], a[168:171], a[24:27], v[220:223]\n\t"
    "s_add_u32 m0, s69, 118272\n\t"
    "v_mfma_f32_16x16x32_bf16 v[220:223], a[172:175], a[28:31], v[220:223]\n\t"
    "buffer_load_dwordx4 v3, s[44:47], s65 offen lds\n\t"
    "v_mfma_f32_16x16x32_bf16 v[216:219], a[168:171], a[16:19], v[216:219]\n\t"
    "v_mfma_f32_16x16x32_bf16 v[216:219], a[172:175], a[20:23], v[216:219]\n\t"
    "v_mfma_f32_16x16x32_bf16 v[212:215], a[168:171], a[8:11], v[212:215]\n\t"
    "v_mfma_f32_16x16x32_bf16 v[212:215], a[172:175], a[12:15], v[212:215]\n\t"
    "buffer_load_dwordx4 a[116:119], v6, s[40:43], s66 offen offset:2048 sc1\n\t"
    "v_mfma_f32_16x16x32_bf16 v[208:211], a[168:171], a[0:3], v[208:211]\n\t"
    "v_mfma_f32_16x16x32_bf16 v[208:211], a[172:175], a[4:7], v[208:211]\n\t"
    "s_waitcnt vmcnt(27) lgkmcnt(0)\n\t"
    "s_barrier\n\t"
    "v_mfma_f32_16x16x32_bf16 v[224:227], a[176:179], a[0:3], v[224:227]\n\t"
    "s_add_u32 m0, s70, 118272\n\t"
    "ds_read_b64_tr_b16 a[128:129], v112 offset:33792\n\t"
    "v_mfma_f32_16x16x32_bf16 v[224:227], a[180:183], a[4:7], v[224:227]\n\t"
    "buffer_load_dwordx4 v4, s[44:47], s65 offen lds\n\t"
    "v_mfma_f32_16x16x32_bf16 v[228:231], a[176:179], a[8:11], v[228:231]\n\t"
    "ds_read_b64_tr_b16 a[130:131], v112 offset:34048\n\t"
    "v_mfma_f32_16x16x32_bf16 v[228:231], a[180:183], a[12:15], v[228:231]\n\t"
    "ds_read_b64_tr_b16 a[132:133], v112 offset:34304\n\t"
    "v_mfma_f32_16x16x32_bf16 v[232:235], a[176:179], a[16:19], v[232:235]\n\t"
    "ds_read_b64_tr_b16 a[134:135], v112 offset:34560\n\t"
    "v_mfma_f32_16x16x32_bf16 v[232:235], a[180:183], a[20:23], v[232:235]\n\t"
    "buffer_load_dwordx4 a[120:123], v18, s[40:43], s66 offen offset:3072 sc1\n\t"
    "v_mfma_f32_16x16x32_bf16 v[236:239], a[176:179], a[24:27], v[236:239]\n\t"
    "ds_read_b64_tr_b16 a[136:137], v112 offset:33824\n\t"
    "v_mfma_f32_16x16x32_bf16 v[236:239], a[180:183], a[28:31], v[236:239]\n\t"
    "ds_read_b64_tr_b16 a[138:139], v112 offset:34080\n\t"
    "v_mfma_f32_16x16x32_bf16 v[252:255], a[184:187], a[24:27], v[252:255]\n\t"
    "s_add_u32 m0, s71, 118272\n\t"
    "ds_read_b64_tr_b16 a[140:141], v112 offset:34336\n\t"
    "v_mfma_f32_16x16x32_bf16 v[252:255], a[188:191], a[28:31], v[252:255]\n\t"
    "buffer_load_dwordx4 v5, s[44:47], s65 offen lds\n\t"
    "v_mfma_f32_16x16x32_bf16 v[248:251], a[184:187], a[16:19], v[248:251]\n\t"
    "s_add_u32 s64, s64, s72\n\t"
    "ds_read_b64_tr_b16 a[142:143], v112 offset:34592\n\t"
    "v_mfma_f32_16x16x32_bf16 v[248:251], a[188:191], a[20:23], v[248:251]\n\t"
    "s_add_u32 s65, s65, s72\n\t"
    "ds_read_b64_tr_b16 a[144:145], v113 offset:33792\n\t"
    "v_mfma_f32_16x16x32_bf16 v[244:247], a[184:187], a[8:11], v[244:247]\n\t"
    "ds_read_b64_tr_b16 a[146:147], v113 offset:34048\n\t"
    "v_mfma_f32_16x16x32_bf16 v[244:247], a[188:191], a[12:15], v[244:247]\n\t"
    "buffer_load_dwordx4 a[124:127], v6, s[40:43], s66 offen offset:3072 sc1\n\t"
    "v_mfma_f32_16x16x32_bf16 v[240:243], a[184:187], a[0:3], v[240:243]\n\t"
    "s_add_u32 s66, s66, 524288\n\t"
    "ds_read_b64_tr_b16 a[148:149], v113 offset:34304\n\t"
    "v_mfma_f32_16x16x32_bf16 v[240:243], a[188:191], a[4:7], v[240:243]\n\t"
    "ds_read_b64_tr_b16 a[150:151], v113 offset:34560\n\t"
    "L_loopW1_%=:\n\t"
    "s_cmp_lg_u32 s74, s75\n\t"
    "s_cbranch_scc1 L_retW_%=\n\t"
    "s_mov_b32 s90, 0\n\t"
    "L_spS_%=:\n\t"
    "v_cmp_eq_u64_e32 vcc, s[82:83], v[12:13]\n\t"
    "s_cmp_eq_u64 vcc, -1\n\t"
    "s_cbranch_scc1 L_spSok_%=\n\t"
    "buffer_load_dwordx2 v[12:13], v14, s[16:19], s95 offen sc1\n\t"
    "s_add_u32 s90, s90, 1\n\t"
    "s_waitcnt vmcnt(0)\n\t"
    "s_cmp_lt_u32 s90, 0x40000\n\t"
    "s_cbranch_scc1 L_spS_%=\n\t"
    "L_spSok_%=:\n\t"
    "s_cmp_eq_u32 s73, 0\n\t"
    "s_cselect_b32 s44, s48, s52\n\t"
    "s_cselect_b32 s45, s49, s53\n\t"
    "s_mov_b32 s46, s50\n\t"
    "s_cselect_b32 s64, 0, 0x200\n\t"
    "s_cselect_b32 s90, 0, 11534336\n\t"
    "s_cselect_b32 s75, 4, -1\n\t"
    "s_add_u32 s66, s66, s90\n\t"
    "s_add_u32 s65, s64, 128\n\t"
    "s_mov_b32 s72, 256\n\t"
    "v_mov_b32_e32 v2, v17\n\t"
    "v_add_u32_e32 v3, 0x4000, v17\n\t"
    "v_add_u32_e32 v4, 0x8000, v17\n\t"
    "v_add_u32_e32 v5, 0xc000, v17\n\t"
    "s_mov_b32 s68, s78\n\t"
    "s_mov_b32 s69, s79\n\t"
    "s_mov_b32 s70, s80\n\t"
    "s_mov_b32 s71, s81\n\t"
    "L_retW_%=:\n\t"
    "s_waitcnt lgkmcnt(8)\n\t"
    "v_mfma_f32_16x16x32_bf16 v[128:131], a[128:131], a[32:35], v[128:131]\n\t"
    "s_add_u32 m0, s68, 0\n\t"
    "ds_read_b64_tr_b16 a[152:153], v113 offset:33824\n\t"
    "v_mfma_f32_16x16x32_bf16 v[128:131], a[132:135], a[36:39], v[128:131]\n\t"
    "buffer_load_dwordx4 v2, s[44:47], s64 offen lds\n\t"
    "v_mfma_f32_16x16x32_bf16 v[132:135], a[128:131], a[40:43], v[132:135]\n\t"
    "ds_read_b64_tr_b16 a[154:155], v113 offset:34080\n\t"
    "v_mfma_f32_16x16x32_bf16 v[132:135], a[132:135], a[44:47], v[132:135]\n\t"
    "v_mfma_f32_16x16x32_bf16 v[136:139], a[128:131], a[48:51], v[136:139]\n\t"
    "ds_read_b64_tr_b16 a[156:157], v113 offset:34336\n\t"
    "v_mfma_f32_16x16x32_bf16 v[136:139], a[132:135], a[52:55], v[136:139]\n\t"
    "buffer_load_dwordx4 a[0:3], v18, s[40:43], s66 offen offset:0 sc1\n\t"
    "v_mfma_f32_16x16x32_bf16 v[140:143], a[128:131], a[56:59], v[140:143]\n\t"
    "ds_read_b64_tr_b16 a[158:159], v113 offset:34592\n\t"
    "v_mfma_f32_16x16x32_bf16 v[140:143], a[132:135], a[60:63], v[140:143]\n\t"
    "s_waitcnt lgkmcnt(8)\n\t"
    "v_mfma_f32_16x16x32_bf16 v[156:159], a[136:139], a[56:59], v[156:159]\n\t"
    "s_add_u32 m0, s69, 0\n\t"
    "ds_read_b64_tr_b16 a[160:161], v114 offset:33792\n\t"
    "v_mfma_f32_16x16x32_bf16 v[156:159], a[140:143], a[60:63], v[156:159]\n\t"
    "buffer_load_dwordx4 v3, s[44:47], s64 offen lds\n\t"
    "v_mfma_f32_16x16x32_bf16 v[152:155], a[136:139], a[48:51], v[152:155]\n\t"
    "ds_read_b64_tr_b16 a[162:163], v114 offset:34048\n\t"
    "v_mfma_f32_16x16x32_bf16 v[152:155], a[140:143], a[52:55], v[152:155]\n\t"
    "v_mfma_f32_16x16x32_bf16 v[148:151], a[136:139], a[40:43], v[148:151]\n\t"
    "ds_read_b64_tr_b16 a[164:165], v114 offset:34304\n\t"
    "v_mfma_f32_16x16x32_bf16 v[148:151], a[140:143], a[44:47], v[148:151]\n\t"
    "buffer_load_dwordx4 a[4:7], v6, s[40:43], s66 offen offset:0 sc1\n\t"
    "v_mfma_f32_16x16x32_bf16 v[144:147], a[136:139], a[32:35], v[144:147]\n\t"
    "ds_read_b64_tr_b16 a[166:167], v114 offset:34560\n\t"
    "v_mfma_f32_16x16x32_bf16 v[144:147], a[140:143], a[36:39], v[144:147]\n\t"
    "s_waitcnt lgkmcnt(8)\n\t"
    "v_mfma_f32_16x16x32_bf16 v[160:163], a[144:147], a[32:35], v[160:163]\n\t"
    "s_add_u32 m0, s70, 0\n\t"
    "ds_read_b64_tr_b16 a[168:169], v114 offset:33824\n\t"
    "v_mfma_f32_16x16x32_bf16 v[160:163], a[148:151], a[36:39], v[160:163]\n\t"
    "buffer_load_dwordx4 v4, s[44:47], s64 offen lds\n\t"
    "v_mfma_f32_16x16x32_bf16 v[164:167], a[144:147], a[40:43], v[164:167]\n\t"
    "ds_read_b64_tr_b16 a[170:171], v114 offset:34080\n\t"
    "v_mfma_f32_16x16x32_bf16 v[164:167], a[148:151], a[44:47], v[164:167]\n\t"
    "v_mfma_f32_16x16x32_bf16 v[168:171], a[144:147], a[48:51], v[168:171]\n\t"
    "ds_read_b64_tr_b16 a[172:173], v114 offset:34336\n\t"
    "v_mfma_f32_16x16x32_bf16 v[168:171], a[148:151], a[52:55], v[168:171]\n\t"
    "buffer_load_dwordx4 a[8:11], v18, s[40:43], s66 offen offset:1024 sc1\n\t"
    "v_mfma_f32_16x16x32_bf16 v[172:175], a[144:147], a[56:59], v[172:175]\n\t"
    "ds_read_b64_tr_b16 a[174:175], v114 offset:34592\n\t"
    "v_mfma_f32_16x16x32_bf16 v[172:175], a[148:151], a[60:63], v[172:175]\n\t"
    "s_waitcnt lgkmcnt(8)\n\t"
    "v_mfma_f32_16x16x32_bf16 v[188:191], a[152:155], a[56:59], v[188:191]\n\t"
    "s_add_u32 m0, s71, 0\n\t"
    "ds_read_b64_tr_b16 a[176:177], v115 offset:33792\n\t"
    "v_mfma_f32_16x16x32_bf16 v[188:191], a[156:159], a[60:63], v[188:191]\n\t"
    "buffer_load_dwordx4 v5, s[44:47], s64 offen lds\n\t"
    "v_mfma_f32_16x16x32_bf16 v[184:187], a[152:155], a[48:51], v[184:187]\n\t"
    "ds_read_b64_tr_b16 a[178:179], v115 offset:34048\n\t"
    "v_mfma_f32_16x16x32_bf16 v[184:187], a[156:159], a[52:55], v[184:187]\n\t"
    "v_mfma_f32_16x16x32_bf16 v[180:183], a[152:155], a[40:43], v[180:183]\n\t"
    "ds_read_b64_tr_b16 a[180:181], v115 offset:34304\n\t"
    "v_mfma_f32_16x16x32_bf16 v[180:183], a[156:159], a[44:47], v[180:183]\n\t"
    "buffer_load_dwordx4 a[12:15], v6, s[40:43], s66 offen offset:1024 sc1\n\t"
    "v_mfma_f32_16x16x32_bf16 v[176:179], a[152:155], a[32:35], v[176:179]\n\t"
    "ds_read_b64_tr_b16 a[182:183], v115 offset:34560\n\t"
    "v_mfma_f32_16x16x32_bf16 v[176:179], a[156:159], a[36:39], v[176:179]\n\t"
    "s_waitcnt lgkmcnt(8)\n\t"
    "v_mfma_f32_16x16x32_bf16 v[192:195], a[160:163], a[32:35], v[192:195]\n\t"
    "s_add_u32 m0, s68, 16896\n\t"
    "ds_read_b64_tr_b16 a[184:185], v115 offset:33824\n\t"
    "v_mfma_f32_16x16x32_bf16 v[192:195], a[164:167], a[36:39], v[192:195]\n\t"
    "buffer_load_dwordx4 v2, s[44:47], s65 offen lds\n\t"
    "v_mfma_f32_16x16x32_bf16 v[196:199], a[160:163], a[40:43], v[196:199]\n\t"
    "ds_read_b64_tr_b16 a[186:187], v115 offset:34080\n\t"
    "v_mfma_f32_16x16x32_bf16 v[196:199], a[164:167], a[44:47], v[196:199]\n\t"
    "v_mfma_f32_16x16x32_bf16 v[200:203], a[160:163], a[48:51], v[200:203]\n\t"
    "ds_read_b64_tr_b16 a[188:189], v115 offset:34336\n\t"
    "v_mfma_f32_16x16x32_bf16 v[200:203], a[164:167], a[52:55], v[200:203]\n\t"
    "buffer_load_dwordx4 a[16:19], v18, s[40:43], s66 offen offset:2048 sc1\n\t"
    "v_mfma_f32_16x16x32_bf16 v[204:207], a[160:163], a[56:59], v[204:207]\n\t"
    "ds_read_b64_tr_b16 a[190:191], v115 offset:34592\n\t"
    "v_mfma_f32_16x16x32_bf16 v[204:207], a[164:167], a[60:63], v[204:207]\n\t"
    "s_waitcnt lgkmcnt(8)\n\t"
    "v_mfma_f32_16x16x32_bf16 v[220:223], a[168:171], a[56:59], v[220:223]\n\t"
    "s_add_u32 m0, s69, 16896\n\t"
    "v_mfma_f32_16x16x32_bf16 v[220:223], a[172:175], a[60:63], v[220:223]\n\t"
    "buffer_load_dwordx4 v3, s[44:47], s65 offen lds\n\t"
    "v_mfma_f32_16x16x32_bf16 v[216:219], a[168:171], a[48:51], v[216:219]\n\t"
    "v_mfma_f32_16x16x32_bf16 v[216:219], a[172:175], a[52:55], v[216:219]\n\t"
    "v_mfma_f32_16x16x32_bf16 v[212:215], a[168:171], a[40:43], v[212:215]\n\t"
    "v_mfma_f32_16x16x32_bf16 v[212:215], a[172:175], a[44:47], v[212:215]\n\t"
    "buffer_load_dwordx4 a[20:23], v6, s[40:43], s66 offen offset:2048 sc1\n\t"
    "v_mfma_f32_16x16x32_bf16 v[208:211], a[168:171], a[32:35], v[208:211]\n\t"
    "v_mfma_f32_16x16x32_bf16 v[208:211], a[172:175], a[36:39], v[208:211]\n\t"
    "s_waitcnt vmcnt(28) lgkmcnt(0)\n\t"
    "s_barrier\n\t"
    "v_mfma_f32_16x16x32_bf16 v[224:227], a[176:179], a[32:35], v[224:227]\n\t"
    "s_add_u32 m0, s70, 16896\n\t"
    "ds_read_b64_tr_b16 a[128:129], v116 offset:0\n\t"
    "v_mfma_f32_16x16x32_bf16 v[224:227], a[180:183], a[36:39], v[224:227]\n\t"
    "buffer_load_dwordx4 v4, s[44:47], s65 offen lds\n\t"
    "v_mfma_f32_16x16x32_bf16 v[228:231], a[176:179], a[40:43], v[228:231]\n\t"
    "ds_read_b64_tr_b16 a[130:131], v116 offset:256\n\t"
    "v_mfma_f32_16x16x32_bf16 v[228:231], a[180:183], a[44:47], v[228:231]\n\t"
    "ds_read_b64_tr_b16 a[132:133], v116 offset:512\n\t"
    "v_mfma_f32_16x16x32_bf16 v[232:235], a[176:179], a[48:51], v[232:235]\n\t"
    "ds_read_b64_tr_b16 a[134:135], v116 offset:768\n\t"
    "v_mfma_f32_16x16x32_bf16 v[232:235], a[180:183], a[52:55], v[232:235]\n\t"
    "buffer_load_dwordx4 a[24:27], v18, s[40:43], s66 offen offset:3072 sc1\n\t"
    "v_mfma_f32_16x16x32_bf16 v[236:239], a[176:179], a[56:59], v[236:239]\n\t"
    "ds_read_b64_tr_b16 a[136:137], v116 offset:32\n\t"
    "v_mfma_f32_16x16x32_bf16 v[236:239], a[180:183], a[60:63], v[236:239]\n\t"
    "ds_read_b64_tr_b16 a[138:139], v116 offset:288\n\t"
    "v_mfma_f32_16x16x32_bf16 v[252:255], a[184:187], a[56:59], v[252:255]\n\t"
    "s_add_u32 m0, s71, 16896\n\t"
    "ds_read_b64_tr_b16 a[140:141], v116 offset:544\n\t"
    "v_mfma_f32_16x16x32_bf16 v[252:255], a[188:191], a[60:63], v[252:255]\n\t"
    "buffer_load_dwordx4 v5, s[44:47], s65 offen lds\n\t"
    "v_mfma_f32_16x16x32_bf16 v[248:251], a[184:187], a[48:51], v[248:251]\n\t"
    "s_add_u32 s64, s64, s72\n\t"
    "ds_read_b64_tr_b16 a[142:143], v116 offset:800\n\t"
    "v_mfma_f32_16x16x32_bf16 v[248:251], a[188:191], a[52:55], v[248:251]\n\t"
    "s_add_u32 s65, s65, s72\n\t"
    "ds_read_b64_tr_b16 a[144:145], v117 offset:0\n\t"
    "v_mfma_f32_16x16x32_bf16 v[244:247], a[184:187], a[40:43], v[244:247]\n\t"
    "ds_read_b64_tr_b16 a[146:147], v117 offset:256\n\t"
    "v_mfma_f32_16x16x32_bf16 v[244:247], a[188:191], a[44:47], v[244:247]\n\t"
    "buffer_load_dwordx4 a[28:31], v6, s[40:43], s66 offen offset:3072 sc1\n\t"
    "v_mfma_f32_16x16x32_bf16 v[240:243], a[184:187], a[32:35], v[240:243]\n\t"
    "s_add_u32 s66, s66, 524288\n\t"
    "ds_read_b64_tr_b16 a[148:149], v117 offset:512\n\t"
    "v_mfma_f32_16x16x32_bf16 v[240:243], a[188:191], a[36:39], v[240:243]\n\t"
    "ds_read_b64_tr_b16 a[150:151], v117 offset:768\n\t"
    "s_waitcnt lgkmcnt(8)\n\t"
    "v_mfma_f32_16x16x32_bf16 v[128:131], a[128:131], a[64:67], v[128:131]\n\t"
    "s_add_u32 m0, s68, 33792\n\t"
    "ds_read_b64_tr_b16 a[152:153], v117 offset:32\n\t"
    "v_mfma_f32_16x16x32_bf16 v[128:131], a[132:135], a[68:71], v[128:131]\n\t"
    "buffer_load_dwordx4 v2, s[44:47], s64 offen lds\n\t"
    "v_mfma_f32_16x16x32_bf16 v[132:135], a[128:131], a[72:75], v[132:135]\n\t"
    "ds_read_b64_tr_b16 a[154:155], v117 offset:288\n\t"
    "v_mfma_f32_16x16x32_bf16 v[132:135], a[132:135], a[76:79], v[132:135]\n\t"
    "v_mfma_f32_16x16x32_bf16 v[136:139], a[128:131], a[80:83], v[136:139]\n\t"
    "ds_read_b64_tr_b16 a[156:157], v117 offset:544\n\t"
    "v_mfma_f32_16x16x32_bf16 v[136:139], a[132:135], a[84:87], v[136:139]\n\t"
    "buffer_load_dwordx4 a[32:35], v18, s[40:43], s66 offen offset:0 sc1\n\t"
    "v_mfma_f32_16x16x32_bf16 v[140:143], a[128:131], a[88:91], v[140:143]\n\t"
    "ds_read_b64_tr_b16 a[158:159], v117 offset:800\n\t"
    "v_mfma_f32_16x16x32_bf16 v[140:143], a[132:135], a[92:95], v[140:143]\n\t"
    "s_waitcnt lgkmcnt(8)\n\t"
    "v_mfma_f32_16x16x32_bf16 v[156:159], a[136:139], a[88:91], v[156:159]\n\t"
    "s_add_u32 m0, s69, 33792\n\t"
    "ds_read_b64_tr_b16 a[160:161], v118 offset:0\n\t"
    "v_mfma_f32_16x16x32_bf16 v[156:159], a[140:143], a[92:95], v[156:159]\n\t"
    "buffer_load_dwordx4 v3, s[44:47], s64 offen lds\n\t"
    "v_mfma_f32_16x16x32_bf16 v[152:155], a[136:139], a[80:83], v[152:155]\n\t"
    "ds_read_b64_tr_b16 a[162:163], v118 offset:256\n\t"
    "v_mfma_f32_16x16x32_bf16 v[152:155], a[140:143], a[84:87], v[152:155]\n\t"
    "v_mfma_f32_16x16x32_bf16 v[148:151], a[136:139], a[72:75], v[148:151]\n\t"
    "ds_read_b64_tr_b16 a[164:165], v118 offset:512\n\t"
    "v_mfma_f32_16x16x32_bf16 v[148:151], a[140:143], a[76:79], v[148:151]\n\t"
    "buffer_load_dwordx4 a[36:39], v6, s[40:43], s66 offen offset:0 sc1\n\t"
    "v_mfma_f32_16x16x32_bf16 v[144:147], a[136:139], a[64:67], v[144:147]\n\t"
    "ds_read_b64_tr_b16 a[166:167], v118 offset:768\n\t"
    "v_mfma_f32_16x16x32_bf16 v[144:147], a[140:143], a[68:71], v[144:147]\n\t"
    "s_waitcnt lgkmcnt(8)\n\t"
    "v_mfma_f32_16x16x32_bf16 v[160:163], a[144:147], a[64:67], v[160:163]\n\t"
    "s_add_u32 m0, s70, 33792\n\t"
    "ds_read_b64_tr_b16 a[168:169], v118 offset:32\n\t"
    "v_mfma_f32_16x16x32_bf16 v[160:163], a[148:151], a[68:71], v[160:163]\n\t"
    "buffer_load_dwordx4 v4, s[44:47], s64 offen lds\n\t"
    "v_mfma_f32_16x16x32_bf16 v[164:167], a[144:147], a[72:75], v[164:167]\n\t"
    "ds_read_b64_tr_b16 a[170:171], v118 offset:288\n\t"
    "v_mfma_f32_16x16x32_bf16 v[164:167], a[148:151], a[76:79], v[164:167]\n\t"
    "v_mfma_f32_16x16x32_bf16 v[168:171], a[144:147], a[80:83], v[168:171]\n\t"
    "ds_read_b64_tr_b16 a[172:173], v118 offset:544\n\t"
    "v_mfma_f32_16x16x32_bf16 v[168:171], a[148:151], a[84:87], v[168:171]\n\t"
    "buffer_load_dwordx4 a[40:43], v18, s[40:43], s66 offen offset:1024 sc1\n\t"
    "v_mfma_f32_16x16x32_bf16 v[172:175], a[144:147], a[88:91], v[172:175]\n\t"
    "ds_read_b64_tr_b16 a[174:175], v118 offset:800\n\t"
    "v_mfma_f32_16x16x32_bf16 v[172:175], a[148:151], a[92:95], v[172:175]\n\t"
    "s_waitcnt lgkmcnt(8)\n\t"
    "v_mfma_f32_16x16x32_bf16 v[188:191], a[152:155], a[88:91], v[188:191]\n\t"
    "s_add_u32 m0, s71, 33792\n\t"
    "ds_read_b64_tr_b16 a[176:177], v119 offset:0\n\t"
    "v_mfma_f32_16x16x32_bf16 v[188:191], a[156:159], a[92:95], v[188:191]\n\t"
    "buffer_load_dwordx4 v5, s[44:47], s64 offen lds\n\t"
    "v_mfma_f32_16x16x32_bf16 v[184:187], a[152:155], a[80:83], v[184:187]\n\t"
    "ds_read_b64_tr_b16 a[178:179], v119 offset:256\n\t"
    "v_mfma_f32_16x16x32_bf16 v[184:187], a[156:159], a[84:87], v[184:187]\n\t"
    "v_mfma_f32_16x16x32_bf16 v[180:183], a[152:155], a[72:75], v[180:183]\n\t"
    "ds_read_b64_tr_b16 a[180:181], v119 offset:512\n\t"
    "v_mfma_f32_16x16x32_bf16 v[180:183], a[156:159], a[76:79], v[180:183]\n\t"
    "buffer_load_dwordx4 a[44:47], v6, s[40:43], s66 offen offset:1024 sc1\n\t"
    "v_mfma_f32_16x16x32_bf16 v[176:179], a[152:155], a[64:67], v[176:179]\n\t"
    "ds_read_b64_tr_b16 a[182:183], v119 offset:768\n\t"
    "v_mfma_f32_16x16x32_bf16 v[176:179], a[156:159], a[68:71], v[176:179]\n\t"
    "s_waitcnt lgkmcnt(8)\n\t"
    "v_mfma_f32_16x16x32_bf16 v[192:195], a[160:163], a[64:67], v[192:195]\n\t"
    "s_add_u32 m0, s68, 50688\n\t"
    "ds_read_b64_tr_b16 a[184:185], v119 offset:32\n\t"
    "v_mfma_f32_16x16x32_bf16 v[192:195], a[164:167], a[68:71], v[192:195]\n\t"
    "buffer_load_dwordx4 v2, s[44:47], s65 offen lds\n\t"
    "v_mfma_f32_16x16x32_bf16 v[196:199], a[160:163], a[72:75], v[196:199]\n\t"
    "ds_read_b64_tr_b16 a[186:187], v119 offset:288\n\t"
    "v_mfma_f32_16x16x32_bf16 v[196:199], a[164:167], a[76:79], v[196:199]\n\t"
    "v_mfma_f32_16x16x32_bf16 v[200:203], a[160:163], a[80:83], v[200:203]\n\t"
    "ds_read_b64_tr_b16 a[188:189], v119 offset:544\n\t"
    "v_mfma_f32_16x16x32_bf16 v[200:203], a[164:167], a[84:87], v[200:203]\n\t"
    "buffer_load_dwordx4 a[48:51], v18, s[40:43], s66 offen offset:2048 sc1\n\t"
    "v_mfma_f32_16x16x32_bf16 v[204:207], a[160:163], a[88:91], v[204:207]\n\t"
    "ds_read_b64_tr_b16 a[190:191], v119 offset:800\n\t"
    "v_mfma_f32_16x16x32_bf16 v[204:207], a[164:167], a[92:95], v[204:207]\n\t"
    "s_waitcnt lgkmcnt(8)\n\t"
    "v_mfma_f32_16x16x32_bf16 v[220:223], a[168:171], a[88:91], v[220:223]\n\t"
    "s_add_u32 m0, s69, 50688\n\t"
    "v_mfma_f32_16x16x32_bf16 v[220:223], a[172:175], a[92:95], v[220:223]\n\t"
    "buffer_load_dwordx4 v3, s[44:47], s65 offen lds\n\t"
    "v_mfma_f32_16x16x32_bf16 v[216:219], a[168:171], a[80:83], v[216:219]\n\t"
    "v_mfma_f32_16x16x32_bf16 v[216:219], a[172:175], a[84:87], v[216:219]\n\t"
    "v_mfma_f32_16x16x32_bf16 v[212:215], a[168:171], a[72:75], v[212:215]\n\t"
    "v_mfma_f32_16x16x32_bf16 v[212:215], a[172:175], a[76:79], v[212:215]\n\t"
    "buffer_load_dwordx4 a[52:55], v6, s[40:43], s66 offen offset:2048 sc1\n\t"
    "v_mfma_f32_16x16x32_bf16 v[208:211], a[168:171], a[64:67], v[208:211]\n\t"
    "v_mfma_f32_16x16x32_bf16 v[208:211], a[172:175], a[68:71], v[208:211]\n\t"
    "s_waitcnt vmcnt(28) lgkmcnt(0)\n\t"
    "s_barrier\n\t"
    "v_mfma_f32_16x16x32_bf16 v[224:227], a[176:179], a[64:67], v[224:227]\n\t"
    "s_add_u32 m0, s70, 50688\n\t"
    "ds_read_b64_tr_b16 a[128:129], v116 offset:33792\n\t"
    "v_mfma_f32_16x16x32_bf16 v[224:227], a[180:183], a[68:71], v[224:227]\n\t"
    "buffer_load_dwordx4 v4, s[44:47], s65 offen lds\n\t"
    "v_mfma_f32_16x16x32_bf16 v[228:231], a[176:179], a[72:75], v[228:231]\n\t"
    "buffer_store_dwordx2 v[104:105], v106, s[16:19], s94 offen sc1\n\t"
    "ds_read_b64_tr_b16 a[130:131], v116 offset:34048\n\t"
    "v_mfma_f32_16x16x32_bf16 v[228:231], a[180:183], a[76:79], v[228:231]\n\t"
    "ds_read_b64_tr_b16 a[132:133], v116 offset:34304\n\t"
    "v_mfma_f32_16x16x32_bf16 v[232:235], a[176:179], a[80:83], v[232:235]\n\t"
    "ds_read_b64_tr_b16 a[134:135], v116 offset:34560\n\t"
    "v_bfrev_b32_e32 v106, 1\n\t"
    "v_mfma_f32_16x16x32_bf16 v[232:235], a[180:183], a[84:87], v[232:235]\n\t"
    "buffer_load_dwordx4 a[56:59], v18, s[40:43], s66 offen offset:3072 sc1\n\t"
    "v_mfma_f32_16x16x32_bf16 v[236:239], a[176:179], a[88:91], v[236:239]\n\t"
    "ds_read_b64_tr_b16 a[136:137], v116 offset:33824\n\t"
    "v_mfma_f32_16x16x32_bf16 v[236:239], a[180:183], a[92:95], v[236:239]\n\t"
    "ds_read_b64_tr_b16 a[138:139], v116 offset:34080\n\t"
    "v_mfma_f32_16x16x32_bf16 v[252:255], a[184:187], a[88:91], v[252:255]\n\t"
    "s_add_u32 m0, s71, 50688\n\t"
    "ds_read_b64_tr_b16 a[140:141], v116 offset:34336\n\t"
    "v_mfma_f32_16x16x32_bf16 v[252:255], a[188:191], a[92:95], v[252:255]\n\t"
    "buffer_load_dwordx4 v5, s[44:47], s65 offen lds\n\t"
    "v_mfma_f32_16x16x32_bf16 v[248:251], a[184:187], a[80:83], v[248:251]\n\t"
    "s_add_u32 s64, s64, s72\n\t"
    "ds_read_b64_tr_b16 a[142:143], v116 offset:34592\n\t"
    "v_mfma_f32_16x16x32_bf16 v[248:251], a[188:191], a[84:87], v[248:251]\n\t"
    "s_add_u32 s65, s65, s72\n\t"
    "ds_read_b64_tr_b16 a[144:145], v117 offset:33792\n\t"
    "v_mfma_f32_16x16x32_bf16 v[244:247], a[184:187], a[72:75], v[244:247]\n\t"
    "ds_read_b64_tr_b16 a[146:147], v117 offset:34048\n\t"
    "v_mfma_f32_16x16x32_bf16 v[244:247], a[188:191], a[76:79], v[244:247]\n\t"
    "buffer_load_dwordx4 a[60:63], v6, s[40:43], s66 offen offset:3072 sc1\n\t"
    "v_mfma_f32_16x16x32_bf16 v[240:243], a[184:187], a[64:67], v[240:243]\n\t"
    "s_add_u32 s66, s66, 524288\n\t"
    "ds_read_b64_tr_b16 a[148:149], v117 offset:34304\n\t"
    "v_mfma_f32_16x16x32_bf16 v[240:243], a[188:191], a[68:71], v[240:243]\n\t"
    "ds_read_b64_tr_b16 a[150:151], v117 offset:34560\n\t"
    "s_waitcnt lgkmcnt(8)\n\t"
    "v_mfma_f32_16x16x32_bf16 v[128:131], a[128:131], a[96:99], v[128:131]\n\t"
    "s_add_u32 m0, s68, 67584\n\t"
    "ds_read_b64_tr_b16 a[152:153], v117 offset:33824\n\t"
    "v_mfma_f32_16x16x32_bf16 v[128:131], a[132:135], a[100:103], v[128:131]\n\t"
    "buffer_load_dwordx4 v2, s[44:47], s64 offen lds\n\t"
    "v_mfma_f32_16x16x32_bf16 v[132:135], a[128:131], a[104:107], v[132:135]\n\t"
    "ds_read_b64_tr_b16 a[154:155], v117 offset:34080\n\t"
    "v_mfma_f32_16x16x32_bf16 v[132:135], a[132:135], a[108:111], v[132:135]\n\t"
    "buffer_load_dwordx2 v[12:13], v14, s[16:19], s95 offen sc1\n\t"
    "v_mfma_f32_16x16x32_bf16 v[136:139], a[128:131], a[112:115], v[136:139]\n\t"
    "ds_read_b64_tr_b16 a[156:157], v117 offset:34336\n\t"
    "v_mfma_f32_16x16x32_bf16 v[136:139], a[132:135], a[116:119], v[136:139]\n\t"
    "buffer_load_dwordx4 a[64:67], v18, s[40:43], s66 offen offset:0 sc1\n\t"
    "v_mfma_f32_16x16x32_bf16 v[140:143], a[128:131], a[120:123], v[140:143]\n\t"
    "ds_read_b64_tr_b16 a[158:159], v117 offset:34592\n\t"
    "v_mfma_f32_16x16x32_bf16 v[140:143], a[132:135], a[124:127], v[140:143]\n\t"
    "s_waitcnt lgkmcnt(8)\n\t"
    "v_mfma_f32_16x16x32_bf16 v[156:159], a[136:139], a[120:123], v[156:159]\n\t"
    "s_add_u32 m0, s69, 67584\n\t"
    "ds_read_b64_tr_b16 a[160:161], v118 offset:33792\n\t"
    "v_mfma_f32_16x16x32_bf16 v[156:159], a[140:143], a[124:127], v[156:159]\n\t"
    "buffer_load_dwordx4 v3, s[44:47], s64 offen lds\n\t"
    "v_mfma_f32_16x16x32_bf16 v[152:155], a[136:139], a[112:115], v[152:155]\n\t"
    "ds_read_b64_tr_b16 a[162:163], v118 offset:34048\n\t"
    "v_mfma_f32_16x16x32_bf16 v[152:155], a[140:143], a[116:119], v[152:155]\n\t"
    "v_mfma_f32_16x16x32_bf16 v[148:151], a[136:139], a[104:107], v[148:151]\n\t"
    "ds_read_b64_tr_b16 a[164:165], v118 offset:34304\n\t"
    "v_mfma_f32_16x16x32_bf16 v[148:151], a[140:143], a[108:111], v[148:151]\n\t"
    "buffer_load_dwordx4 a[68:71], v6, s[40:43], s66 offen offset:0 sc1\n\t"
    "v_mfma_f32_16x16x32_bf16 v[144:147], a[136:139], a[96:99], v[144:147]\n\t"
    "ds_read_b64_tr_b16 a[166:167], v118 offset:34560\n\t"
    "v_mfma_f32_16x16x32_bf16 v[144:147], a[140:143], a[100:103], v[144:147]\n\t"
    "s_waitcnt lgkmcnt(8)\n\t"
    "v_mfma_f32_16x16x32_bf16 v[160:163], a[144:147], a[96:99], v[160:163]\n\t"
    "s_add_u32 m0, s70, 67584\n\t"
    "ds_read_b64_tr_b16 a[168:169], v118 offset:33824\n\t"
    "v_mfma_f32_16x16x32_bf16 v[160:163], a[148:151], a[100:103], v[160:163]\n\t"
    "buffer_load_dwordx4 v4, s[44:47], s64 offen lds\n\t"
    "v_mfma_f32_16x16x32_bf16 v[164:167], a[144:147], a[104:107], v[164:167]\n\t"
    "ds_read_b64_tr_b16 a[170:171], v118 offset:34080\n\t"
    "v_mfma_f32_16x16x32_bf16 v[164:167], a[148:151], a[108:111], v[164:167]\n\t"
    "v_mfma_f32_16x16x32_bf16 v[168:171], a[144:147], a[112:115], v[168:171]\n\t"
    "ds_read_b64_tr_b16 a[172:173], v118 offset:34336\n\t"
    "v_mfma_f32_16x16x32_bf16 v[168:171], a[148:151], a[116:119], v[168:171]\n\t"
    "buffer_load_dwordx4 a[72:75], v18, s[40:43], s66 offen offset:1024 sc1\n\t"
    "v_mfma_f32_16x16x32_bf16 v[172:175], a[144:147], a[120:123], v[172:175]\n\t"
    "ds_read_b64_tr_b16 a[174:175], v118 offset:34592\n\t"
    "v_mfma_f32_16x16x32_bf16 v[172:175], a[148:151], a[124:127], v[172:175]\n\t"
    "s_waitcnt lgkmcnt(8)\n\t"
    "v_mfma_f32_16x16x32_bf16 v[188:191], a[152:155], a[120:123], v[188:191]\n\t"
    "s_add_u32 m0, s71, 67584\n\t"
    "ds_read_b64_tr_b16 a[176:177], v119 offset:33792\n\t"
    "v_mfma_f32_16x16x32_bf16 v[188:191], a[156:159], a[124:127], v[188:191]\n\t"
    "buffer_load_dwordx4 v5, s[44:47], s64 offen lds\n\t"
    "v_mfma_f32_16x16x32_bf16 v[184:187], a[152:155], a[112:115], v[184:187]\n\t"
    "ds_read_b64_tr_b16 a[178:179], v119 offset:34048\n\t"
    "v_mfma_f32_16x16x32_bf16 v[184:187], a[156:159], a[116:119], v[184:187]\n\t"
    "v_mfma_f32_16x16x32_bf16 v[180:183], a[152:155], a[104:107], v[180:183]\n\t"
    "ds_read_b64_tr_b16 a[180:181], v119 offset:34304\n\t"
    "v_mfma_f32_16x16x32_bf16 v[180:183], a[156:159], a[108:111], v[180:183]\n\t"
    "buffer_load_dwordx4 a[76:79], v6, s[40:43], s66 offen offset:1024 sc1\n\t"
    "v_mfma_f32_16x16x32_bf16 v[176:179], a[152:155], a[96:99], v[176:179]\n\t"
    "ds_read_b64_tr_b16 a[182:183], v119 offset:34560\n\t"
    "v_mfma_f32_16x16x32_bf16 v[176:179], a[156:159], a[100:103], v[176:179]\n\t"
    "s_waitcnt lgkmcnt(8)\n\t"
    "v_mfma_f32_16x16x32_bf16 v[192:195], a[160:163], a[96:99], v[192:195]\n\t"
    "s_add_u32 m0, s68, 84480\n\t"
    "ds_read_b64_tr_b16 a[184:185], v119 offset:33824\n\t"
    "v_mfma_f32_16x16x32_bf16 v[192:195], a[164:167], a[100:103], v[192:195]\n\t"
    "buffer_load_dwordx4 v2, s[44:47], s65 offen lds\n\t"
    "v_mfma_f32_16x16x32_bf16 v[196:199], a[160:163], a[104:107], v[196:199]\n\t"
    "ds_read_b64_tr_b16 a[186:187], v119 offset:34080\n\t"
    "v_mfma_f32_16x16x32_bf16 v[196:199], a[164:167], a[108:111], v[196:199]\n\t"
    "v_mfma_f32_16x16x32_bf16 v[200:203], a[160:163], a[112:115], v[200:203]\n\t"
    "ds_read_b64_tr_b16 a[188:189], v119 offset:34336\n\t"
    "v_mfma_f32_16x16x32_bf16 v[200:203], a[164:167], a[116:119], v[200:203]\n\t"
    "buffer_load_dwordx4 a[80:83], v18, s[40:43], s66 offen offset:2048 sc1\n\t"
    "v_mfma_f32_16x16x32_bf16 v[204:207], a[160:163], a[120:123], v[204:207]\n\t"
    "ds_read_b64_tr_b16 a[190:191], v119 offset:34592\n\t"
    "v_mfma_f32_16x16x32_bf16 v[204:207], a[164:167], a[124:127], v[204:207]\n\t"
    "s_waitcnt lgkmcnt(8)\n\t"
    "v_mfma_f32_16x16x32_bf16 v[220:223], a[168:171], a[120:123], v[220:223]\n\t"
    "s_add_u32 m0, s69, 84480\n\t"
    "v_mfma_f32_16x16x32_bf16 v[220:223], a[172:175], a[124:127], v[220:223]\n\t"
    "buffer_load_dwordx4 v3, s[44:47], s65 offen lds\n\t"
    "v_mfma_f32_16x16x32_bf16 v[216:219], a[168:171], a[112:115], v[216:219]\n\t"
    "v_mfma_f32_16x16x32_bf16 v[216:219], a[172:175], a[116:119], v[216:219]\n\t"
    "v_mfma_f32_16x16x32_bf16 v[212:215], a[168:171], a[104:107], v[212:215]\n\t"
    "v_mfma_f32_16x16x32_bf16 v[212:215], a[172:175], a[108:111], v[212:215]\n\t"
    "buffer_load_dwordx4 a[84:87], v6, s[40:43], s66 offen offset:2048 sc1\n\t"
    "v_mfma_f32_16x16x32_bf16 v[208:211], a[168:171], a[96:99], v[208:211]\n\t"
    "v_mfma_f32_16x16x32_bf16 v[208:211], a[172:175], a[100:103], v[208:211]\n\t"
    "s_waitcnt vmcnt(29) lgkmcnt(0)\n\t"
    "s_barrier\n\t"
    "v_mfma_f32_16x16x32_bf16 v[224:227], a[176:179], a[96:99], v[224:227]\n\t"
    "s_add_u32 m0, s70, 84480\n\t"
    "ds_read_b64_tr_b16 a[128:129], v112 offset:0\n\t"
    "v_mfma_f32_16x16x32_bf16 v[224:227], a[180:183], a[100:103], v[224:227]\n\t"
    "buffer_load_dwordx4 v4, s[44:47], s65 offen lds\n\t"
    "v_mfma_f32_16x16x32_bf16 v[228:231], a[176:179], a[104:107], v[228:231]\n\t"
    "ds_read_b64_tr_b16 a[130:131], v112 offset:256\n\t"
    "v_mfma_f32_16x16x32_bf16 v[228:231], a[180:183], a[108:111], v[228:231]\n\t"
    "ds_read_b64_tr_b16 a[132:133], v112 offset:512\n\t"
    "v_mfma_f32_16x16x32_bf16 v[232:235], a[176:179], a[112:115], v[232:235]\n\t"
    "ds_read_b64_tr_b16 a[134:135], v112 offset:768\n\t"
    "v_mfma_f32_16x16x32_bf16 v[232:235], a[180:183], a[116:119], v[232:235]\n\t"
    "buffer_load_dwordx4 a[88:91], v18, s[40:43], s66 offen offset:3072 sc1\n\t"
    "v_mfma_f32_16x16x32_bf16 v[236:239], a[176:179], a[120:123], v[236:239]\n\t"
    "ds_read_b64_tr_b16 a[136:137], v112 offset:32\n\t"
    "v_mfma_f32_16x16x32_bf16 v[236:239], a[180:183], a[124:127], v[236:239]\n\t"
    "ds_read_b64_tr_b16 a[138:139], v112 offset:288\n\t"
    "v_mfma_f32_16x16x32_bf16 v[252:255], a[184:187], a[120:123], v[252:255]\n\t"
    "s_add_u32 m0, s71, 84480\n\t"
    "ds_read_b64_tr_b16 a[140:141], v112 offset:544\n\t"
    "v_mfma_f32_16x16x32_bf16 v[252:255], a[188:191], a[124:127], v[252:255]\n\t"
    "buffer_load_dwordx4 v5, s[44:47], s65 offen lds\n\t"
    "v_mfma_f32_16x16x32_bf16 v[248:251], a[184:187], a[112:115], v[248:251]\n\t"
    "s_add_u32 s64, s64, s72\n\t"
    "ds_read_b64_tr_b16 a[142:143], v112 offset:800\n\t"
    "v_mfma_f32_16x16x32_bf16 v[248:251], a[188:191], a[116:119], v[248:251]\n\t"
    "s_add_u32 s65, s65, s72\n\t"
    "ds_read_b64_tr_b16 a[144:145], v113 offset:0\n\t"
    "v_mfma_f32_16x16x32_bf16 v[244:247], a[184:187], a[104:107], v[244:247]\n\t"
    "ds_read_b64_tr_b16 a[146:147], v113 offset:256\n\t"
    "v_mfma_f32_16x16x32_bf16 v[244:247], a[188:191], a[108:111], v[244:247]\n\t"
    "buffer_load_dwordx4 a[92:95], v6, s[40:43], s66 offen offset:3072 sc1\n\t"
    "v_mfma_f32_16x16x32_bf16 v[240:243], a[184:187], a[96:99], v[240:243]\n\t"
    "s_add_u32 s66, s66, 524288\n\t"
    "ds_read_b64_tr_b16 a[148:149], v113 offset:512\n\t"
    "v_mfma_f32_16x16x32_bf16 v[240:243], a[188:191], a[100:103], v[240:243]\n\t"
    "ds_read_b64_tr_b16 a[150:151], v113 offset:768\n\t"
    "s_add_u32 s74, s74, 1\n\t"
    "s_cmp_lg_u32 s74, s76\n\t"
    "s_cbranch_scc1 L_loopW_%=\n\t"
    "s_waitcnt lgkmcnt(0)\n\t"
    "ds_read_b128 a[128:131], v64 offset:0\n\t"
    "ds_read_b128 a[132:135], v65 offset:0\n\t"
    "ds_read_b128 a[136:139], v64 offset:2048\n\t"
    "ds_read_b128 a[140:143], v65 offset:2048\n\t"
    "ds_read_b128 a[144:147], v66 offset:0\n\t"
    "ds_read_b128 a[148:151], v67 offset:0\n\t"
    "L_loopC_%=:\n\t"
    "s_waitcnt lgkmcnt(4)\n\t"
    "v_mfma_f32_16x16x32_bf16 v[128:131], a[128:131], a[0:3], v[128:131]\n\t"
    "s_add_u32 m0, s68, 101376\n\t"
    "ds_read_b128 a[152:155], v66 offset:2048\n\t"
    "v_mfma_f32_16x16x32_bf16 v[128:131], a[132:135], a[4:7], v[128:131]\n\t"
    "buffer_load_dwordx4 v2, s[44:47], s64 offen lds\n\t"
    "v_mfma_f32_16x16x32_bf16 v[132:135], a[128:131], a[8:11], v[132:135]\n\t"
    "v_mfma_f32_16x16x32_bf16 v[132:135], a[132:135], a[12:15], v[132:135]\n\t"
    "v_mfma_f32_16x16x32_bf16 v[136:139], a[128:131], a[16:19], v[136:139]\n\t"
    "ds_read_b128 a[156:159], v67 offset:2048\n\t"
    "v_mfma_f32_16x16x32_bf16 v[136:139], a[132:135], a[20:23], v[136:139]\n\t"
    "buffer_load_dwordx4 a[96:99], v18, s[40:43], s66 offen offset:0 sc1\n\t"
    "v_mfma_f32_16x16x32_bf16 v[140:143], a[128:131], a[24:27], v[140:143]\n\t"
    "v_mfma_f32_16x16x32_bf16 v[140:143], a[132:135], a[28:31], v[140:143]\n\t"
    "s_waitcnt lgkmcnt(4)\n\t"
    "v_mfma_f32_16x16x32_bf16 v[156:159], a[136:139], a[24:27], v[156:159]\n\t"
    "s_add_u32 m0, s69, 101376\n\t"
    "ds_read_b128 a[160:163], v68 offset:0\n\t"
    "v_mfma_f32_16x16x32_bf16 v[156:159], a[140:143], a[28:31], v[156:159]\n\t"
    "buffer_load_dwordx4 v3, s[44:47], s64 offen lds\n\t"
    "v_mfma_f32_16x16x32_bf16 v[152:155], a[136:139], a[16:19], v[152:155]\n\t"
    "v_mfma_f32_16x16x32_bf16 v[152:155], a[140:143], a[20:23], v[152:155]\n\t"
    "v_mfma_f32_16x16x32_bf16 v[148:151], a[136:139], a[8:11], v[148:151]\n\t"
    "ds_read_b128 a[164:167], v69 offset:0\n\t"
    "v_mfma_f32_16x16x32_bf16 v[148:151], a[140:143], a[12:15], v[148:151]\n\t"
    "buffer_load_dwordx4 a[100:103], v6, s[40:43], s66 offen offset:0 sc1\n\t"
    "v_mfma_f32_16x16x32_bf16 v[144:147], a[136:139], a[0:3], v[144:147]\n\t"
    "v_mfma_f32_16x16x32_bf16 v[144:147], a[140:143], a[4:7], v[144:147]\n\t"
    "s_waitcnt lgkmcnt(4)\n\t"
    "v_mfma_f32_16x16x32_bf16 v[160:163], a[144:147], a[0:3], v[160:163]\n\t"
    "s_add_u32 m0, s70, 101376\n\t"
    "ds_read_b128 a[168:171], v68 offset:2048\n\t"
    "v_mfma_f32_16x16x32_bf16 v[160:163], a[148:151], a[4:7], v[160:163]\n\t"
    "buffer_load_dwordx4 v4, s[44:47], s64 offen lds\n\t"
    "v_mfma_f32_16x16x32_bf16 v[164:167], a[144:147], a[8:11], v[164:167]\n\t"
    "v_mfma_f32_16x16x32_bf16 v[164:167], a[148:151], a[12:15], v[164:167]\n\t"
    "v_mfma_f32_16x16x32_bf16 v[168:171], a[144:147], a[16:19], v[168:171]\n\t"
    "ds_read_b128 a[172:175], v69 offset:2048\n\t"
    "v_mfma_f32_16x16x32_bf16 v[168:171], a[148:151], a[20:23], v[168:171]\n\t"
    "buffer_load_dwordx4 a[104:107], v18, s[40:43], s66 offen offset:1024 sc1\n\t"
    "v_mfma_f32_16x16x32_bf16 v[172:175], a[144:147], a[24:27], v[172:175]\n\t"
    "v_mfma_f32_16x16x32_bf16 v[172:175], a[148:151], a[28:31], v[172:175]\n\t"
    "s_waitcnt lgkmcnt(4)\n\t"
    "v_mfma_f32_16x16x32_bf16 v[188:191], a[152:155], a[24:27], v[188:191]\n\t"
    "s_add_u32 m0, s71, 101376\n\t"
    "ds_read_b128 a[176:179], v70 offset:0\n\t"
    "v_mfma_f32_16x16x32_bf16 v[188:191], a[156:159], a[28:31], v[188:191]\n\t"
    "buffer_load_dwordx4 v5, s[44:47], s64 offen lds\n\t"
    "v_mfma_f32_16x16x32_bf16 v[184:187], a[152:155], a[16:19], v[184:187]\n\t"
    "v_mfma_f32_16x16x32_bf16 v[184:187], a[156:159], a[20:23], v[184:187]\n\t"
    "v_mfma_f32_16x16x32_bf16 v[180:183], a[152:155], a[8:11], v[180:183]\n\t"
    "ds_read_b128 a[180:183], v71 offset:0\n\t"
    "v_mfma_f32_16x16x32_bf16 v[180:183], a[156:159], a[12:15], v[180:183]\n\t"
    "buffer_load_dwordx4 a[108:111], v6, s[40:43], s66 offen offset:1024 sc1\n\t"
    "v_mfma_f32_16x16x32_bf16 v[176:179], a[152:155], a[0:3], v[176:179]\n\t"
    "v_mfma_f32_16x16x32_bf16 v[176:179], a[156:159], a[4:7], v[176:179]\n\t"
    "s_waitcnt lgkmcnt(4)\n\t"
    "v_mfma_f32_16x16x32_bf16 v[192:195], a[160:163], a[0:3], v[192:195]\n\t"
    "s_add_u32 m0, s68, 118272\n\t"
    "ds_read_b128 a[184:187], v70 offset:2048\n\t"
    "v_mfma_f32_16x16x32_bf16 v[192:195], a[164:167], a[4:7], v[192:195]\n\t"
    "buffer_load_dwordx4 v2, s[44:47], s65 offen lds\n\t"
    "v_mfma_f32_16x16x32_bf16 v[196:199], a[160:163], a[8:11], v[196:199]\n\t"
    "v_mfma_f32_16x16x32_bf16 v[196:199], a[164:167], a[12:15], v[196:199]\n\t"
    "v_mfma_f32_16x16x32_bf16 v[200:203], a[160:163], a[16:19], v[200:203]\n\t"
    "ds_read_b128 a[188:191], v71 offset:2048\n\t"
    "v_mfma_f32_16x16x32_bf16 v[200:203], a[164:167], a[20:23], v[200:203]\n\t"
    "buffer_load_dwordx4 a[112:115], v18, s[40:43], s66 offen offset:2048 sc1\n\t"
    "v_mfma_f32_16x16x32_bf16 v[204:207], a[160:163], a[24:27], v[204:207]\n\t"
    "v_mfma_f32_16x16x32_bf16 v[204:207], a[164:167], a[28:31], v[204:207]\n\t"
    "s_waitcnt lgkmcnt(4)\n\t"
    "v_mfma_f32_16x16x32_bf16 v[220:223], a[168:171], a[24:27], v[220:223]\n\t"
    "s_add_u32 m0, s69, 118272\n\t"
    "v_mfma_f32_16x16x32_bf16 v[220:223], a[172:175], a[28:31], v[220:223]\n\t"
    "buffer_load_dwordx4 v3, s[44:47], s65 offen lds\n\t"
    "v_mfma_f32_16x16x32_bf16 v[216:219], a[168:171], a[16:19], v[216:219]\n\t"
    "v_mfma_f32_16x16x32_bf16 v[216:219], a[172:175], a[20:23], v[216:219]\n\t"
    "v_mfma_f32_16x16x32_bf16 v[212:215], a[168:171], a[8:11], v[212:215]\n\t"
    "v_mfma_f32_16x16x32_bf16 v[212:215], a[172:175], a[12:15], v[212:215]\n\t"
    "buffer_load_dwordx4 a[116:119], v6, s[40:43], s66 offen offset:2048 sc1\n\t"
    "v_mfma_f32_16x16x32_bf16 v[208:211], a[168:171], a[0:3], v[208:211]\n\t"
    "v_mfma_f32_16x16x32_bf16 v[208:211], a[172:175], a[4:7], v[208:211]\n\t"
    "s_waitcnt vmcnt(28) lgkmcnt(0)\n\t"
    "s_barrier\n\t"
    "v_mfma_f32_16x16x32_bf16 v[224:227], a[176:179], a[0:3], v[224:227]\n\t"
    "s_add_u32 m0, s70, 118272\n\t"
    "ds_read_b128 a[128:131], v64 offset:33792\n\t"
    "v_mfma_f32_16x16x32_bf16 v[224:227], a[180:183], a[4:7], v[224:227]\n\t"
    "buffer_load_dwordx4 v4, s[44:47], s65 offen lds\n\t"
    "v_mfma_f32_16x16x32_bf16 v[228:231], a[176:179], a[8:11], v[228:231]\n\t"
    "v_mfma_f32_16x16x32_bf16 v[228:231], a[180:183], a[12:15], v[228:231]\n\t"
    "ds_read_b128 a[132:135], v65 offset:33792\n\t"
    "v_mfma_f32_16x16x32_bf16 v[232:235], a[176:179], a[16:19], v[232:235]\n\t"
    "v_mfma_f32_16x16x32_bf16 v[232:235], a[180:183], a[20:23], v[232:235]\n\t"
    "buffer_load_dwordx4 a[120:123], v18, s[40:43], s66 offen offset:3072 sc1\n\t"
    "v_mfma_f32_16x16x32_bf16 v[236:239], a[176:179], a[24:27], v[236:239]\n\t"
    "ds_read_b128 a[136:139], v64 offset:35840\n\t"
    "v_mfma_f32_16x16x32_bf16 v[236:239], a[180:183], a[28:31], v[236:239]\n\t"
    "v_mfma_f32_16x16x32_bf16 v[252:255], a[184:187], a[24:27], v[252:255]\n\t"
    "s_add_u32 m0, s71, 118272\n\t"
    "ds_read_b128 a[140:143], v65 offset:35840\n\t"
    "v_mfma_f32_16x16x32_bf16 v[252:255], a[188:191], a[28:31], v[252:255]\n\t"
    "buffer_load_dwordx4 v5, s[44:47], s65 offen lds\n\t"
    "v_mfma_f32_16x16x32_bf16 v[248:251], a[184:187], a[16:19], v[248:251]\n\t"
    "s_add_u32 s64, s64, s72\n\t"
    "v_mfma_f32_16x16x32_bf16 v[248:251], a[188:191], a[20:23], v[248:251]\n\t"
    "s_add_u32 s65, s65, s72\n\t"
    "ds_read_b128 a[144:147], v66 offset:33792\n\t"
    "v_mfma_f32_16x16x32_bf16 v[244:247], a[184:187], a[8:11], v[244:247]\n\t"
    "v_mfma_f32_16x16x32_bf16 v[244:247], a[188:191], a[12:15], v[244:247]\n\t"
    "buffer_load_dwordx4 a[124:127], v6, s[40:43], s66 offen offset:3072 sc1\n\t"
    "v_mfma_f32_16x16x32_bf16 v[240:243], a[184:187], a[0:3], v[240:243]\n\t"
    "s_add_u32 s66, s66, 524288\n\t"
    "ds_read_b128 a[148:151], v67 offset:33792\n\t"
    "v_mfma_f32_16x16x32_bf16 v[240:243], a[188:191], a[4:7], v[240:243]\n\t"
    "s_cmp_eq_u32 s74, s75\n\t"
    "s_cselect_b32 s44, s52, s44\n\t"
    "s_cselect_b32 s45, s53, s45\n\t"
    "s_cselect_b32 s64, 0, s64\n\t"
    "s_add_u32 s65, s64, 128\n\t"
    "s_waitcnt lgkmcnt(4)\n\t"
    "v_mfma_f32_16x16x32_bf16 v[128:131], a[128:131], a[32:35], v[128:131]\n\t"
    "s_add_u32 m0, s68, 0\n\t"
    "ds_read_b128 a[152:155], v66 offset:35840\n\t"
    "v_mfma_f32_16x16x32_bf16 v[128:131], a[132:135], a[36:39], v[128:131]\n\t"
    "buffer_load_dwordx4 v2, s[44:47], s64 offen lds\n\t"
    "v_mfma_f32_16x16x32_bf16 v[132:135], a[128:131], a[40:43], v[132:135]\n\t"
    "v_mfma_f32_16x16x32_bf16 v[132:135], a[132:135], a[44:47], v[132:135]\n\t"
    "v_mfma_f32_16x16x32_bf16 v[136:139], a[128:131], a[48:51], v[136:139]\n\t"
    "ds_read_b128 a[156:159], v67 offset:35840\n\t"
    "v_mfma_f32_16x16x32_bf16 v[136:139], a[132:135], a[52:55], v[136:139]\n\t"
    "buffer_load_dwordx4 a[0:3], v18, s[40:43], s66 offen offset:0 sc1\n\t"
    "v_mfma_f32_16x16x32_bf16 v[140:143], a[128:131], a[56:59], v[140:143]\n\t"
    "v_mfma_f32_16x16x32_bf16 v[140:143], a[132:135], a[60:63], v[140:143]\n\t"
    "s_waitcnt lgkmcnt(4)\n\t"
    "v_mfma_f32_16x16x32_bf16 v[156:159], a[136:139], a[56:59], v[156:159]\n\t"
    "s_add_u32 m0, s69, 0\n\t"
    "ds_read_b128 a[160:163], v68 offset:33792\n\t"
    "v_mfma_f32_16x16x32_bf16 v[156:159], a[140:143], a[60:63], v[156:159]\n\t"
    "buffer_load_dwordx4 v3, s[44:47], s64 offen lds\n\t"
    "v_mfma_f32_16x16x32_bf16 v[152:155], a[136:139], a[48:51], v[152:155]\n\t"
    "v_mfma_f32_16x16x32_bf16 v[152:155], a[140:143], a[52:55], v[152:155]\n\t"
    "v_mfma_f32_16x16x32_bf16 v[148:151], a[136:139], a[40:43], v[148:151]\n\t"
    "ds_read_b128 a[164:167], v69 offset:33792\n\t"
    "v_mfma_f32_16x16x32_bf16 v[148:151], a[140:143], a[44:47], v[148:151]\n\t"
    "buffer_load_dwordx4 a[4:7], v6, s[40:43], s66 offen offset:0 sc1\n\t"
    "v_mfma_f32_16x16x32_bf16 v[144:147], a[136:139], a[32:35], v[144:147]\n\t"
    "v_mfma_f32_16x16x32_bf16 v[144:147], a[140:143], a[36:39], v[144:147]\n\t"
    "s_waitcnt lgkmcnt(4)\n\t"
    "v_mfma_f32_16x16x32_bf16 v[160:163], a[144:147], a[32:35], v[160:163]\n\t"
    "s_add_u32 m0, s70, 0\n\t"
    "ds_read_b128 a[168:171], v68 offset:35840\n\t"
    "v_mfma_f32_16x16x32_bf16 v[160:163], a[148:151], a[36:39], v[160:163]\n\t"
    "buffer_load_dwordx4 v4, s[44:47], s64 offen lds\n\t"
    "v_mfma_f32_16x16x32_bf16 v[164:167], a[144:147], a[40:43], v[164:167]\n\t"
    "v_mfma_f32_16x16x32_bf16 v[164:167], a[148:151], a[44:47], v[164:167]\n\t"
    "v_mfma_f32_16x16x32_bf16 v[168:171], a[144:147], a[48:51], v[168:171]\n\t"
    "ds_read_b128 a[172:175], v69 offset:35840\n\t"
    "v_mfma_f32_16x16x32_bf16 v[168:171], a[148:151], a[52:55], v[168:171]\n\t"
    "buffer_load_dwordx4 a[8:11], v18, s[40:43], s66 offen offset:1024 sc1\n\t"
    "v_mfma_f32_16x16x32_bf16 v[172:175], a[144:147], a[56:59], v[172:175]\n\t"
    "v_mfma_f32_16x16x32_bf16 v[172:175], a[148:151], a[60:63], v[172:175]\n\t"
    "s_waitcnt lgkmcnt(4)\n\t"
    "v_mfma_f32_16x16x32_bf16 v[188:191], a[152:155], a[56:59], v[188:191]\n\t"
    "s_add_u32 m0, s71, 0\n\t"
    "ds_read_b128 a[176:179], v70 offset:33792\n\t"
    "v_mfma_f32_16x16x32_bf16 v[188:191], a[156:159], a[60:63], v[188:191]\n\t"
    "buffer_load_dwordx4 v5, s[44:47], s64 offen lds\n\t"
    "v_mfma_f32_16x16x32_bf16 v[184:187], a[152:155], a[48:51], v[184:187]\n\t"
    "v_mfma_f32_16x16x32_bf16 v[184:187], a[156:159], a[52:55], v[184:187]\n\t"
    "v_mfma_f32_16x16x32_bf16 v[180:183], a[152:155], a[40:43], v[180:183]\n\t"
    "ds_read_b128 a[180:183], v71 offset:33792\n\t"
    "v_mfma_f32_16x16x32_bf16 v[180:183], a[156:159], a[44:47], v[180:183]\n\t"
    "buffer_load_dwordx4 a[12:15], v6, s[40:43], s66 offen offset:1024 sc1\n\t"
    "v_mfma_f32_16x16x32_bf16 v[176:179], a[152:155], a[32:35], v[176:179]\n\t"
    "v_mfma_f32_16x16x32_bf16 v[176:179], a[156:159], a[36:39], v[176:179]\n\t"
    "s_waitcnt lgkmcnt(4)\n\t"
    "v_mfma_f32_16x16x32_bf16 v[192:195], a[160:163], a[32:35], v[192:195]\n\t"
    "s_add_u32 m0, s68, 16896\n\t"
    "ds_read_b128 a[184:187], v70 offset:35840\n\t"
    "v_mfma_f32_16x16x32_bf16 v[192:195], a[164:167], a[36:39], v[192:195]\n\t"
    "buffer_load_dwordx4 v2, s[44:47], s65 offen lds\n\t"
    "v_mfma_f32_16x16x32_bf16 v[196:199], a[160:163], a[40:43], v[196:199]\n\t"
    "v_mfma_f32_16x16x32_bf16 v[196:199], a[164:167], a[44:47], v[196:199]\n\t"
    "v_mfma_f32_16x16x32_bf16 v[200:203], a[160:163], a[48:51], v[200:203]\n\t"
    "ds_read_b128 a[188:191], v71 offset:35840\n\t"
    "v_mfma_f32_16x16x32_bf16 v[200:203], a[164:167], a[52:55], v[200:203]\n\t"
    "buffer_load_dwordx4 a[16:19], v18, s[40:43], s66 offen offset:2048 sc1\n\t"
    "v_mfma_f32_16x16x32_bf16 v[204:207], a[160:163], a[56:59], v[204:207]\n\t"
    "v_mfma_f32_16x16x32_bf16 v[204:207], a[164:167], a[60:63], v[204:207]\n\t"
    "s_waitcnt lgkmcnt(4)\n\t"
    "v_mfma_f32_16x16x32_bf16 v[220:223], a[168:171], a[56:59], v[220:223]\n\t"
    "s_add_u32 m0, s69, 16896\n\t"
    "v_mfma_f32_16x16x32_bf16 v[220:223], a[172:175], a[60:63], v[220:223]\n\t"
    "buffer_load_dwordx4 v3, s[44:47], s65 offen lds\n\t"
    "v_mfma_f32_16x16x32_bf16 v[216:219], a[168:171], a[48:51], v[216:219]\n\t"
    "v_mfma_f32_16x16x32_bf16 v[216:219], a[172:175], a[52:55], v[216:219]\n\t"
    "v_mfma_f32_16x16x32_bf16 v[212:215], a[168:171], a[40:43], v[212:215]\n\t"
    "v_mfma_f32_16x16x32_bf16 v[212:215], a[172:175], a[44:47], v[212:215]\n\t"
    "buffer_load_dwordx4 a[20:23], v6, s[40:43], s66 offen offset:2048 sc1\n\t"
    "v_mfma_f32_16x16x32_bf16 v[208:211], a[168:171], a[32:35], v[208:211]\n\t"
    "v_mfma_f32_16x16x32_bf16 v[208:211], a[172:175], a[36:39], v[208:211]\n\t"
    "s_waitcnt vmcnt(28) lgkmcnt(0)\n\t"
    "s_barrier\n\t"
    "v_mfma_f32_16x16x32_bf16 v[224:227], a[176:179], a[32:35], v[224:227]\n\t"
    "s_add_u32 m0, s70, 16896\n\t"
    "ds_read_b128 a[128:131], v72 offset:0\n\t"
    "v_mfma_f32_16x16x32_bf16 v[224:227], a[180:183], a[36:39], v[224:227]\n\t"
    "buffer_load_dwordx4 v4, s[44:47], s65 offen lds\n\t"
    "v_mfma_f32_16x16x32_bf16 v[228:231], a[176:179], a[40:43], v[228:231]\n\t"
    "v_mfma_f32_16x16x32_bf16 v[228:231], a[180:183], a[44:47], v[228:231]\n\t"
    "ds_read_b128 a[132:135], v73 offset:0\n\t"
    "v_mfma_f32_16x16x32_bf16 v[232:235], a[176:179], a[48:51], v[232:235]\n\t"
    "v_mfma_f32_16x16x32_bf16 v[232:235], a[180:183], a[52:55], v[232:235]\n\t"
    "buffer_load_dwordx4 a[24:27], v18, s[40:43], s66 offen offset:3072 sc1\n\t"
    "v_mfma_f32_16x16x32_bf16 v[236:239], a[176:179], a[56:59], v[236:239]\n\t"
    "ds_read_b128 a[136:139], v72 offset:2048\n\t"
    "v_mfma_f32_16x16x32_bf16 v[236:239], a[180:183], a[60:63], v[236:239]\n\t"
    "v_mfma_f32_16x16x32_bf16 v[252:255], a[184:187], a[56:59], v[252:255]\n\t"
    "s_add_u32 m0, s71, 16896\n\t"
    "ds_read_b128 a[140:143], v73 offset:2048\n\t"
    "v_mfma_f32_16x16x32_bf16 v[252:255], a[188:191], a[60:63], v[252:255]\n\t"
    "buffer_load_dwordx4 v5, s[44:47], s65 offen lds\n\t"
    "v_mfma_f32_16x16x32_bf16 v[248:251], a[184:187], a[48:51], v[248:251]\n\t"
    "s_add_u32 s64, s64, s72\n\t"
    "v_mfma_f32_16x16x32_bf16 v[248:251], a[188:191], a[52:55], v[248:251]\n\t"
    "s_add_u32 s65, s65, s72\n\t"
    "ds_read_b128 a[144:147], v74 offset:0\n\t"
    "v_mfma_f32_16x16x32_bf16 v[244:247], a[184:187], a[40:43], v[244:247]\n\t"
    "v_mfma_f32_16x16x32_bf16 v[244:247], a[188:191], a[44:47], v[244:247]\n\t"
    "buffer_load_dwordx4 a[28:31], v6, s[40:43], s66 offen offset:3072 sc1\n\t"
    "v_mfma_f32_16x16x32_bf16 v[240:243], a[184:187], a[32:35], v[240:243]\n\t"
    "s_add_u32 s66, s66, 524288\n\t"
    "ds_read_b128 a[148:151], v75 offset:0\n\t"
    "v_mfma_f32_16x16x32_bf16 v[240:243], a[188:191], a[36:39], v[240:243]\n\t"
    "s_cmp_eq_u32 s74, 4\n\t"
    "s_cbranch_scc1 L_final_%=\n\t"
    "s_waitcnt lgkmcnt(4)\n\t"
    "v_mfma_f32_16x16x32_bf16 v[128:131], a[128:131], a[64:67], v[128:131]\n\t"
    "s_add_u32 m0, s68, 33792\n\t"
    "ds_read_b128 a[152:155], v74 offset:2048\n\t"
    "v_mfma_f32_16x16x32_bf16 v[128:131], a[132:135], a[68:71], v[128:131]\n\t"
    "buffer_load_dwordx4 v2, s[44:47], s64 offen lds\n\t"
    "v_mfma_f32_16x16x32_bf16 v[132:135], a[128:131], a[72:75], v[132:135]\n\t"
    "v_mfma_f32_16x16x32_bf16 v[132:135], a[132:135], a[76:79], v[132:135]\n\t"
    "v_mfma_f32_16x16x32_bf16 v[136:139], a[128:131], a[80:83], v[136:139]\n\t"
    "ds_read_b128 a[156:159], v75 offset:2048\n\t"
    "v_mfma_f32_16x16x32_bf16 v[136:139], a[132:135], a[84:87], v[136:139]\n\t"
    "buffer_load_dwordx4 a[32:35], v18, s[40:43], s66 offen offset:0 sc1\n\t"
    "v_mfma_f32_16x16x32_bf16 v[140:143], a[128:131], a[88:91], v[140:143]\n\t"
    "v_mfma_f32_16x16x32_bf16 v[140:143], a[132:135], a[92:95], v[140:143]\n\t"
    "s_waitcnt lgkmcnt(4)\n\t"
    "v_mfma_f32_16x16x32_bf16 v[156:159], a[136:139], a[88:91], v[156:159]\n\t"
    "s_add_u32 m0, s69, 33792\n\t"
    "ds_read_b128 a[160:163], v76 offset:0\n\t"
    "v_mfma_f32_16x16x32_bf16 v[156:159], a[140:143], a[92:95], v[156:159]\n\t"
    "buffer_load_dwordx4 v3, s[44:47], s64 offen lds\n\t"
    "v_mfma_f32_16x16x32_bf16 v[152:155], a[136:139], a[80:83], v[152:155]\n\t"
    "v_mfma_f32_16x16x32_bf16 v[152:155], a[140:143], a[84:87], v[152:155]\n\t"
    "v_mfma_f32_16x16x32_bf16 v[148:151], a[136:139], a[72:75], v[148:151]\n\t"
    "ds_read_b128 a[164:167], v77 offset:0\n\t"
    "v_mfma_f32_16x16x32_bf16 v[148:151], a[140:143], a[76:79], v[148:151]\n\t"
    "buffer_load_dwordx4 a[36:39], v6, s[40:43], s66 offen offset:0 sc1\n\t"
    "v_mfma_f32_16x16x32_bf16 v[144:147], a[136:139], a[64:67], v[144:147]\n\t"
    "v_mfma_f32_16x16x32_bf16 v[144:147], a[140:143], a[68:71], v[144:147]\n\t"
    "s_waitcnt lgkmcnt(4)\n\t"
    "v_mfma_f32_16x16x32_bf16 v[160:163], a[144:147], a[64:67], v[160:163]\n\t"
    "s_add_u32 m0, s70, 33792\n\t"
    "ds_read_b128 a[168:171], v76 offset:2048\n\t"
    "v_mfma_f32_16x16x32_bf16 v[160:163], a[148:151], a[68:71], v[160:163]\n\t"
    "buffer_load_dwordx4 v4, s[44:47], s64 offen lds\n\t"
    "v_mfma_f32_16x16x32_bf16 v[164:167], a[144:147], a[72:75], v[164:167]\n\t"
    "v_mfma_f32_16x16x32_bf16 v[164:167], a[148:151], a[76:79], v[164:167]\n\t"
    "v_mfma_f32_16x16x32_bf16 v[168:171], a[144:147], a[80:83], v[168:171]\n\t"
    "ds_read_b128 a[172:175], v77 offset:2048\n\t"
    "v_mfma_f32_16x16x32_bf16 v[168:171], a[148:151], a[84:87], v[168:171]\n\t"
    "buffer_load_dwordx4 a[40:43], v18, s[40:43], s66 offen offset:1024 sc1\n\t"
    "v_mfma_f32_16x16x32_bf16 v[172:175], a[144:147], a[88:91], v[172:175]\n\t"
    "v_mfma_f32_16x16x32_bf16 v[172:175], a[148:151], a[92:95], v[172:175]\n\t"
    "s_waitcnt lgkmcnt(4)\n\t"
    "v_mfma_f32_16x16x32_bf16 v[188:191], a[152:155], a[88:91], v[188:191]\n\t"
    "s_add_u32 m0, s71, 33792\n\t"
    "ds_read_b128 a[176:179], v78 offset:0\n\t"
    "v_mfma_f32_16x16x32_bf16 v[188:191], a[156:159], a[92:95], v[188:191]\n\t"
    "buffer_load_dwordx4 v5, s[44:47], s64 offen lds\n\t"
    "v_mfma_f32_16x16x32_bf16 v[184:187], a[152:155], a[80:83], v[184:187]\n\t"
    "v_mfma_f32_16x16x32_bf16 v[184:187], a[156:159], a[84:87], v[184:187]\n\t"
    "v_mfma_f32_16x16x32_bf16 v[180:183], a[152:155], a[72:75], v[180:183]\n\t"
    "ds_read_b128 a[180:183], v79 offset:0\n\t"
    "v_mfma_f32_16x16x32_bf16 v[180:183], a[156:159], a[76:79], v[180:183]\n\t"
    "buffer_load_dwordx4 a[44:47], v6, s[40:43], s66 offen offset:1024 sc1\n\t"
    "v_mfma_f32_16x16x32_bf16 v[176:179], a[152:155], a[64:67], v[176:179]\n\t"
    "v_mfma_f32_16x16x32_bf16 v[176:179], a[156:159], a[68:71], v[176:179]\n\t"
    "s_waitcnt lgkmcnt(4)\n\t"
    "v_mfma_f32_16x16x32_bf16 v[192:195], a[160:163], a[64:67], v[192:195]\n\t"
    "s_add_u32 m0, s68, 50688\n\t"
    "ds_read_b128 a[184:187], v78 offset:2048\n\t"
    "v_mfma_f32_16x16x32_bf16 v[192:195], a[164:167], a[68:71], v[192:195]\n\t"
    "buffer_load_dwordx4 v2, s[44:47], s65 offen lds\n\t"
    "v_mfma_f32_16x16x32_bf16 v[196:199], a[160:163], a[72:75], v[196:199]\n\t"
    "v_mfma_f32_16x16x32_bf16 v[196:199], a[164:167], a[76:79], v[196:199]\n\t"
    "v_mfma_f32_16x16x32_bf16 v[200:203], a[160:163], a[80:83], v[200:203]\n\t"
    "ds_read_b128 a[188:191], v79 offset:2048\n\t"
    "v_mfma_f32_16x16x32_bf16 v[200:203], a[164:167], a[84:87], v[200:203]\n\t"
    "buffer_load_dwordx4 a[48:51], v18, s[40:43], s66 offen offset:2048 sc1\n\t"
    "v_mfma_f32_16x16x32_bf16 v[204:207], a[160:163], a[88:91], v[204:207]\n\t"
    "v_mfma_f32_16x16x32_bf16 v[204:207], a[164:167], a[92:95], v[204:207]\n\t"
    "s_waitcnt lgkmcnt(4)\n\t"
    "v_mfma_f32_16x16x32_bf16 v[220:223], a[168:171], a[88:91], v[220:223]\n\t"
    "s_add_u32 m0, s69, 50688\n\t"
    "v_mfma_f32_16x16x32_bf16 v[220:223], a[172:175], a[92:95], v[220:223]\n\t"
    "buffer_load_dwordx4 v3, s[44:47], s65 offen lds\n\t"
    "v_mfma_f32_16x16x32_bf16 v[216:219], a[168:171], a[80:83], v[216:219]\n\t"
    "v_mfma_f32_16x16x32_bf16 v[216:219], a[172:175], a[84:87], v[216:219]\n\t"
    "v_mfma_f32_16x16x32_bf16 v[212:215], a[168:171], a[72:75], v[212:215]\n\t"
    "v_mfma_f32_16x16x32_bf16 v[212:215], a[172:175], a[76:79], v[212:215]\n\t"
    "buffer_load_dwordx4 a[52:55], v6, s[40:43], s66 offen offset:2048 sc1\n\t"
    "v_mfma_f32_16x16x32_bf16 v[208:211], a[168:171], a[64:67], v[208:211]\n\t"
    "v_mfma_f32_16x16x32_bf16 v[208:211], a[172:175], a[68:71], v[208:211]\n\t"
    "s_waitcnt vmcnt(28) lgkmcnt(0)\n\t"
    "s_barrier\n\t"
    "v_mfma_f32_16x16x32_bf16 v[224:227], a[176:179], a[64:67], v[224:227]\n\t"
    "s_add_u32 m0, s70, 50688\n\t"
    "ds_read_b128 a[128:131], v72 offset:33792\n\t"
    "v_mfma_f32_16x16x32_bf16 v[224:227], a[180:183], a[68:71], v[224:227]\n\t"
    "buffer_load_dwordx4 v4, s[44:47], s65 offen lds\n\t"
    "v_mfma_f32_16x16x32_bf16 v[228:231], a[176:179], a[72:75], v[228:231]\n\t"
    "v_mfma_f32_16x16x32_bf16 v[228:231], a[180:183], a[76:79], v[228:231]\n\t"
    "ds_read_b128 a[132:135], v73 offset:33792\n\t"
    "v_mfma_f32_16x16x32_bf16 v[232:235], a[176:179], a[80:83], v[232:235]\n\t"
    "v_mfma_f32_16x16x32_bf16 v[232:235], a[180:183], a[84:87], v[232:235]\n\t"
    "buffer_load_dwordx4 a[56:59], v18, s[40:43], s66 offen offset:3072 sc1\n\t"
    "v_mfma_f32_16x16x32_bf16 v[236:239], a[176:179], a[88:91], v[236:239]\n\t"
    "ds_read_b128 a[136:139], v72 offset:35840\n\t"
    "v_mfma_f32_16x16x32_bf16 v[236:239], a[180:183], a[92:95], v[236:239]\n\t"
    "v_mfma_f32_16x16x32_bf16 v[252:255], a[184:187], a[88:91], v[252:255]\n\t"
    "s_add_u32 m0, s71, 50688\n\t"
    "ds_read_b128 a[140:143], v73 offset:35840\n\t"
    "v_mfma_f32_16x16x32_bf16 v[252:255], a[188:191], a[92:95], v[252:255]\n\t"
    "buffer_load_dwordx4 v5, s[44:47], s65 offen lds\n\t"
    "v_mfma_f32_16x16x32_bf16 v[248:251], a[184:187], a[80:83], v[248:251]\n\t"
    "s_add_u32 s64, s64, s72\n\t"
    "v_mfma_f32_16x16x32_bf16 v[248:251], a[188:191], a[84:87], v[248:251]\n\t"
    "s_add_u32 s65, s65, s72\n\t"
    "ds_read_b128 a[144:147], v74 offset:33792\n\t"
    "v_mfma_f32_16x16x32_bf16 v[244:247], a[184:187], a[72:75], v[244:247]\n\t"
    "v_mfma_f32_16x16x32_bf16 v[244:247], a[188:191], a[76:79], v[244:247]\n\t"
    "buffer_load_dwordx4 a[60:63], v6, s[40:43], s66 offen offset:3072 sc1\n\t"
    "v_mfma_f32_16x16x32_bf16 v[240:243], a[184:187], a[64:67], v[240:243]\n\t"
    "s_add_u32 s66, s66, 524288\n\t"
    "ds_read_b128 a[148:151], v75 offset:33792\n\t"
    "v_mfma_f32_16x16x32_bf16 v[240:243], a[188:191], a[68:71], v[240:243]\n\t"
    "s_waitcnt lgkmcnt(4)\n\t"
    "v_mfma_f32_16x16x32_bf16 v[128:131], a[128:131], a[96:99], v[128:131]\n\t"
    "s_add_u32 m0, s68, 67584\n\t"
    "ds_read_b128 a[152:155], v74 offset:35840\n\t"
    "v_mfma_f32_16x16x32_bf16 v[128:131], a[132:135], a[100:103], v[128:131]\n\t"
    "buffer_load_dwordx4 v2, s[44:47], s64 offen lds\n\t"
    "v_mfma_f32_16x16x32_bf16 v[132:135], a[128:131], a[104:107], v[132:135]\n\t"
    "v_mfma_f32_16x16x32_bf16 v[132:135], a[132:135], a[108:111], v[132:135]\n\t"
    "v_mfma_f32_16x16x32_bf16 v[136:139], a[128:131], a[112:115], v[136:139]\n\t"
    "ds_read_b128 a[156:159], v75 offset:35840\n\t"
    "v_mfma_f32_16x16x32_bf16 v[136:139], a[132:135], a[116:119], v[136:139]\n\t"
    "buffer_load_dwordx4 a[64:67], v18, s[40:43], s66 offen offset:0 sc1\n\t"
    "v_mfma_f32_16x16x32_bf16 v[140:143], a[128:131], a[120:123], v[140:143]\n\t"
    "v_mfma_f32_16x16x32_bf16 v[140:143], a[132:135], a[124:127], v[140:143]\n\t"
    "s_waitcnt lgkmcnt(4)\n\t"
    "v_mfma_f32_16x16x32_bf16 v[156:159], a[136:139], a[120:123], v[156:159]\n\t"
    "s_add_u32 m0, s69, 67584\n\t"
    "ds_read_b128 a[160:163], v76 offset:33792\n\t"
    "v_mfma_f32_16x16x32_bf16 v[156:159], a[140:143], a[124:127], v[156:159]\n\t"
    "buffer_load_dwordx4 v3, s[44:47], s64 offen lds\n\t"
    "v_mfma_f32_16x16x32_bf16 v[152:155], a[136:139], a[112:115], v[152:155]\n\t"
    "v_mfma_f32_16x16x32_bf16 v[152:155], a[140:143], a[116:119], v[152:155]\n\t"
    "v_mfma_f32_16x16x32_bf16 v[148:151], a[136:139], a[104:107], v[148:151]\n\t"
    "ds_read_b128 a[164:167], v77 offset:33792\n\t"
    "v_mfma_f32_16x16x32_bf16 v[148:151], a[140:143], a[108:111], v[148:151]\n\t"
    "buffer_load_dwordx4 a[68:71], v6, s[40:43], s66 offen offset:0 sc1\n\t"
    "v_mfma_f32_16x16x32_bf16 v[144:147], a[136:139], a[96:99], v[144:147]\n\t"
    "v_mfma_f32_16x16x32_bf16 v[144:147], a[140:143], a[100:103], v[144:147]\n\t"
    "s_waitcnt lgkmcnt(4)\n\t"
    "v_mfma_f32_16x16x32_bf16 v[160:163], a[144:147], a[96:99], v[160:163]\n\t"
    "s_add_u32 m0, s70, 67584\n\t"
    "ds_read_b128 a[168:171], v76 offset:35840\n\t"
    "v_mfma_f32_16x16x32_bf16 v[160:163], a[148:151], a[100:103], v[160:163]\n\t"
    "buffer_load_dwordx4 v4, s[44:47], s64 offen lds\n\t"
    "v_mfma_f32_16x16x32_bf16 v[164:167], a[144:147], a[104:107], v[164:167]\n\t"
    "v_mfma_f32_16x16x32_bf16 v[164:167], a[148:151], a[108:111], v[164:167]\n\t"
    "v_mfma_f32_16x16x32_bf16 v[168:171], a[144:147], a[112:115], v[168:171]\n\t"
    "ds_read_b128 a[172:175], v77 offset:35840\n\t"
    "v_mfma_f32_16x16x32_bf16 v[168:171], a[148:151], a[116:119], v[168:171]\n\t"
    "buffer_load_dwordx4 a[72:75], v18, s[40:43], s66 offen offset:1024 sc1\n\t"
    "v_mfma_f32_16x16x32_bf16 v[172:175], a[144:147], a[120:123], v[172:175]\n\t"
    "v_mfma_f32_16x16x32_bf16 v[172:175], a[148:151], a[124:127], v[172:175]\n\t"
    "s_waitcnt lgkmcnt(4)\n\t"
    "v_mfma_f32_16x16x32_bf16 v[188:191], a[152:155], a[120:123], v[188:191]\n\t"
    "s_add_u32 m0, s71, 67584\n\t"
    "ds_read_b128 a[176:179], v78 offset:33792\n\t"
    "v_mfma_f32_16x16x32_bf16 v[188:191], a[156:159], a[124:127], v[188:191]\n\t"
    "buffer_load_dwordx4 v5, s[44:47], s64 offen lds\n\t"
    "v_mfma_f32_16x16x32_bf16 v[184:187], a[152:155], a[112:115], v[184:187]\n\t"
    "v_mfma_f32_16x16x32_bf16 v[184:187], a[156:159], a[116:119], v[184:187]\n\t"
    "v_mfma_f32_16x16x32_bf16 v[180:183], a[152:155], a[104:107], v[180:183]\n\t"
    "ds_read_b128 a[180:183], v79 offset:33792\n\t"
    "v_mfma_f32_16x16x32_bf16 v[180:183], a[156:159], a[108:111], v[180:183]\n\t"
    "buffer_load_dwordx4 a[76:79], v6, s[40:43], s66 offen offset:1024 sc1\n\t"
    "v_mfma_f32_16x16x32_bf16 v[176:179], a[152:155], a[96:99], v[176:179]\n\t"
    "v_mfma_f32_16x16x32_bf16 v[176:179], a[156:159], a[100:103], v[176:179]\n\t"
    "s_waitcnt lgkmcnt(4)\n\t"
    "v_mfma_f32_16x16x32_bf16 v[192:195], a[160:163], a[96:99], v[192:195]\n\t"
    "s_add_u32 m0, s68, 84480\n\t"
    "ds_read_b128 a[184:187], v78 offset:35840\n\t"
    "v_mfma_f32_16x16x32_bf16 v[192:195], a[164:167], a[100:103], v[192:195]\n\t"
    "buffer_load_dwordx4 v2, s[44:47], s65 offen lds\n\t"
    "v_mfma_f32_16x16x32_bf16 v[196:199], a[160:163], a[104:107], v[196:199]\n\t"
    "v_mfma_f32_16x16x32_bf16 v[196:199], a[164:167], a[108:111], v[196:199]\n\t"
    "v_mfma_f32_16x16x32_bf16 v[200:203], a[160:163], a[112:115], v[200:203]\n\t"
    "ds_read_b128 a[188:191], v79 offset:35840\n\t"
    "v_mfma_f32_16x16x32_bf16 v[200:203], a[164:167], a[116:119], v[200:203]\n\t"
    "buffer_load_dwordx4 a[80:83], v18, s[40:43], s66 offen offset:2048 sc1\n\t"
    "v_mfma_f32_16x16x32_bf16 v[204:207], a[160:163], a[120:123], v[204:207]\n\t"
    "v_mfma_f32_16x16x32_bf16 v[204:207], a[164:167], a[124:127], v[204:207]\n\t"
    "s_waitcnt lgkmcnt(4)\n\t"
    "v_mfma_f32_16x16x32_bf16 v[220:223], a[168:171], a[120:123], v[220:223]\n\t"
    "s_add_u32 m0, s69, 84480\n\t"
    "v_mfma_f32_16x16x32_bf16 v[220:223], a[172:175], a[124:127], v[220:223]\n\t"
    "buffer_load_dwordx4 v3, s[44:47], s65 offen lds\n\t"
    "v_mfma_f32_16x16x32_bf16 v[216:219], a[168:171], a[112:115], v[216:219]\n\t"
    "v_mfma_f32_16x16x32_bf16 v[216:219], a[172:175], a[116:119], v[216:219]\n\t"
    "v_mfma_f32_16x16x32_bf16 v[212:215], a[168:171], a[104:107], v[212:215]\n\t"
    "v_mfma_f32_16x16x32_bf16 v[212:215], a[172:175], a[108:111], v[212:215]\n\t"
    "buffer_load_dwordx4 a[84:87], v6, s[40:43], s66 offen offset:2048 sc1\n\t"
    "v_mfma_f32_16x16x32_bf16 v[208:211], a[168:171], a[96:99], v[208:211]\n\t"
    "v_mfma_f32_16x16x32_bf16 v[208:211], a[172:175], a[100:103], v[208:211]\n\t"
    "s_waitcnt vmcnt(28) lgkmcnt(0)\n\t"
    "s_barrier\n\t"
    "v_mfma_f32_16x16x32_bf16 v[224:227], a[176:179], a[96:99], v[224:227]\n\t"
    "s_add_u32 m0, s70, 84480\n\t"
    "ds_read_b128 a[128:131], v64 offset:0\n\t"
    "v_mfma_f32_16x16x32_bf16 v[224:227], a[180:183], a[100:103], v[224:227]\n\t"
    "buffer_load_dwordx4 v4, s[44:47], s65 offen lds\n\t"
    "v_mfma_f32_16x16x32_bf16 v[228:231], a[176:179], a[104:107], v[228:231]\n\t"
    "v_mfma_f32_16x16x32_bf16 v[228:231], a[180:183], a[108:111], v[228:231]\n\t"
    "ds_read_b128 a[132:135], v65 offset:0\n\t"
    "v_mfma_f32_16x16x32_bf16 v[232:235], a[176:179], a[112:115], v[232:235]\n\t"
    "v_mfma_f32_16x16x32_bf16 v[232:235], a[180:183], a[116:119], v[232:235]\n\t"
    "buffer_load_dwordx4 a[88:91], v18, s[40:43], s66 offen offset:3072 sc1\n\t"
    "v_mfma_f32_16x16x32_bf16 v[236:239], a[176:179], a[120:123], v[236:239]\n\t"
    "ds_read_b128 a[136:139], v64 offset:2048\n\t"
    "v_mfma_f32_16x16x32_bf16 v[236:239], a[180:183], a[124:127], v[236:239]\n\t"
    "v_mfma_f32_16x16x32_bf16 v[252:255], a[184:187], a[120:123], v[252:255]\n\t"
    "s_add_u32 m0, s71, 84480\n\t"
    "ds_read_b128 a[140:143], v65 offset:2048\n\t"
    "v_mfma_f32_16x16x32_bf16 v[252:255], a[188:191], a[124:127], v[252:255]\n\t"
    "buffer_load_dwordx4 v5, s[44:47], s65 offen lds\n\t"
    "v_mfma_f32_16x16x32_bf16 v[248:251], a[184:187], a[112:115], v[248:251]\n\t"
    "s_add_u32 s64, s64, s72\n\t"
    "v_mfma_f32_16x16x32_bf16 v[248:251], a[188:191], a[116:119], v[248:251]\n\t"
    "s_add_u32 s65, s65, s72\n\t"
    "ds_read_b128 a[144:147], v66 offset:0\n\t"
    "v_mfma_f32_16x16x32_bf16 v[244:247], a[184:187], a[104:107], v[244:247]\n\t"
    "v_mfma_f32_16x16x32_bf16 v[244:247], a[188:191], a[108:111], v[244:247]\n\t"
    "buffer_load_dwordx4 a[92:95], v6, s[40:43], s66 offen offset:3072 sc1\n\t"
    "v_mfma_f32_16x16x32_bf16 v[240:243], a[184:187], a[96:99], v[240:243]\n\t"
    "s_add_u32 s66, s66, 524288\n\t"
    "ds_read_b128 a[148:151], v67 offset:0\n\t"
    "v_mfma_f32_16x16x32_bf16 v[240:243], a[188:191], a[100:103], v[240:243]\n\t"
    "s_add_u32 s74, s74, 1\n\t"
    "s_branch L_loopC_%=\n\t"
    "L_final_%=:\n\t"
    "s_waitcnt lgkmcnt(4)\n\t"
    "v_mfma_f32_16x16x32_bf16 v[128:131], a[128:131], a[64:67], v[128:131]\n\t"
    "s_add_u32 m0, s68, 33792\n\t"
    "ds_read_b128 a[152:155], v74 offset:2048\n\t"
    "v_mfma_f32_16x16x32_bf16 v[128:131], a[132:135], a[68:71], v[128:131]\n\t"
    "buffer_load_dwordx4 v2, s[44:47], s64 offen lds\n\t"
    "v_mfma_f32_16x16x32_bf16 v[132:135], a[128:131], a[72:75], v[132:135]\n\t"
    "v_mfma_f32_16x16x32_bf16 v[132:135], a[132:135], a[76:79], v[132:135]\n\t"
    "v_mfma_f32_16x16x32_bf16 v[136:139], a[128:131], a[80:83], v[136:139]\n\t"
    "ds_read_b128 a[156:159], v75 offset:2048\n\t"
    "v_mfma_f32_16x16x32_bf16 v[136:139], a[132:135], a[84:87], v[136:139]\n\t"
    "buffer_load_dwordx4 a[32:35], v18, s[40:43], s66 offen offset:0 sc1\n\t"
    "v_mfma_f32_16x16x32_bf16 v[140:143], a[128:131], a[88:91], v[140:143]\n\t"
    "v_mfma_f32_16x16x32_bf16 v[140:143], a[132:135], a[92:95], v[140:143]\n\t"
    "s_waitcnt lgkmcnt(4)\n\t"
    "v_mfma_f32_16x16x32_bf16 v[156:159], a[136:139], a[88:91], v[156:159]\n\t"
    "s_add_u32 m0, s69, 33792\n\t"
    "ds_read_b128 a[160:163], v76 offset:0\n\t"
    "v_mfma_f32_16x16x32_bf16 v[156:159], a[140:143], a[92:95], v[156:159]\n\t"
    "buffer_load_dwordx4 v3, s[44:47], s64 offen lds\n\t"
    "v_mfma_f32_16x16x32_bf16 v[152:155], a[136:139], a[80:83], v[152:155]\n\t"
    "v_mfma_f32_16x16x32_bf16 v[152:155], a[140:143], a[84:87], v[152:155]\n\t"
    "v_mfma_f32_16x16x32_bf16 v[148:151], a[136:139], a[72:75], v[148:151]\n\t"
    "ds_read_b128 a[164:167], v77 offset:0\n\t"
    "v_mfma_f32_16x16x32_bf16 v[148:151], a[140:143], a[76:79], v[148:151]\n\t"
    "buffer_load_dwordx4 a[36:39], v6, s[40:43], s66 offen offset:0 sc1\n\t"
    "v_mfma_f32_16x16x32_bf16 v[144:147], a[136:139], a[64:67], v[144:147]\n\t"
    "v_mfma_f32_16x16x32_bf16 v[144:147], a[140:143], a[68:71], v[144:147]\n\t"
    "s_waitcnt lgkmcnt(4)\n\t"
    "v_mfma_f32_16x16x32_bf16 v[160:163], a[144:147], a[64:67], v[160:163]\n\t"
    "s_add_u32 m0, s70, 33792\n\t"
    "ds_read_b128 a[168:171], v76 offset:2048\n\t"
    "v_mfma_f32_16x16x32_bf16 v[160:163], a[148:151], a[68:71], v[160:163]\n\t"
    "buffer_load_dwordx4 v4, s[44:47], s64 offen lds\n\t"
    "v_mfma_f32_16x16x32_bf16 v[164:167], a[144:147], a[72:75], v[164:167]\n\t"
    "v_mfma_f32_16x16x32_bf16 v[164:167], a[148:151], a[76:79], v[164:167]\n\t"
    "v_mfma_f32_16x16x32_bf16 v[168:171], a[144:147], a[80:83], v[168:171]\n\t"
    "ds_read_b128 a[172:175], v77 offset:2048\n\t"
    "v_mfma_f32_16x16x32_bf16 v[168:171], a[148:151], a[84:87], v[168:171]\n\t"
    "buffer_load_dwordx4 a[40:43], v18, s[40:43], s66 offen offset:1024 sc1\n\t"
    "v_mfma_f32_16x16x32_bf16 v[172:175], a[144:147], a[88:91], v[172:175]\n\t"
    "v_mfma_f32_16x16x32_bf16 v[172:175], a[148:151], a[92:95], v[172:175]\n\t"
    "s_waitcnt lgkmcnt(4)\n\t"
    "v_mfma_f32_16x16x32_bf16 v[188:191], a[152:155], a[88:91], v[188:191]\n\t"
    "s_add_u32 m0, s71, 33792\n\t"
    "ds_read_b128 a[176:179], v78 offset:0\n\t"
    "v_mfma_f32_16x16x32_bf16 v[188:191], a[156:159], a[92:95], v[188:191]\n\t"
    "buffer_load_dwordx4 v5, s[44:47], s64 offen lds\n\t"
    "v_mfma_f32_16x16x32_bf16 v[184:187], a[152:155], a[80:83], v[184:187]\n\t"
    "v_mfma_f32_16x16x32_bf16 v[184:187], a[156:159], a[84:87], v[184:187]\n\t"
    "v_mfma_f32_16x16x32_bf16 v[180:183], a[152:155], a[72:75], v[180:183]\n\t"
    "ds_read_b128 a[180:183], v79 offset:0\n\t"
    "v_mfma_f32_16x16x32_bf16 v[180:183], a[156:159], a[76:79], v[180:183]\n\t"
    "buffer_load_dwordx4 a[44:47], v6, s[40:43], s66 offen offset:1024 sc1\n\t"
    "v_mfma_f32_16x16x32_bf16 v[176:179], a[152:155], a[64:67], v[176:179]\n\t"
    "v_mfma_f32_16x16x32_bf16 v[176:179], a[156:159], a[68:71], v[176:179]\n\t"
    "s_waitcnt lgkmcnt(4)\n\t"
    "v_mfma_f32_16x16x32_bf16 v[192:195], a[160:163], a[64:67], v[192:195]\n\t"
    "s_add_u32 m0, s68, 50688\n\t"
    "ds_read_b128 a[184:187], v78 offset:2048\n\t"
    "v_mfma_f32_16x16x32_bf16 v[192:195], a[164:167], a[68:71], v[192:195]\n\t"
    "buffer_load_dwordx4 v2, s[44:47], s65 offen lds\n\t"
    "v_mfma_f32_16x16x32_bf16 v[196:199], a[160:163], a[72:75], v[196:199]\n\t"
    "v_mfma_f32_16x16x32_bf16 v[196:199], a[164:167], a[76:79], v[196:199]\n\t"
    "v_mfma_f32_16x16x32_bf16 v[200:203], a[160:163], a[80:83], v[200:203]\n\t"
    "ds_read_b128 a[188:191], v79 offset:2048\n\t"
    "v_mfma_f32_16x16x32_bf16 v[200:203], a[164:167], a[84:87], v[200:203]\n\t"
    "buffer_load_dwordx4 a[48:51], v18, s[40:43], s66 offen offset:2048 sc1\n\t"
    "v_mfma_f32_16x16x32_bf16 v[204:207], a[160:163], a[88:91], v[204:207]\n\t"
    "v_mfma_f32_16x16x32_bf16 v[204:207], a[164:167], a[92:95], v[204:207]\n\t"
    "s_waitcnt lgkmcnt(4)\n\t"
    "v_mfma_f32_16x16x32_bf16 v[220:223], a[168:171], a[88:91], v[220:223]\n\t"
    "s_add_u32 m0, s69, 50688\n\t"
    "v_mfma_f32_16x16x32_bf16 v[220:223], a[172:175], a[92:95], v[220:223]\n\t"
    "buffer_load_dwordx4 v3, s[44:47], s65 offen lds\n\t"
    "v_mfma_f32_16x16x32_bf16 v[216:219], a[168:171], a[80:83], v[216:219]\n\t"
    "v_mfma_f32_16x16x32_bf16 v[216:219], a[172:175], a[84:87], v[216:219]\n\t"
    "v_mfma_f32_16x16x32_bf16 v[212:215], a[168:171], a[72:75], v[212:215]\n\t"
    "v_mfma_f32_16x16x32_bf16 v[212:215], a[172:175], a[76:79], v[212:215]\n\t"
    "buffer_load_dwordx4 a[52:55], v6, s[40:43], s66 offen offset:2048 sc1\n\t"
    "v_mfma_f32_16x16x32_bf16 v[208:211], a[168:171], a[64:67], v[208:211]\n\t"
    "v_mfma_f32_16x16x32_bf16 v[208:211], a[172:175], a[68:71], v[208:211]\n\t"
    "s_waitcnt vmcnt(28) lgkmcnt(0)\n\t"
    "s_barrier\n\t"
    "v_mfma_f32_16x16x32_bf16 v[224:227], a[176:179], a[64:67], v[224:227]\n\t"
    "s_add_u32 m0, s70, 50688\n\t"
    "ds_read_b128 a[128:131], v72 offset:33792\n\t"
    "v_mfma_f32_16x16x32_bf16 v[224:227], a[180:183], a[68:71], v[224:227]\n\t"
    "buffer_load_dwordx4 v4, s[44:47], s65 offen lds\n\t"
    "v_mfma_f32_16x16x32_bf16 v[228:231], a[176:179], a[72:75], v[228:231]\n\t"
    "v_mfma_f32_16x16x32_bf16 v[228:231], a[180:183], a[76:79], v[228:231]\n\t"
    "ds_read_b128 a[132:135], v73 offset:33792\n\t"
    "v_mfma_f32_16x16x32_bf16 v[232:235], a[176:179], a[80:83], v[232:235]\n\t"
    "v_mfma_f32_16x16x32_bf16 v[232:235], a[180:183], a[84:87], v[232:235]\n\t"
    "buffer_load_dwordx4 a[56:59], v18, s[40:43], s66 offen offset:3072 sc1\n\t"
    "v_mfma_f32_16x16x32_bf16 v[236:239], a[176:179], a[88:91], v[236:239]\n\t"
    "ds_read_b128 a[136:139], v72 offset:35840\n\t"
    "v_mfma_f32_16x16x32_bf16 v[236:239], a[180:183], a[92:95], v[236:239]\n\t"
    "v_mfma_f32_16x16x32_bf16 v[252:255], a[184:187], a[88:91], v[252:255]\n\t"
    "s_add_u32 m0, s71, 50688\n\t"
    "ds_read_b128 a[140:143], v73 offset:35840\n\t"
    "v_mfma_f32_16x16x32_bf16 v[252:255], a[188:191], a[92:95], v[252:255]\n\t"
    "buffer_load_dwordx4 v5, s[44:47], s65 offen lds\n\t"
    "v_mfma_f32_16x16x32_bf16 v[248:251], a[184:187], a[80:83], v[248:251]\n\t"
    "v_mfma_f32_16x16x32_bf16 v[248:251], a[188:191], a[84:87], v[248:251]\n\t"
    "ds_read_b128 a[144:147], v74 offset:33792\n\t"
    "v_mfma_f32_16x16x32_bf16 v[244:247], a[184:187], a[72:75], v[244:247]\n\t"
    "v_mfma_f32_16x16x32_bf16 v[244:247], a[188:191], a[76:79], v[244:247]\n\t"
    "buffer_load_dwordx4 a[60:63], v6, s[40:43], s66 offen offset:3072 sc1\n\t"
    "v_mfma_f32_16x16x32_bf16 v[240:243], a[184:187], a[64:67], v[240:243]\n\t"
    "ds_read_b128 a[148:151], v75 offset:33792\n\t"
    "v_mfma_f32_16x16x32_bf16 v[240:243], a[188:191], a[68:71], v[240:243]\n\t"
    "s_waitcnt lgkmcnt(4)\n\t"
    "v_mfma_f32_16x16x32_bf16 v[128:131], a[128:131], a[96:99], v[128:131]\n\t"
    "ds_read_b128 a[152:155], v74 offset:35840\n\t"
    "v_mfma_f32_16x16x32_bf16 v[128:131], a[132:135], a[100:103], v[128:131]\n\t"
    "v_mfma_f32_16x16x32_bf16 v[132:135], a[128:131], a[104:107], v[132:135]\n\t"
    "s_lshl_b32 s94, s98, 3\n\t"
    "v_mfma_f32_16x16x32_bf16 v[132:135], a[132:135], a[108:111], v[132:135]\n\t"
    "v_mfma_f32_16x16x32_bf16 v[136:139], a[128:131], a[112:115], v[136:139]\n\t"
    "s_lshl_b32 s90, s73, 2\n\t"
    "ds_read_b128 a[156:159], v75 offset:35840\n\t"
    "v_mfma_f32_16x16x32_bf16 v[136:139], a[132:135], a[116:119], v[136:139]\n\t"
    "v_mfma_f32_16x16x32_bf16 v[140:143], a[128:131], a[120:123], v[140:143]\n\t"
    "s_add_u32 s94, s94, s90\n\t"
    "v_mfma_f32_16x16x32_bf16 v[140:143], a[132:135], a[124:127], v[140:143]\n\t"
    "s_waitcnt lgkmcnt(4)\n\t"
    "v_mfma_f32_16x16x32_bf16 v[156:159], a[136:139], a[120:123], v[156:159]\n\t"
    "s_add_u32 s94, s94, s87\n\t"
    "ds_read_b128 a[160:163], v76 offset:33792\n\t"
    "v_mfma_f32_16x16x32_bf16 v[156:159], a[140:143], a[124:127], v[156:159]\n\t"
    "v_mfma_f32_16x16x32_bf16 v[152:155], a[136:139], a[112:115], v[152:155]\n\t"
    "s_lshl_b32 s94, s94, 6\n\t"
    "v_mfma_f32_16x16x32_bf16 v[152:155], a[140:143], a[116:119], v[152:155]\n\t"
    "v_mfma_f32_16x16x32_bf16 v[148:151], a[136:139], a[104:107], v[148:151]\n\t"
    "s_add_u32 s94, s94, 35782656\n\t"
    "ds_read_b128 a[164:167], v77 offset:33792\n\t"
    "v_mfma_f32_16x16x32_bf16 v[148:151], a[140:143], a[108:111], v[148:151]\n\t"
    "v_mfma_f32_16x16x32_bf16 v[144:147], a[136:139], a[96:99], v[144:147]\n\t"
    "s_xor_b32 s95, s94, 256\n\t"
    "v_mfma_f32_16x16x32_bf16 v[144:147], a[140:143], a[100:103], v[144:147]\n\t"
    "s_waitcnt lgkmcnt(4)\n\t"
    "v_mfma_f32_16x16x32_bf16 v[160:163], a[144:147], a[96:99], v[160:163]\n\t"
    "s_lshl_b32 s90, s87, 14\n\t"
    "ds_read_b128 a[168:171], v76 offset:35840\n\t"
    "v_mfma_f32_16x16x32_bf16 v[160:163], a[148:151], a[100:103], v[160:163]\n\t"
    "v_mfma_f32_16x16x32_bf16 v[164:167], a[144:147], a[104:107], v[164:167]\n\t"
    "s_add_u32 s90, s90, 67584\n\t"
    "v_mfma_f32_16x16x32_bf16 v[164:167], a[148:151], a[108:111], v[164:167]\n\t"
    "v_mfma_f32_16x16x32_bf16 v[168:171], a[144:147], a[112:115], v[168:171]\n\t"
    "v_add_u32_e32 v15, s90, v31\n\t"
    "ds_read_b128 a[172:175], v77 offset:35840\n\t"
    "v_mfma_f32_16x16x32_bf16 v[168:171], a[148:151], a[116:119], v[168:171]\n\t"
    "v_mfma_f32_16x16x32_bf16 v[172:175], a[144:147], a[120:123], v[172:175]\n\t"
    "s_lshl_b32 s90, s87, 14\n\t"
    "v_mfma_f32_16x16x32_bf16 v[172:175], a[148:151], a[124:127], v[172:175]\n\t"
    "s_waitcnt lgkmcnt(4)\n\t"
    "v_mfma_f32_16x16x32_bf16 v[188:191], a[152:155], a[120:123], v[188:191]\n\t"
    "s_xor_b32 s90, s90, 0x8000\n\t"
    "ds_read_b128 a[176:179], v78 offset:33792\n\t"
    "v_mfma_f32_16x16x32_bf16 v[188:191], a[156:159], a[124:127], v[188:191]\n\t"
    "v_mfma_f32_16x16x32_bf16 v[184:187], a[152:155], a[112:115], v[184:187]\n\t"
    "s_add_u32 s90, s90, 67584\n\t"
    "v_mfma_f32_16x16x32_bf16 v[184:187], a[156:159], a[116:119], v[184:187]\n\t"
    "v_mfma_f32_16x16x32_bf16 v[180:183], a[152:155], a[104:107], v[180:183]\n\t"
    "v_add_u32_e32 v14, s90, v31\n\t"
    "ds_read_b128 a[180:183], v79 offset:33792\n\t"
    "v_mfma_f32_16x16x32_bf16 v[180:183], a[156:159], a[108:111], v[180:183]\n\t"
    "v_mfma_f32_16x16x32_bf16 v[176:179], a[152:155], a[96:99], v[176:179]\n\t"
    "s_mov_b32 s60, 0\n\t"
    "v_mfma_f32_16x16x32_bf16 v[176:179], a[156:159], a[100:103], v[176:179]\n\t"
    "s_waitcnt lgkmcnt(4)\n\t"
    "v_mfma_f32_16x16x32_bf16 v[192:195], a[160:163], a[96:99], v[192:195]\n\t"
    "s_mov_b32 s61, 37748736\n\t"
    "ds_read_b128 a[184:187], v78 offset:35840\n\t"
    "v_mfma_f32_16x16x32_bf16 v[192:195], a[164:167], a[100:103], v[192:195]\n\t"
    "v_mfma_f32_16x16x32_bf16 v[196:199], a[160:163], a[104:107], v[196:199]\n\t"
    "v_xor_b32_e32 v13, 0x20000, v107\n\t"
    "v_mfma_f32_16x16x32_bf16 v[196:199], a[164:167], a[108:111], v[196:199]\n\t"
    "v_mfma_f32_16x16x32_bf16 v[200:203], a[160:163], a[112:115], v[200:203]\n\t"
    "v_cmp_eq_u32_e32 vcc, 0, v31\n\t"
    "ds_read_b128 a[188:191], v79 offset:35840\n\t"
    "v_mfma_f32_16x16x32_bf16 v[200:203], a[164:167], a[116:119], v[200:203]\n\t"
    "v_mfma_f32_16x16x32_bf16 v[204:207], a[160:163], a[120:123], v[204:207]\n\t"
    "v_bfrev_b32_e32 v106, 1\n\t"
    "v_mfma_f32_16x16x32_bf16 v[204:207], a[164:167], a[124:127], v[204:207]\n\t"
    "s_waitcnt lgkmcnt(4)\n\t"
    "v_mfma_f32_16x16x32_bf16 v[220:223], a[168:171], a[120:123], v[220:223]\n\t"
    "v_mov_b32_e32 v12, 0\n\t"
    "v_mfma_f32_16x16x32_bf16 v[220:223], a[172:175], a[124:127], v[220:223]\n\t"
    "v_mfma_f32_16x16x32_bf16 v[216:219], a[168:171], a[112:115], v[216:219]\n\t"
    "v_cndmask_b32_e64 v106, v106, 0, vcc\n\t"
    "v_mfma_f32_16x16x32_bf16 v[216:219], a[172:175], a[116:119], v[216:219]\n\t"
    "v_mfma_f32_16x16x32_bf16 v[212:215], a[168:171], a[104:107], v[212:215]\n\t"
    "s_mul_i32 s90, s83, 0x9e3779b1\n\t"
    "v_mfma_f32_16x16x32_bf16 v[212:215], a[172:175], a[108:111], v[212:215]\n\t"
    "v_mfma_f32_16x16x32_bf16 v[208:211], a[168:171], a[96:99], v[208:211]\n\t"
    "s_movk_i32 s33, 0xff00\n\t"
    "v_mfma_f32_16x16x32_bf16 v[208:211], a[172:175], a[100:103], v[208:211]\n\t"
    "s_waitcnt vmcnt(16) lgkmcnt(0)\n\t"
    "s_barrier\n\t"
    "v_mfma_f32_16x16x32_bf16 v[224:227], a[176:179], a[96:99], v[224:227]\n\t"
    "v_mov_b32_e32 v111, s90\n\t"
    "ds_read_b128 a[128:131], v64 offset:0\n\t"
    "v_mfma_f32_16x16x32_bf16 v[224:227], a[180:183], a[100:103], v[224:227]\n\t"
    "v_mfma_f32_16x16x32_bf16 v[228:231], a[176:179], a[104:107], v[228:231]\n\t"
    "v_and_b32_e32 v108, 0xff, v111\n\t"
    "v_mfma_f32_16x16x32_bf16 v[228:231], a[180:183], a[108:111], v[228:231]\n\t"
    "ds_read_b128 a[132:135], v65 offset:0\n\t"
    "v_mfma_f32_16x16x32_bf16 v[232:235], a[176:179], a[112:115], v[232:235]\n\t"
    "v_bfe_u32 v109, v111, 8, 8\n\t"
    "v_mfma_f32_16x16x32_bf16 v[232:235], a[180:183], a[116:119], v[232:235]\n\t"
    "v_mfma_f32_16x16x32_bf16 v[236:239], a[176:179], a[120:123], v[236:239]\n\t"
    "v_bfe_u32 v110, v111, 16, 8\n\t"
    "ds_read_b128 a[136:139], v64 offset:2048\n\t"
    "v_mfma_f32_16x16x32_bf16 v[236:239], a[180:183], a[124:127], v[236:239]\n\t"
    "v_mfma_f32_16x16x32_bf16 v[252:255], a[184:187], a[120:123], v[252:255]\n\t"
    "v_lshrrev_b32_e32 v111, 24, v111\n\t"
    "ds_read_b128 a[140:143], v65 offset:2048\n\t"
    "v_mfma_f32_16x16x32_bf16 v[252:255], a[188:191], a[124:127], v[252:255]\n\t"
    "v_mfma_f32_16x16x32_bf16 v[248:251], a[184:187], a[112:115], v[248:251]\n\t"
    "s_mov_b32 s64, 0\n\t"
    "v_mfma_f32_16x16x32_bf16 v[248:251], a[188:191], a[116:119], v[248:251]\n\t"
    "ds_read_b128 a[144:147], v66 offset:0\n\t"
    "v_mfma_f32_16x16x32_bf16 v[244:247], a[184:187], a[104:107], v[244:247]\n\t"
    "s_mov_b32 s65, 8192\n\t"
    "v_mfma_f32_16x16x32_bf16 v[244:247], a[188:191], a[108:111], v[244:247]\n\t"
    "v_mfma_f32_16x16x32_bf16 v[240:243], a[184:187], a[96:99], v[240:243]\n\t"
    "s_mov_b32 s66, 16384\n\t"
    "ds_read_b128 a[148:151], v67 offset:0\n\t"
    "v_mfma_f32_16x16x32_bf16 v[240:243], a[188:191], a[100:103], v[240:243]\n\t"
    "s_waitcnt lgkmcnt(4)\n\t"
    "v_mfma_f32_16x16x32_bf16 v[128:131], a[128:131], a[0:3], v[128:131]\n\t"
    "s_mov_b32 s67, 24576\n\t"
    "ds_read_b128 a[176:179], v70 offset:0\n\t"
    "v_mfma_f32_16x16x32_bf16 v[128:131], a[132:135], a[4:7], v[128:131]\n\t"
    "v_mfma_f32_16x16x32_bf16 v[132:135], a[128:131], a[8:11], v[132:135]\n\t"
    "v_mfma_f32_16x16x32_bf16 v[132:135], a[132:135], a[12:15], v[132:135]\n\t"
    "v_mfma_f32_16x16x32_bf16 v[136:139], a[128:131], a[16:19], v[136:139]\n\t"
    "ds_read_b128 a[180:183], v71 offset:0\n\t"
    "v_mfma_f32_16x16x32_bf16 v[136:139], a[132:135], a[20:23], v[136:139]\n\t"
    "v_mfma_f32_16x16x32_bf16 v[140:143], a[128:131], a[24:27], v[140:143]\n\t"
    "v_mfma_f32_16x16x32_bf16 v[140:143], a[132:135], a[28:31], v[140:143]\n\t"
    "s_waitcnt lgkmcnt(4)\n\t"
    "v_mfma_f32_16x16x32_bf16 v[156:159], a[136:139], a[24:27], v[156:159]\n\t"
    "ds_read_b128 a[184:187], v70 offset:2048\n\t"
    "v_mfma_f32_16x16x32_bf16 v[156:159], a[140:143], a[28:31], v[156:159]\n\t"
    "v_mfma_f32_16x16x32_bf16 v[152:155], a[136:139], a[16:19], v[152:155]\n\t"
    "v_mfma_f32_16x16x32_bf16 v[152:155], a[140:143], a[20:23], v[152:155]\n\t"
    "v_mfma_f32_16x16x32_bf16 v[148:151], a[136:139], a[8:11], v[148:151]\n\t"
    "ds_read_b128 a[188:191], v71 offset:2048\n\t"
    "v_mfma_f32_16x16x32_bf16 v[148:151], a[140:143], a[12:15], v[148:151]\n\t"
    "v_mfma_f32_16x16x32_bf16 v[144:147], a[136:139], a[0:3], v[144:147]\n\t"
    "v_mfma_f32_16x16x32_bf16 v[144:147], a[140:143], a[4:7], v[144:147]\n\t"
    "s_waitcnt lgkmcnt(4)\n\t"
    "v_mfma_f32_16x16x32_bf16 v[160:163], a[144:147], a[0:3], v[160:163]\n\t"
    "ds_read_b128 a[152:155], v66 offset:2048\n\t"
    "v_mfma_f32_16x16x32_bf16 v[160:163], a[148:151], a[4:7], v[160:163]\n\t"
    "v_mfma_f32_16x16x32_bf16 v[164:167], a[144:147], a[8:11], v[164:167]\n\t"
    "v_mfma_f32_16x16x32_bf16 v[164:167], a[148:151], a[12:15], v[164:167]\n\t"
    "v_mfma_f32_16x16x32_bf16 v[168:171], a[144:147], a[16:19], v[168:171]\n\t"
    "ds_read_b128 a[156:159], v67 offset:2048\n\t"
    "v_mfma_f32_16x16x32_bf16 v[168:171], a[148:151], a[20:23], v[168:171]\n\t"
    "v_mfma_f32_16x16x32_bf16 v[172:175], a[144:147], a[24:27], v[172:175]\n\t"
    "v_mfma_f32_16x16x32_bf16 v[172:175], a[148:151], a[28:31], v[172:175]\n\t"
    "s_waitcnt lgkmcnt(4)\n\t"
    "v_mfma_f32_16x16x32_bf16 v[236:239], a[176:179], a[24:27], v[236:239]\n\t"
    "ds_read_b128 a[160:163], v68 offset:0\n\t"
    "v_mfma_f32_16x16x32_bf16 v[236:239], a[180:183], a[28:31], v[236:239]\n\t"
    "v_mfma_f32_16x16x32_bf16 v[232:235], a[176:179], a[16:19], v[232:235]\n\t"
    "v_mfma_f32_16x16x32_bf16 v[232:235], a[180:183], a[20:23], v[232:235]\n\t"
    "v_mfma_f32_16x16x32_bf16 v[228:231], a[176:179], a[8:11], v[228:231]\n\t"
    "ds_read_b128 a[164:167], v69 offset:0\n\t"
    "v_mfma_f32_16x16x32_bf16 v[228:231], a[180:183], a[12:15], v[228:231]\n\t"
    "v_mfma_f32_16x16x32_bf16 v[224:227], a[176:179], a[0:3], v[224:227]\n\t"
    "v_mfma_f32_16x16x32_bf16 v[224:227], a[180:183], a[4:7], v[224:227]\n\t"
    "s_waitcnt lgkmcnt(4)\n\t"
    "v_mfma_f32_16x16x32_bf16 v[240:243], a[184:187], a[0:3], v[240:243]\n\t"
    "ds_read_b128 a[168:171], v68 offset:2048\n\t"
    "v_mfma_f32_16x16x32_bf16 v[240:243], a[188:191], a[4:7], v[240:243]\n\t"
    "v_mfma_f32_16x16x32_bf16 v[244:247], a[184:187], a[8:11], v[244:247]\n\t"
    "v_mfma_f32_16x16x32_bf16 v[244:247], a[188:191], a[12:15], v[244:247]\n\t"
    "v_mfma_f32_16x16x32_bf16 v[248:251], a[184:187], a[16:19], v[248:251]\n\t"
    "ds_read_b128 a[172:175], v69 offset:2048\n\t"
    "v_mfma_f32_16x16x32_bf16 v[248:251], a[188:191], a[20:23], v[248:251]\n\t"
    "v_mfma_f32_16x16x32_bf16 v[252:255], a[184:187], a[24:27], v[252:255]\n\t"
    "v_mfma_f32_16x16x32_bf16 v[252:255], a[188:191], a[28:31], v[252:255]\n\t"
    "s_waitcnt lgkmcnt(4)\n\t"
    "v_mfma_f32_16x16x32_bf16 v[188:191], a[152:155], a[24:27], v[188:191]\n\t"
    "v_mfma_f32_16x16x32_bf16 v[188:191], a[156:159], a[28:31], v[188:191]\n\t"
    "v_mfma_f32_16x16x32_bf16 v[184:187], a[152:155], a[16:19], v[184:187]\n\t"
    "v_mfma_f32_16x16x32_bf16 v[184:187], a[156:159], a[20:23], v[184:187]\n\t"
    "v_mfma_f32_16x16x32_bf16 v[180:183], a[152:155], a[8:11], v[180:183]\n\t"
    "v_mfma_f32_16x16x32_bf16 v[180:183], a[156:159], a[12:15], v[180:183]\n\t"
    "v_mfma_f32_16x16x32_bf16 v[176:179], a[152:155], a[0:3], v[176:179]\n\t"
    "v_mfma_f32_16x16x32_bf16 v[176:179], a[156:159], a[4:7], v[176:179]\n\t"
    "s_waitcnt vmcnt(0) lgkmcnt(0)\n\t"
    "s_barrier\n\t"
    "v_mfma_f32_16x16x32_bf16 v[192:195], a[160:163], a[0:3], v[192:195]\n\t"
    "ds_read_b128 a[176:179], v70 offset:33792\n\t"
    "v_mfma_f32_16x16x32_bf16 v[192:195], a[164:167], a[4:7], v[192:195]\n\t"
    "v_mfma_f32_16x16x32_bf16 v[196:199], a[160:163], a[8:11], v[196:199]\n\t"
    "v_mfma_f32_16x16x32_bf16 v[196:199], a[164:167], a[12:15], v[196:199]\n\t"
    "ds_read_b128 a[180:183], v71 offset:33792\n\t"
    "v_mfma_f32_16x16x32_bf16 v[200:203], a[160:163], a[16:19], v[200:203]\n\t"
    "v_mfma_f32_16x16x32_bf16 v[200:203], a[164:167], a[20:23], v[200:203]\n\t"
    "v_mfma_f32_16x16x32_bf16 v[204:207], a[160:163], a[24:27], v[204:207]\n\t"
    "ds_read_b128 a[184:187], v70 offset:35840\n\t"
    "v_mfma_f32_16x16x32_bf16 v[204:207], a[164:167], a[28:31], v[204:207]\n\t"
    "v_mfma_f32_16x16x32_bf16 v[220:223], a[168:171], a[24:27], v[220:223]\n\t"
    "ds_read_b128 a[188:191], v71 offset:35840\n\t"
    "v_mfma_f32_16x16x32_bf16 v[220:223], a[172:175], a[28:31], v[220:223]\n\t"
    "v_mfma_f32_16x16x32_bf16 v[216:219], a[168:171], a[16:19], v[216:219]\n\t"
    "v_mfma_f32_16x16x32_bf16 v[216:219], a[172:175], a[20:23], v[216:219]\n\t"
    "ds_read_b128 a[144:147], v66 offset:33792\n\t"
    "v_mfma_f32_16x16x32_bf16 v[212:215], a[168:171], a[8:11], v[212:215]\n\t"
    "v_mfma_f32_16x16x32_bf16 v[212:215], a[172:175], a[12:15], v[212:215]\n\t"
    "v_mfma_f32_16x16x32_bf16 v[208:211], a[168:171], a[0:3], v[208:211]\n\t"
    "ds_read_b128 a[148:151], v67 offset:33792\n\t"
    "v_mfma_f32_16x16x32_bf16 v[208:211], a[172:175], a[4:7], v[208:211]\n\t"
    "s_waitcnt lgkmcnt(4)\n\t"
    "v_mfma_f32_16x16x32_bf16 v[224:227], a[176:179], a[32:35], v[224:227]\n\t"
    "ds_read_b128 a[152:155], v66 offset:35840\n\t"
    "v_mfma_f32_16x16x32_bf16 v[224:227], a[180:183], a[36:39], v[224:227]\n\t"
    "v_mfma_f32_16x16x32_bf16 v[228:231], a[176:179], a[40:43], v[228:231]\n\t"
    "v_mfma_f32_16x16x32_bf16 v[228:231], a[180:183], a[44:47], v[228:231]\n\t"
    "v_mfma_f32_16x16x32_bf16 v[232:235], a[176:179], a[48:51], v[232:235]\n\t"
    "ds_read_b128 a[156:159], v67 offset:35840\n\t"
    "v_mfma_f32_16x16x32_bf16 v[232:235], a[180:183], a[52:55], v[232:235]\n\t"
    "v_mfma_f32_16x16x32_bf16 v[236:239], a[176:179], a[56:59], v[236:239]\n\t"
    "v_mfma_f32_16x16x32_bf16 v[236:239], a[180:183], a[60:63], v[236:239]\n\t"
    "s_waitcnt lgkmcnt(4)\n\t"
    "v_mfma_f32_16x16x32_bf16 v[252:255], a[184:187], a[56:59], v[252:255]\n\t"
    "ds_read_b128 a[160:163], v68 offset:33792\n\t"
    "v_mfma_f32_16x16x32_bf16 v[252:255], a[188:191], a[60:63], v[252:255]\n\t"
    "v_mfma_f32_16x16x32_bf16 v[248:251], a[184:187], a[48:51], v[248:251]\n\t"
    "v_mfma_f32_16x16x32_bf16 v[248:251], a[188:191], a[52:55], v[248:251]\n\t"
    "v_mfma_f32_16x16x32_bf16 v[244:247], a[184:187], a[40:43], v[244:247]\n\t"
    "ds_read_b128 a[164:167], v69 offset:33792\n\t"
    "v_mfma_f32_16x16x32_bf16 v[244:247], a[188:191], a[44:47], v[244:247]\n\t"
    "v_mfma_f32_16x16x32_bf16 v[240:243], a[184:187], a[32:35], v[240:243]\n\t"
    "v_mfma_f32_16x16x32_bf16 v[240:243], a[188:191], a[36:39], v[240:243]\n\t"
    "s_waitcnt lgkmcnt(4)\n\t"
    "v_mfma_f32_16x16x32_bf16 v[160:163], a[144:147], a[32:35], v[160:163]\n\t"
    "ds_read_b128 a[168:171], v68 offset:35840\n\t"
    "v_mfma_f32_16x16x32_bf16 v[160:163], a[148:151], a[36:39], v[160:163]\n\t"
    "v_mfma_f32_16x16x32_bf16 v[164:167], a[144:147], a[40:43], v[164:167]\n\t"
    "v_mfma_f32_16x16x32_bf16 v[164:167], a[148:151], a[44:47], v[164:167]\n\t"
    "ds_write_b128 v15, v[224:227] offset:0\n\t"
    "v_mfma_f32_16x16x32_bf16 v[168:171], a[144:147], a[48:51], v[168:171]\n\t"
    "ds_read_b128 a[172:175], v69 offset:35840\n\t"
    "ds_write_b128 v15, v[228:231] offset:1024\n\t"
    "v_mfma_f32_16x16x32_bf16 v[168:171], a[148:151], a[52:55], v[168:171]\n\t"
    "v_mfma_f32_16x16x32_bf16 v[172:175], a[144:147], a[56:59], v[172:175]\n\t"
    "ds_write_b128 v15, v[232:235] offset:2048\n\t"
    "v_mfma_f32_16x16x32_bf16 v[172:175], a[148:151], a[60:63], v[172:175]\n\t"
    "ds_write_b128 v15, v[236:239] offset:3072\n\t"
    "s_waitcnt lgkmcnt(8)\n\t"
    "v_mfma_f32_16x16x32_bf16 v[188:191], a[152:155], a[56:59], v[188:191]\n\t"
    "ds_read_b128 a[128:131], v64 offset:33792\n\t"
    "v_mfma_f32_16x16x32_bf16 v[188:191], a[156:159], a[60:63], v[188:191]\n\t"
    "ds_write_b128 v15, v[240:243] offset:4096\n\t"
    "v_mfma_f32_16x16x32_bf16 v[184:187], a[152:155], a[48:51], v[184:187]\n\t"
    "ds_write_b128 v15, v[244:247] offset:5120\n\t"
    "v_mfma_f32_16x16x32_bf16 v[184:187], a[156:159], a[52:55], v[184:187]\n\t"
    "v_mfma_f32_16x16x32_bf16 v[180:183], a[152:155], a[40:43], v[180:183]\n\t"
    "ds_read_b128 a[132:135], v65 offset:33792\n\t"
    "ds_write_b128 v15, v[248:251] offset:6144\n\t"
    "v_mfma_f32_16x16x32_bf16 v[180:183], a[156:159], a[44:47], v[180:183]\n\t"
    "ds_write_b128 v15, v[252:255] offset:7168\n\t"
    "v_mfma_f32_16x16x32_bf16 v[176:179], a[152:155], a[32:35], v[176:179]\n\t"
    "v_mfma_f32_16x16x32_bf16 v[176:179], a[156:159], a[36:39], v[176:179]\n\t"
    "s_waitcnt lgkmcnt(0)\n\t"
    "s_barrier\n\t"
    "v_mfma_f32_16x16x32_bf16 v[192:195], a[160:163], a[32:35], v[192:195]\n\t"
    "ds_read_b128 v[224:227], v14 offset:0\n\t"
    "ds_read_b128 a[136:139], v64 offset:35840\n\t"
    "v_mfma_f32_16x16x32_bf16 v[192:195], a[164:167], a[36:39], v[192:195]\n\t"
    "ds_read_b128 v[228:231], v14 offset:1024\n\t"
    "v_mfma_f32_16x16x32_bf16 v[196:199], a[160:163], a[40:43], v[196:199]\n\t"
    "ds_read_b128 v[232:235], v14 offset:2048\n\t"
    "v_mfma_f32_16x16x32_bf16 v[196:199], a[164:167], a[44:47], v[196:199]\n\t"
    "ds_read_b128 v[236:239], v14 offset:3072\n\t"
    "v_mfma_f32_16x16x32_bf16 v[200:203], a[160:163], a[48:51], v[200:203]\n\t"
    "ds_read_b128 v[240:243], v14 offset:4096\n\t"
    "ds_read_b128 a[140:143], v65 offset:35840\n\t"
    "v_mfma_f32_16x16x32_bf16 v[200:203], a[164:167], a[52:55], v[200:203]\n\t"
    "ds_read_b128 v[244:247], v14 offset:5120\n\t"
    "v_mfma_f32_16x16x32_bf16 v[204:207], a[160:163], a[56:59], v[204:207]\n\t"
    "ds_read_b128 v[248:251], v14 offset:6144\n\t"
    "v_mfma_f32_16x16x32_bf16 v[204:207], a[164:167], a[60:63], v[204:207]\n\t"
    "ds_read_b128 v[252:255], v14 offset:7168\n\t"
    "v_mfma_f32_16x16x32_bf16 v[220:223], a[168:171], a[56:59], v[220:223]\n\t"
    "v_mfma_f32_16x16x32_bf16 v[220:223], a[172:175], a[60:63], v[220:223]\n\t"
    "s_waitcnt lgkmcnt(9)\n\t"
    "v_add_f32_e32 v160, v160, v224\n\t"
    "v_add_f32_e32 v161, v161, v225\n\t"
    "v_add_f32_e32 v162, v162, v226\n\t"
    "v_add_f32_e32 v163, v163, v227\n\t"
    "v_mfma_f32_16x16x32_bf16 v[216:219], a[168:171], a[48:51], v[216:219]\n\t"
    "v_and_or_b32 v160, v160, s33, v108\n\t"
    "v_and_or_b32 v161, v161, s33, v109\n\t"
    "v_and_or_b32 v162, v162, s33, v110\n\t"
    "v_and_or_b32 v163, v163, s33, v111\n\t"
    "v_mfma_f32_16x16x32_bf16 v[216:219], a[172:175], a[52:55], v[216:219]\n\t"
    "buffer_store_dwordx4 v[160:163], v107, s[16:19], s60 offen offset:0 sc1\n\t"
    "s_waitcnt lgkmcnt(7)\n\t"
    "v_add_f32_e32 v164, v164, v228\n\t"
    "v_mfma_f32_16x16x32_bf16 v[212:215], a[168:171], a[40:43], v[212:215]\n\t"
    "v_add_f32_e32 v165, v165, v229\n\t"
    "v_add_f32_e32 v166, v166, v230\n\t"
    "v_add_f32_e32 v167, v167, v231\n\t"
    "v_and_or_b32 v164, v164, s33, v108\n\t"
    "v_mfma_f32_16x16x32_bf16 v[212:215], a[172:175], a[44:47], v[212:215]\n\t"
    "v_and_or_b32 v165, v165, s33, v109\n\t"
    "v_and_or_b32 v166, v166, s33, v110\n\t"
    "v_and_or_b32 v167, v167, s33, v111\n\t"
    "v_mfma_f32_16x16x32_bf16 v[208:211], a[168:171], a[32:35], v[208:211]\n\t"
    "buffer_store_dwordx4 v[164:167], v107, s[16:19], s60 offen offset:1024 sc1\n\t"
    "s_waitcnt lgkmcnt(6)\n\t"
    "v_add_f32_e32 v168, v168, v232\n\t"
    "v_mfma_f32_16x16x32_bf16 v[208:211], a[172:175], a[36:39], v[208:211]\n\t"
    "v_add_f32_e32 v169, v169, v233\n\t"
    "v_add_f32_e32 v170, v170, v234\n\t"
    "v_add_f32_e32 v171, v171, v235\n\t"
    "v_and_or_b32 v168, v168, s33, v108\n\t"
    "v_mfma_f32_16x16x32_bf16 v[128:131], a[128:131], a[32:35], v[128:131]\n\t"
    "v_and_or_b32 v169, v169, s33, v109\n\t"
    "v_and_or_b32 v170, v170, s33, v110\n\t"
    "v_and_or_b32 v171, v171, s33, v111\n\t"
    "v_mfma_f32_16x16x32_bf16 v[128:131], a[132:135], a[36:39], v[128:131]\n\t"
    "buffer_store_dwordx4 v[168:171], v107, s[16:19], s60 offen offset:2048 sc1\n\t"
    "s_waitcnt lgkmcnt(5)\n\t"
    "v_add_f32_e32 v172, v172, v236\n\t"
    "v_mfma_f32_16x16x32_bf16 v[132:135], a[128:131], a[40:43], v[132:135]\n\t"
    "v_add_f32_e32 v173, v173, v237\n\t"
    "v_add_f32_e32 v174, v174, v238\n\t"
    "v_add_f32_e32 v175, v175, v239\n\t"
    "v_and_or_b32 v172, v172, s33, v108\n\t"
    "v_mfma_f32_16x16x32_bf16 v[132:135], a[132:135], a[44:47], v[132:135]\n\t"
    "v_and_or_b32 v173, v173, s33, v109\n\t"
    "v_and_or_b32 v174, v174, s33, v110\n\t"
    "v_and_or_b32 v175, v175, s33, v111\n\t"
    "v_mfma_f32_16x16x32_bf16 v[136:139], a[128:131], a[48:51], v[136:139]\n\t"
    "buffer_store_dwordx4 v[172:175], v107, s[16:19], s60 offen offset:3072 sc1\n\t"
    "s_waitcnt lgkmcnt(4)\n\t"
    "v_add_f32_e32 v176, v176, v240\n\t"
    "v_mfma_f32_16x16x32_bf16 v[136:139], a[132:135], a[52:55], v[136:139]\n\t"
    "v_add_f32_e32 v177, v177, v241\n\t"
    "v_add_f32_e32 v178, v178, v242\n\t"
    "v_add_f32_e32 v179, v179, v243\n\t"
    "v_and_or_b32 v176, v176, s33, v108\n\t"
    "v_mfma_f32_16x16x32_bf16 v[140:143], a[128:131], a[56:59], v[140:143]\n\t"
    "v_and_or_b32 v177, v177, s33, v109\n\t"
    "v_and_or_b32 v178, v178, s33, v110\n\t"
    "v_and_or_b32 v179, v179, s33, v111\n\t"
    "v_mfma_f32_16x16x32_bf16 v[140:143], a[132:135], a[60:63], v[140:143]\n\t"
    "buffer_store_dwordx4 v[176:179], v107, s[16:19], s61 offen offset:0 sc1\n\t"
    "s_waitcnt lgkmcnt(2)\n\t"
    "v_add_f32_e32 v180, v180, v244\n\t"
    "v_mfma_f32_16x16x32_bf16 v[156:159], a[136:139], a[56:59], v[156:159]\n\t"
    "v_add_f32_e32 v181, v181, v245\n\t"
    "v_add_f32_e32 v182, v182, v246\n\t"
    "v_add_f32_e32 v183, v183, v247\n\t"
    "v_and_or_b32 v180, v180, s33, v108\n\t"
    "v_mfma_f32_16x16x32_bf16 v[156:159], a[140:143], a[60:63], v[156:159]\n\t"
    "v_and_or_b32 v181, v181, s33, v109\n\t"
    "v_and_or_b32 v182, v182, s33, v110\n\t"
    "v_and_or_b32 v183, v183, s33, v111\n\t"
    "v_mfma_f32_16x16x32_bf16 v[152:155], a[136:139], a[48:51], v[152:155]\n\t"
    "buffer_store_dwordx4 v[180:183], v107, s[16:19], s61 offen offset:1024 sc1\n\t"
    "s_waitcnt lgkmcnt(1)\n\t"
    "v_add_f32_e32 v184, v184, v248\n\t"
    "v_mfma_f32_16x16x32_bf16 v[152:155], a[140:143], a[52:55], v[152:155]\n\t"
    "v_add_f32_e32 v185, v185, v249\n\t"
    "v_add_f32_e32 v186, v186, v250\n\t"
    "v_add_f32_e32 v187, v187, v251\n\t"
    "v_and_or_b32 v184, v184, s33, v108\n\t"
    "v_mfma_f32_16x16x32_bf16 v[148:151], a[136:139], a[40:43], v[148:151]\n\t"
    "v_and_or_b32 v185, v185, s33, v109\n\t"
    "v_and_or_b32 v186, v186, s33, v110\n\t"
    "v_and_or_b32 v187, v187, s33, v111\n\t"
    "v_mfma_f32_16x16x32_bf16 v[148:151], a[140:143], a[44:47], v[148:151]\n\t"
    "buffer_store_dwordx4 v[184:187], v107, s[16:19], s61 offen offset:2048 sc1\n\t"
    "s_waitcnt lgkmcnt(0)\n\t"
    "v_add_f32_e32 v188, v188, v252\n\t"
    "v_mfma_f32_16x16x32_bf16 v[144:147], a[136:139], a[32:35], v[144:147]\n\t"
    "v_add_f32_e32 v189, v189, v253\n\t"
    "v_add_f32_e32 v190, v190, v254\n\t"
    "v_add_f32_e32 v191, v191, v255\n\t"
    "v_and_or_b32 v188, v188, s33, v108\n\t"
    "v_mfma_f32_16x16x32_bf16 v[144:147], a[140:143], a[36:39], v[144:147]\n\t"
    "v_and_or_b32 v189, v189, s33, v109\n\t"
    "v_and_or_b32 v190, v190, s33, v110\n\t"
    "v_and_or_b32 v191, v191, s33, v111\n\t"
    "buffer_store_dwordx4 v[188:191], v107, s[16:19], s61 offen offset:3072 sc1\n\t"
    "buffer_store_dwordx2 v[104:105], v106, s[16:19], s94 offen sc1\n\t"
    "ds_write_b128 v15, v[192:195] offset:8192\n\t"
    "ds_write_b128 v15, v[196:199] offset:9216\n\t"
    "ds_write_b128 v15, v[200:203] offset:10240\n\t"
    "ds_write_b128 v15, v[204:207] offset:11264\n\t"
    "ds_write_b128 v15, v[208:211] offset:12288\n\t"
    "ds_write_b128 v15, v[212:215] offset:13312\n\t"
    "ds_write_b128 v15, v[216:219] offset:14336\n\t"
    "ds_write_b128 v15, v[220:223] offset:15360\n\t"
    "buffer_load_dwordx2 v[96:97], v12, s[16:19], s95 offen sc1\n\t"
    "s_mov_b32 s90, 0\n\t"
    "s_waitcnt lgkmcnt(0)\n\t"
    "s_barrier\n\t"
    "ds_read_b128 v[192:195], v14 offset:8192\n\t"
    "ds_read_b128 v[196:199], v14 offset:9216\n\t"
    "ds_read_b128 v[200:203], v14 offset:10240\n\t"
    "ds_read_b128 v[204:207], v14 offset:11264\n\t"
    "ds_read_b128 v[208:211], v14 offset:12288\n\t"
    "ds_read_b128 v[212:215], v14 offset:13312\n\t"
    "ds_read_b128 v[216:219], v14 offset:14336\n\t"
    "ds_read_b128 v[220:223], v14 offset:15360\n\t"
    "s_waitcnt lgkmcnt(7)\n\t"
    "v_pk_add_f32 v[128:129], v[128:129], v[192:193]\n\t"
    "v_pk_add_f32 v[130:131], v[130:131], v[194:195]\n\t"
    "s_waitcnt lgkmcnt(6)\n\t"
    "v_pk_add_f32 v[132:133], v[132:133], v[196:197]\n\t"
    "v_pk_add_f32 v[134:135], v[134:135], v[198:199]\n\t"
    "s_waitcnt lgkmcnt(5)\n\t"
    "v_pk_add_f32 v[136:137], v[136:137], v[200:201]\n\t"
    "v_pk_add_f32 v[138:139], v[138:139], v[202:203]\n\t"
    "s_waitcnt lgkmcnt(4)\n\t"
    "v_pk_add_f32 v[140:141], v[140:141], v[204:205]\n\t"
    "v_pk_add_f32 v[142:143], v[142:143], v[206:207]\n\t"
    "s_waitcnt lgkmcnt(3)\n\t"
    "v_pk_add_f32 v[144:145], v[144:145], v[208:209]\n\t"
    "v_pk_add_f32 v[146:147], v[146:147], v[210:211]\n\t"
    "s_waitcnt lgkmcnt(2)\n\t"
    "v_pk_add_f32 v[148:149], v[148:149], v[212:213]\n\t"
    "v_pk_add_f32 v[150:151], v[150:151], v[214:215]\n\t"
    "s_waitcnt lgkmcnt(1)\n\t"
    "v_pk_add_f32 v[152:153], v[152:153], v[216:217]\n\t"
    "v_pk_add_f32 v[154:155], v[154:155], v[218:219]\n\t"
    "s_waitcnt lgkmcnt(0)\n\t"
    "v_pk_add_f32 v[156:157], v[156:157], v[220:221]\n\t"
    "v_pk_add_f32 v[158:159], v[158:159], v[222:223]\n\t"
    "L_spH_%=:\n\t"
    "s_waitcnt vmcnt(0)\n\t"
    "v_cmp_eq_u64_e32 vcc, s[82:83], v[96:97]\n\t"
    "s_cmp_eq_u64 vcc, -1\n\t"
    "s_cbranch_scc1 L_spPld_%=\n\t"
    "buffer_load_dwordx2 v[96:97], v12, s[16:19], s95 offen sc1\n\t"
    "s_add_u32 s90, s90, 1\n\t"
    "s_cmp_lt_u32 s90, 0x20000\n\t"
    "s_cbranch_scc1 L_spH_%=\n\t"
    "L_spPld_%=:\n\t"
    "buffer_load_dwordx4 v[32:35], v13, s[16:19], s60 offen offset:0 sc1 nt\n\t"
    "buffer_load_dwordx4 v[36:39], v13, s[16:19], s60 offen offset:1024 sc1 nt\n\t"
    "buffer_load_dwordx4 v[40:43], v13, s[16:19], s60 offen offset:2048 sc1 nt\n\t"
    "buffer_load_dwordx4 v[44:47], v13, s[16:19], s60 offen offset:3072 sc1 nt\n\t"
    "buffer_load_dwordx4 v[48:51], v13, s[16:19], s61 offen offset:0 sc1 nt\n\t"
    "buffer_load_dwordx4 v[52:55], v13, s[16:19], s61 offen offset:1024 sc1 nt\n\t"
    "buffer_load_dwordx4 v[56:59], v13, s[16:19], s61 offen offset:2048 sc1 nt\n\t"
    "buffer_load_dwordx4 v[60:63], v13, s[16:19], s61 offen offset:3072 sc1 nt\n\t"
    "s_add_u32 s90, s90, 1\n\t"
    "s_waitcnt vmcnt(0)\n\t"
    "v_xor_b32_e32 v96, v32, v108\n\t"
    "v_bitop3_b32 v96, v33, v109, v96 bitop3:0xbe\n\t"
    "v_bitop3_b32 v96, v34, v110, v96 bitop3:0xbe\n\t"
    "v_bitop3_b32 v96, v35, v111, v96 bitop3:0xbe\n\t"
    "v_bitop3_b32 v96, v36, v108, v96 bitop3:0xbe\n\t"
    "v_bitop3_b32 v96, v37, v109, v96 bitop3:0xbe\n\t"
    "v_bitop3_b32 v96, v38, v110, v96 bitop3:0xbe\n\t"
    "v_bitop3_b32 v96, v39, v111, v96 bitop3:0xbe\n\t"
    "v_bitop3_b32 v96, v40, v108, v96 bitop3:0xbe\n\t"
    "v_bitop3_b32 v96, v41, v109, v96 bitop3:0xbe\n\t"
    "v_bitop3_b32 v96, v42, v110, v96 bitop3:0xbe\n\t"
    "v_bitop3_b32 v96, v43, v111, v96 bitop3:0xbe\n\t"
    "v_bitop3_b32 v96, v44, v108, v96 bitop3:0xbe\n\t"
    "v_bitop3_b32 v96, v45, v109, v96 bitop3:0xbe\n\t"
    "v_bitop3_b32 v96, v46, v110, v96 bitop3:0xbe\n\t"
    "v_bitop3_b32 v96, v47, v111, v96 bitop3:0xbe\n\t"
    "v_bitop3_b32 v96, v48, v108, v96 bitop3:0xbe\n\t"
    "v_bitop3_b32 v96, v49, v109, v96 bitop3:0xbe\n\t"
    "v_bitop3_b32 v96, v50, v110, v96 bitop3:0xbe\n\t"
    "v_bitop3_b32 v96, v51, v111, v96 bitop3:0xbe\n\t"
    "v_bitop3_b32 v96, v52, v108, v96 bitop3:0xbe\n\t"
    "v_bitop3_b32 v96, v53, v109, v96 bitop3:0xbe\n\t"
    "v_bitop3_b32 v96, v54, v110, v96 bitop3:0xbe\n\t"
    "v_bitop3_b32 v96, v55, v111, v96 bitop3:0xbe\n\t"
    "v_bitop3_b32 v96, v56, v108, v96 bitop3:0xbe\n\t"
    "v_bitop3_b32 v96, v57, v109, v96 bitop3:0xbe\n\t"
    "v_bitop3_b32 v96, v58, v110, v96 bitop3:0xbe\n\t"
    "v_bitop3_b32 v96, v59, v111, v96 bitop3:0xbe\n\t"
    "v_bitop3_b32 v96, v60, v108, v96 bitop3:0xbe\n\t"
    "v_bitop3_b32 v96, v61, v109, v96 bitop3:0xbe\n\t"
    "v_bitop3_b32 v96, v62, v110, v96 bitop3:0xbe\n\t"
    "v_bitop3_b32 v96, v63, v111, v96 bitop3:0xbe\n\t"
    "v_and_b32_e32 v96, 0xff, v96\n\t"
    "v_cmp_ne_u32_e32 vcc, 0, v96\n\t"
    "s_cbranch_vccz L_spPok_%=\n\t"
    "s_sleep 4\n\t"
    "s_cmp_lt_u32 s90, 0x20000\n\t"
    "s_cbranch_scc1 L_spPld_%=\n\t"
    "L_spPok_%=:\n\t"
    "v_and_b32_e32 v32, s33, v32\n\t"
    "v_and_b32_e32 v33, s33, v33\n\t"
    "v_and_b32_e32 v34, s33, v34\n\t"
    "v_and_b32_e32 v35, s33, v35\n\t"
    "v_pk_add_f32 v[128:129], v[128:129], v[32:33]\n\t"
    "v_pk_add_f32 v[130:131], v[130:131], v[34:35]\n\t"
    "buffer_store_dwordx4 v[128:131], v29, s[56:59], s64 offen offset:0 nt\n\t"
    "v_and_b32_e32 v36, s33, v36\n\t"
    "v_and_b32_e32 v37, s33, v37\n\t"
    "v_and_b32_e32 v38, s33, v38\n\t"
    "v_and_b32_e32 v39, s33, v39\n\t"
    "v_pk_add_f32 v[132:133], v[132:133], v[36:37]\n\t"
    "v_pk_add_f32 v[134:135], v[134:135], v[38:39]\n\t"
    "buffer_store_dwordx4 v[132:135], v29, s[56:59], s65 offen offset:0 nt\n\t"
    "v_and_b32_e32 v40, s33, v40\n\t"
    "v_and_b32_e32 v41, s33, v41\n\t"
    "v_and_b32_e32 v42, s33, v42\n\t"
    "v_and_b32_e32 v43, s33, v43\n\t"
    "v_pk_add_f32 v[136:137], v[136:137], v[40:41]\n\t"
    "v_pk_add_f32 v[138:139], v[138:139], v[42:43]\n\t"
    "buffer_store_dwordx4 v[136:139], v29, s[56:59], s66 offen offset:0 nt\n\t"
    "v_and_b32_e32 v44, s33, v44\n\t"
    "v_and_b32_e32 v45, s33, v45\n\t"
    "v_and_b32_e32 v46, s33, v46\n\t"
    "v_and_b32_e32 v47, s33, v47\n\t"
    "v_pk_add_f32 v[140:141], v[140:141], v[44:45]\n\t"
    "v_pk_add_f32 v[142:143], v[142:143], v[46:47]\n\t"
    "buffer_store_dwordx4 v[140:143], v29, s[56:59], s67 offen offset:0 nt\n\t"
    "v_and_b32_e32 v48, s33, v48\n\t"
    "v_and_b32_e32 v49, s33, v49\n\t"
    "v_and_b32_e32 v50, s33, v50\n\t"
    "v_and_b32_e32 v51, s33, v51\n\t"
    "v_pk_add_f32 v[144:145], v[144:145], v[48:49]\n\t"
    "v_pk_add_f32 v[146:147], v[146:147], v[50:51]\n\t"
    "buffer_store_dwordx4 v[144:147], v29, s[56:59], s64 offen offset:64 nt\n\t"
    "v_and_b32_e32 v52, s33, v52\n\t"
    "v_and_b32_e32 v53, s33, v53\n\t"
    "v_and_b32_e32 v54, s33, v54\n\t"
    "v_and_b32_e32 v55, s33, v55\n\t"
    "v_pk_add_f32 v[148:149], v[148:149], v[52:53]\n\t"
    "v_pk_add_f32 v[150:151], v[150:151], v[54:55]\n\t"
    "buffer_store_dwordx4 v[148:151], v29, s[56:59], s65 offen offset:64 nt\n\t"
    "v_and_b32_e32 v56, s33, v56\n\t"
    "v_and_b32_e32 v57, s33, v57\n\t"
    "v_and_b32_e32 v58, s33, v58\n\t"
    "v_and_b32_e32 v59, s33, v59\n\t"
    "v_pk_add_f32 v[152:153], v[152:153], v[56:57]\n\t"
    "v_pk_add_f32 v[154:155], v[154:155], v[58:59]\n\t"
    "buffer_store_dwordx4 v[152:155], v29, s[56:59], s66 offen offset:64 nt\n\t"
    "v_and_b32_e32 v60, s33, v60\n\t"
    "v_and_b32_e32 v61, s33, v61\n\t"
    "v_and_b32_e32 v62, s33, v62\n\t"
    "v_and_b32_e32 v63, s33, v63\n\t"
    "v_pk_add_f32 v[156:157], v[156:157], v[60:61]\n\t"
    "v_pk_add_f32 v[158:159], v[158:159], v[62:63]\n\t"
    "buffer_store_dwordx4 v[156:159], v29, s[56:59], s67 offen offset:64 nt\n\t"
    "L_end_%=:\n\t"
    ".Lk_code_end:\n\t"
    : "+{v[0:15]}"(va), "+{v[16:31]}"(vb), "+{v255}"(vpl), "+{a[240:255]}"(par)
    : [ka]"{s[0:1]}"(__builtin_amdgcn_kernarg_segment_ptr()), [t1p]"{s[4:5]}"(t1p), [t2p]"{s[6:7]}"(t2p), [t2n]"{s8}"(t2n), [t1i]"{s9}"(t1i),
      [w]"{s10}"(w), [bid]"{s11}"(bid), [tag]"{s12}"(tag), [t2i]"{s13}"(t2i)
    : "s16","s17","s18","s19","s20","s21","s22","s23","s24","s25","s26","s27","s28","s29","s30","s31","s33","s34","s35","s36","s37","s38","s39","s40","s41","s42","s43","s44","s45","s46","s47","s48","s49","s50","s51","s52","s53","s54","s55","s56","s57","s58","s59","s60","s61","s62","s63","s64","s65","s66","s67","s68","s69","s70","s71","s72","s73","s74","s75","s76","s77","s78","s79","s80","s81","s82","s83","s84","s85","s86","s87","s88","s89","s90","s91","s92","s93","s94","s95","s96","s97","s98","s99","v32","v33","v34","v35","v36","v37","v38","v39","v40","v41","v42","v43","v44","v45","v46","v47","v48","v49","v50","v51","v52","v53","v54","v55","v56","v57","v58","v59","v60","v61","v62","v63","v64","v65","v66","v67","v68","v69","v70","v71","v72","v73","v74","v75","v76","v77","v78","v79","v80","v81","v82","v83","v84","v85","v86","v87","v88","v89","v90","v91","v92","v93","v94","v95","v96","v97","v98","v99","v100","v101","v102","v103","v104","v105","v106","v107","v108","v109","v110","v111","v112","v113","v114","v115","v116","v117","v118","v119","v120","v121","v122","v123","v124","v125","v126","v127","v128","v129","v130","v131","v132","v133","v134","v135","v136","v137","v138","v139","v140","v141","v142","v143","v144","v145","v146","v147","v148","v149","v150","v151","v152","v153","v154","v155","v156","v157","v158","v159","v160","v161","v162","v163","v164","v165","v166","v167","v168","v169","v170","v171","v172","v173","v174","v175","v176","v177","v178","v179","v180","v181","v182","v183","v184","v185","v186","v187","v188","v189","v190","v191","v192","v193","v194","v195","v196","v197","v198","v199","v200","v201","v202","v203","v204","v205","v206","v207","v208","v209","v210","v211","v212","v213","v214","v215","v216","v217","v218","v219","v220","v221","v222","v223","v224","v225","v226","v227","v228","v229","v230","v231","v232","v233","v234","v235","v236","v237","v238","v239","v240","v241","v242","v243","v244","v245","v246","v247","v248","v249","v250","v251","v252","v253","v254","a0","a1","a2","a3","a4","a5","a6","a7","a8","a9","a10","a11","a12","a13","a14","a15","a16","a17","a18","a19","a20","a21","a22","a23","a24","a25","a26","a27","a28","a29","a30","a31","a32","a33","a34","a35","a36","a37","a38","a39","a40","a41","a42","a43","a44","a45","a46","a47","a48","a49","a50","a51","a52","a53","a54","a55","a56","a57","a58","a59","a60","a61","a62","a63","a64","a65","a66","a67","a68","a69","a70","a71","a72","a73","a74","a75","a76","a77","a78","a79","a80","a81","a82","a83","a84","a85","a86","a87","a88","a89","a90","a91","a92","a93","a94","a95","a96","a97","a98","a99","a100","a101","a102","a103","a104","a105","a106","a107","a108","a109","a110","a111","a112","a113","a114","a115","a116","a117","a118","a119","a120","a121","a122","a123","a124","a125","a126","a127","a128","a129","a130","a131","a132","a133","a134","a135","a136","a137","a138","a139","a140","a141","a142","a143","a144","a145","a146","a147","a148","a149","a150","a151","a152","a153","a154","a155","a156","a157","a158","a159","a160","a161","a162","a163","a164","a165","a166","a167","a168","a169","a170","a171","a172","a173","a174","a175","a176","a177","a178","a179","a180","a181","a182","a183","a184","a185","a186","a187","a188","a189","a190","a191","a192","a193","a194","a195","a196","a197","a198","a199","a200","a201","a202","a203","a204","a205","a206","a207","a208","a209","a210","a211","a212","a213","a214","a215","a216","a217","a218","a219","a220","a221","a222","a223","a224","a225","a226","a227","a228","a229","a230","a231","a232","a233","a234","a235","a236","a237","a238","a239", "vcc", "scc", "memory");
}

extern "C" void kernel_launch(void* const* d_in, const int* in_sizes, int n_in, void* d_out, int out_size, void* d_ws, size_t ws_size, hipStream_t stream) {
  Params p;
  p.x = (const u16*)d_in[0]; p.lre = (const u16*)d_in[1]; p.lim = (const u16*)d_in[2]; p.bre = (const u16*)d_in[3]; p.bim = (const u16*)d_in[4];
  p.cre = (const u16*)d_in[5]; p.cim = (const u16*)d_in[6]; p.logdt = (const u16*)d_in[7]; p.dskip = (const u16*)d_in[8];
  p.wg = (const u16*)d_in[10]; p.wu = (const u16*)d_in[11]; p.wd = (const u16*)d_in[12]; p.out = (float*)d_out; p.ws = (char*)d_ws;
  (void)hipFuncSetAttribute((const void*)k_s5_glu, hipFuncAttributeMaxDynamicSharedMemorySize, 163840);
  k_s5_glu<<<256, 256, 163840, stream>>>(p);
}
```
